```python
import torch

def _softmax(x, axis=-1):
    return torch.softmax(x, dim=axis)
def _gelu(x):
    return torch.nn.functional.gelu(x, approximate="tanh")
def _silu(x):
    return torch.nn.functional.silu(x)
def _sigmoid(x):
    return torch.sigmoid(x)
def _layernorm(x, axis=-1, eps=1e-5):
    m = x.mean(dim=axis, keepdim=True)
    v = x.var(dim=axis, keepdim=True, unbiased=False)
    return (x - m) / torch.sqrt(v + eps)
def _scatter_add(tgt, idx, src, axis=0):
    return tgt.clone().index_add_(axis, idx, src)
def _segment_sum(src, seg, n):
    out = torch.zeros((n,) + src.shape[1:], dtype=src.dtype)
    return out.index_add_(0, seg, src)
def _pad(x, pw):
    flat = tuple(v for pair in reversed(pw) for v in pair)
    return torch.nn.functional.pad(x, flat)

kernel_name = 'parallel_sum__compressed_kv_cla__geglu_ffn__decode'

def setup_inputs():

    _w = (lambda a: (lambda t: t.to(torch.bfloat16) if t.is_floating_point() else t)(torch.as_tensor(a)))
    def _A_setup():
        B, H, Sq, Sk, D, M = 8, 32, 1, 4096, 128, 512
        Dc = 4 * D
        return {
            "x": _w((lambda *s: torch.randn(*s, dtype=torch.bfloat16) * 0.05)(B, Sq, M)),
            "c_kv": _w((lambda *s: torch.randn(*s, dtype=torch.bfloat16) * 1.6)(B, Sk, Dc)),
            "w_q": _w((lambda *s: torch.randn(*s, dtype=torch.bfloat16) * 3.2)(M, H, D) * (M ** -0.5)),
            "w_uk": _w((lambda *s: torch.randn(*s, dtype=torch.bfloat16) * 3.2)(Dc, H, D) * (Dc ** -0.5)),
            "w_uv": _w((lambda *s: torch.randn(*s, dtype=torch.bfloat16) * 1.6)(Dc, H, D) * (Dc ** -0.5)),
            "w_o": _w((lambda *s: torch.randn(*s, dtype=torch.bfloat16) * 1.6)(H, D, M) * ((H * D) ** -0.5)),
        }
    _A_in = _A_setup()

    def _B_setup():
        B, S, D, F = 8, 1, 512, 1376
        return {
            "x": _w((lambda *s: torch.randn(*s, dtype=torch.bfloat16) * 0.05)(B, S, D)),
            "w_gate": _w((lambda *s: torch.randn(*s, dtype=torch.bfloat16) * 0.1)(D, F)),
            "w_up": _w((lambda *s: torch.randn(*s, dtype=torch.bfloat16) * 0.1)(D, F)),
            "w_down": _w((lambda *s: torch.randn(*s, dtype=torch.bfloat16) * 0.1)(F, D)),
        }
    _B_in = _B_setup()

    return {
        "A_x": _A_in["x"],
        "A_c_kv": _A_in["c_kv"],
        "A_w_q": _A_in["w_q"],
        "A_w_uk": _A_in["w_uk"],
        "A_w_uv": _A_in["w_uv"],
        "A_w_o": _A_in["w_o"],
        "B_x": _B_in["x"],
        "B_w_gate": _B_in["w_gate"],
        "B_w_up": _B_in["w_up"],
        "B_w_down": _B_in["w_down"],
    }

def reference(A_x, A_c_kv, A_w_q, A_w_uk, A_w_uv, A_w_o, B_x, B_w_gate, B_w_up, B_w_down):
    def _A_ref(x, c_kv, w_q, w_uk, w_uv, w_o):
        B, Sq, M = x.shape[0], x.shape[1], x.shape[2]
        Sk = c_kv.shape[1]
        H, D = w_q.shape[1], w_q.shape[2]
        q = torch.einsum("bqm,mhd->bhqd", x, w_q)
        k = torch.einsum("bkc,chd->bhkd", c_kv, w_uk)
        v = torch.einsum("bkc,chd->bhkd", c_kv, w_uv)
        scores = torch.einsum("bhqd,bhkd->bhqk", q, k) * (D ** -0.5)
        off = Sk - Sq
        qi = torch.arange(Sq)[:, None]
        kj = torch.arange(Sk)[None, :]
        cmask = kj <= (qi + off)
        scores = torch.where(cmask[None, None], scores, -1e9)
        p = _softmax(scores, axis=-1)
        ctx = torch.einsum("bhqk,bhkd->bhqd", p, v)
        return torch.einsum("bhqd,hdm->bqm", ctx, w_o)

    def _B_ref(x, w_gate, w_up, w_down):
        return (_gelu(x @ w_gate) * (x @ w_up)) @ w_down

    x = A_x
    xn = _layernorm(x, axis=-1)
    return x + _A_ref(xn, A_c_kv, A_w_q, A_w_uk, A_w_uv, A_w_o) + _B_ref(xn, B_w_gate, B_w_up, B_w_down)
```

```cpp
#include <hip/hip_runtime.h>
#include <hip/hip_bf16.h>
#include <stdint.h>

#define QABS_BF16 1

__device__ __forceinline__ float bf2f(unsigned short b) { return __builtin_bit_cast(float, (unsigned)b << 16); }
__device__ __forceinline__ unsigned short f2bf(float f) {
  unsigned u = __builtin_bit_cast(unsigned, f);
  u += 0x7FFFu + ((u >> 16) & 1u);
  return (unsigned short)(u >> 16);
}
constexpr int B = 8, H = 32, SK = 4096, D = 128, M = 512, DC = 512, F = 1376;

__global__ void k_ln(const unsigned short* x, float* xn) {
  int b = blockIdx.x; int t = threadIdx.x;
  __shared__ float red[16];
  float v = bf2f(x[b * M + t]);
  float s = v;
  for (int o = 32; o; o >>= 1) s += __shfl_xor(s, o);
  if ((t & 63) == 0) red[t >> 6] = s;
  __syncthreads();
  float tot = 0; for (int i = 0; i < 8; ++i) tot += red[i];
  float mean = tot / M;
  __syncthreads();
  float d = v - mean; float q = d * d;
  for (int o = 32; o; o >>= 1) q += __shfl_xor(q, o);
  if ((t & 63) == 0) red[t >> 6] = q;
  __syncthreads();
  float tq = 0; for (int i = 0; i < 8; ++i) tq += red[i];
  float var = tq / M;
  xn[b * M + t] = d / sqrtf(var + 1e-5f);
}
__global__ void k_gemv(const float* x, const unsigned short* W, float* y, int K, int N) {
  int n = blockIdx.x * blockDim.x + threadIdx.x;
  if (n >= N) return;
  float acc[8] = {0};
  for (int k = 0; k < K; ++k) {
    float w = bf2f(W[(size_t)k * N + n]);
    for (int b = 0; b < 8; ++b) acc[b] += x[b * K + k] * w;
  }
  for (int b = 0; b < 8; ++b) y[b * N + n] = acc[b];
}
__global__ void k_qabs(const float* q, const unsigned short* wuk, float* qabs) {
  int h = blockIdx.x; int c = threadIdx.x;
  float acc[8] = {0};
  for (int d = 0; d < D; ++d) {
    float w = bf2f(wuk[((size_t)c * H + h) * D + d]);
    for (int b = 0; b < 8; ++b) acc[b] += q[b * H * D + h * D + d] * w;
  }
  for (int b = 0; b < 8; ++b) {
    float v = acc[b] * 0.08838834764831845f;
    v = bf2f(f2bf(v));
    qabs[(b * H + h) * DC + c] = v;
  }
}
__global__ void k_scores(const float* qabs, const unsigned short* ckv, float* sc) {
  int b = blockIdx.y; int k = blockIdx.x * 64 + (threadIdx.x & 63); int hg = threadIdx.x >> 6;
  float acc[8] = {0};
  const unsigned short* row = ckv + ((size_t)b * SK + k) * DC;
  for (int c = 0; c < DC; ++c) {
    float v = bf2f(row[c]);
    for (int j = 0; j < 8; ++j) acc[j] += qabs[(b * H + hg * 8 + j) * DC + c] * v;
  }
  for (int j = 0; j < 8; ++j) sc[((size_t)b * H + hg * 8 + j) * SK + k] = acc[j];
}
__global__ void k_softpv(float* sc, const unsigned short* ckv, float* ctxl) {
  int bh = blockIdx.x; int b = bh / H; int t = threadIdx.x;
  __shared__ float red[16];
  __shared__ float p[SK];
  float* s = sc + (size_t)bh * SK;
  float m = -1e30f;
  for (int k = t; k < SK; k += 512) m = fmaxf(m, s[k]);
  for (int o = 32; o; o >>= 1) m = fmaxf(m, __shfl_xor(m, o));
  if ((t & 63) == 0) red[t >> 6] = m;
  __syncthreads();
  for (int i = 0; i < 8; ++i) m = fmaxf(m, red[i]);
  __syncthreads();
  float l = 0;
  for (int k = t; k < SK; k += 512) { float e = __expf(s[k] - m); p[k] = e; l += e; }
  for (int o = 32; o; o >>= 1) l += __shfl_xor(l, o);
  if ((t & 63) == 0) red[t >> 6] = l;
  __syncthreads();
  l = 0; for (int i = 0; i < 8; ++i) l += red[i];
  float acc = 0;
  for (int k = 0; k < SK; ++k) acc += p[k] * bf2f(ckv[((size_t)b * SK + k) * DC + t]);
  ctxl[(size_t)bh * DC + t] = acc / l;
}
__global__ void k_ctx(const float* ctxl, const unsigned short* wuv, float* ctx) {
  int h = blockIdx.x; int d = threadIdx.x;
  float acc[8] = {0};
  for (int c = 0; c < DC; ++c) {
    float w = bf2f(wuv[((size_t)c * H + h) * D + d]);
    for (int b = 0; b < 8; ++b) acc[b] += ctxl[(b * H + h) * DC + c] * w;
  }
  for (int b = 0; b < 8; ++b) ctx[b * H * D + h * D + d] = acc[b];
}
__global__ void k_act(const float* g, const float* u, float* hm) {
  int i = blockIdx.x * blockDim.x + threadIdx.x;
  if (i >= B * F) return;
  float x = g[i];
  float ge = 0.5f * x * (1.f + tanhf(0.7978845608028654f * (x + 0.044715f * x * x * x)));
  hm[i] = ge * u[i];
}
__global__ void k_final(const unsigned short* x, const float* a, const float* f, unsigned short* out) {
  int i = blockIdx.x * blockDim.x + threadIdx.x;
  out[i] = f2bf(bf2f(x[i]) + a[i] + f[i]);
}

extern "C" void kernel_launch(void* const* d_in, const int* in_sizes, int n_in, void* d_out, int out_size, void* d_ws, size_t ws_size, hipStream_t stream) {
  const unsigned short* x = (const unsigned short*)d_in[0];
  const unsigned short* ckv = (const unsigned short*)d_in[1];
  const unsigned short* wq = (const unsigned short*)d_in[2];
  const unsigned short* wuk = (const unsigned short*)d_in[3];
  const unsigned short* wuv = (const unsigned short*)d_in[4];
  const unsigned short* wo = (const unsigned short*)d_in[5];
  const unsigned short* wg = (const unsigned short*)d_in[7];
  const unsigned short* wu = (const unsigned short*)d_in[8];
  const unsigned short* wd = (const unsigned short*)d_in[9];
  float* ws = (float*)d_ws;
  float* xn = ws; ws += B * M;
  float* q = ws; ws += B * H * D;
  float* g = ws; ws += B * F;
  float* u = ws; ws += B * F;
  float* hm = ws; ws += B * F;
  float* qabs = ws; ws += B * H * DC;
  float* sc = ws; ws += (size_t)B * H * SK;
  float* ctxl = ws; ws += B * H * DC;
  float* ctx = ws; ws += B * H * D;
  float* ao = ws; ws += B * M;
  float* fo = ws; ws += B * M;
  k_ln<<<B, 512, 0, stream>>>(x, xn);
  k_gemv<<<H * D / 64, 64, 0, stream>>>(xn, wq, q, M, H * D);
  k_gemv<<<(F + 63) / 64, 64, 0, stream>>>(xn, wg, g, M, F);
  k_gemv<<<(F + 63) / 64, 64, 0, stream>>>(xn, wu, u, M, F);
  k_act<<<(B * F + 255) / 256, 256, 0, stream>>>(g, u, hm);
  k_gemv<<<M / 64, 64, 0, stream>>>(hm, wd, fo, F, M);
  k_qabs<<<H, 512, 0, stream>>>(q, wuk, qabs);
  k_scores<<<dim3(SK / 64, B), 256, 0, stream>>>(qabs, ckv, sc);
  k_softpv<<<B * H, 512, 0, stream>>>(sc, ckv, ctxl);
  k_ctx<<<H, 128, 0, stream>>>(ctxl, wuv, ctx);
  k_gemv<<<M / 64, 64, 0, stream>>>(ctx, wo, ao, H * D, M);
  k_final<<<B * M / 256, 256, 0, stream>>>(x, ao, fo, (unsigned short*)d_out);
}
```

```cpp
#include <hip/hip_runtime.h>
#include <hip/hip_bf16.h>
#include <stdint.h>

#define STAMPS 0
#define NROWS 32
#define A_AUX 2
#define W_NT 1
#define WLOAD(p) __builtin_nontemporal_load(p)
#define PDELAY 0
#define WIN 6

typedef unsigned v4u __attribute__((ext_vector_type(4)));
typedef unsigned v2u __attribute__((ext_vector_type(2)));
typedef float f32x2 __attribute__((ext_vector_type(2)));
typedef float f32x4 __attribute__((ext_vector_type(4)));
typedef float f32x16 __attribute__((ext_vector_type(16)));
typedef short bf16x8 __attribute__((ext_vector_type(8)));
typedef short s16x4 __attribute__((ext_vector_type(4)));
typedef __bf16 bf16x2 __attribute__((ext_vector_type(2)));
typedef unsigned short u16;
#define LAS __attribute__((address_space(3)))
#define GAS __attribute__((address_space(1)))

constexpr int NB = 8, NH = 32, SK = 4096, HD = 128, DM = 512, DC = 512, FF = 1376;
constexpr int NWG = 256, NTHR = 512;
constexpr unsigned WS_EPOCH = 0;
constexpr unsigned WS_QG = 4096;
constexpr unsigned WS_QA = WS_QG + 32 * 8 * 64 * 16;
constexpr unsigned WS_PF = WS_QA + 8 * 32 * 128 * 16;
constexpr unsigned WS_OG = WS_PF + 8 * 32 * 8 * 32 * 8;
constexpr unsigned WS_PART = WS_OG + 8 * 32 * 256 * 4 * 16;
constexpr unsigned WS_END = WS_PART + 8 * 32 * 32 * 512 * 2;
constexpr unsigned WS_STAMP = 32u << 20;
constexpr unsigned L_CKV = 0;
constexpr unsigned L_S = 128 * 1024;
constexpr unsigned L_QIMG = L_S;
constexpr unsigned L_SX = L_S;
constexpr unsigned L_P = L_S + 16384;
constexpr unsigned L_MX = L_S + 24576;
constexpr unsigned L_LS = L_S + 25600;
constexpr unsigned L_HM = L_S + 26624;
constexpr unsigned L_CL = L_S + 27648;
constexpr unsigned L_CX = L_S + 28928;
constexpr unsigned L_FIN = L_S + 31232;
constexpr unsigned LDS_BYTES = 160 * 1024;
constexpr int AUX_NT = 2, AUX_SC1 = 16;
constexpr unsigned SPIN_CAP = 1u << 18;

struct Params {
  const u16 *x, *ckv, *wq, *wuk, *wuv, *wo, *wg, *wu, *wd;
  u16* out;
  unsigned char* ws;
};

__device__ __forceinline__ float u2f(unsigned u) { return __uint_as_float(u); }
__device__ __forceinline__ unsigned f2u(float f) { return __float_as_uint(f); }
__device__ __forceinline__ float bflo(unsigned w) { return u2f(w << 16); }
__device__ __forceinline__ float bfhi(unsigned w) { return u2f(w & 0xffff0000u); }
__device__ __forceinline__ unsigned pk2(float lo, float hi) {
  f32x2 f = {lo, hi};
  return __builtin_bit_cast(unsigned, __builtin_convertvector(f, bf16x2));
}
__device__ __forceinline__ float dot_ones(unsigned a, float c) {
  return __builtin_amdgcn_fdot2_f32_bf16(__builtin_bit_cast(bf16x2, a), __builtin_bit_cast(bf16x2, 0x3f803f80u), c, false);
}
__device__ __forceinline__ f32x4 mfma16(v4u a, v4u b, f32x4 c) {
  return __builtin_amdgcn_mfma_f32_16x16x32_bf16(__builtin_bit_cast(bf16x8, a), __builtin_bit_cast(bf16x8, b), c, 0, 0, 0);
}
__device__ __forceinline__ f32x16 mfma32(v4u a, v4u b, f32x16 c) {
  return __builtin_amdgcn_mfma_f32_32x32x16_bf16(__builtin_bit_cast(bf16x8, a), __builtin_bit_cast(bf16x8, b), c, 0, 0, 0);
}
__device__ __forceinline__ unsigned swz(unsigned row) { return ((row & 3) << 2) | ((row >> 2) & 3); }
#define BAR() do { asm volatile("s_waitcnt lgkmcnt(0)" ::: "memory"); __builtin_amdgcn_s_barrier(); asm volatile("" ::: "memory"); } while (0)
#define VMWAIT_(n) asm volatile("s_waitcnt vmcnt(" #n ")" ::: "memory")
#define VMWAIT(n) VMWAIT_(n)
#define LGKM0() asm volatile("s_waitcnt lgkmcnt(0)" ::: "memory")
#define GLDS(src, dst, aux) __builtin_amdgcn_global_load_lds((const GAS unsigned*)(src), (LAS unsigned*)(dst), 16, 0, (aux))
__device__ __forceinline__ v2u tr_read(LAS unsigned char* a) {
  return __builtin_bit_cast(v2u, __builtin_amdgcn_ds_read_tr16_b64_v4i16((LAS s16x4*)a));
}
__device__ __forceinline__ v4u tr_read2(LAS unsigned char* a, LAS unsigned char* b) {
  const v2u x = tr_read(a), y = tr_read(b);
  return (v4u){x.x, x.y, y.x, y.y};
}
template <int CTRL>
__device__ __forceinline__ float dppm(float v) { return u2f(__builtin_amdgcn_update_dpp(f2u(v), f2u(v), CTRL, 0xf, 0xf, false)); }
__device__ __forceinline__ float xsum16(float v) {
  auto s = __builtin_amdgcn_permlane16_swap(f2u(v), f2u(v), false, false);
  return u2f(s[0]) + u2f(s[1]);
}
__device__ __forceinline__ float xmax16(float v) {
  auto s = __builtin_amdgcn_permlane16_swap(f2u(v), f2u(v), false, false);
  return fmaxf(u2f(s[0]), u2f(s[1]));
}
__device__ __forceinline__ float xsum32(float v) {
  auto s = __builtin_amdgcn_permlane32_swap(f2u(v), f2u(v), false, false);
  return u2f(s[0]) + u2f(s[1]);
}
__device__ __forceinline__ float xmax32(float v) {
  auto s = __builtin_amdgcn_permlane32_swap(f2u(v), f2u(v), false, false);
  return fmaxf(u2f(s[0]), u2f(s[1]));
}
__device__ __forceinline__ bool rec_ok(v4u r, unsigned tag) { return __all((r.y == tag) & (r.w == tag)); }

#define STAMP(k) do { } while (0)

#define GATHER16(qr, SENT, THR, ADDR, SLP)                                                                   \
  do {                                                                                                       \
    if (PDELAY && wave >= 2) { for (int z = 0; z < PDELAY; ++z) __builtin_amdgcn_s_sleep(16); }              \
    for (unsigned spin = 0; spin < SPIN_CAP; ++spin) {                                                       \
      const v2u sr = __builtin_amdgcn_raw_buffer_load_b64(wsr, (SENT), 0, AUX_SC1);                          \
      if (__builtin_popcountll(__ballot(sr.y == tag)) >= (THR)) break;                                       \
      __builtin_amdgcn_s_sleep(SLP);                                                                         \
    }                                                                                                        \
    unsigned done = 0;                                                                                       \
    for (unsigned spin = 0; spin < SPIN_CAP; ++spin) {                                                       \
      _Pragma("unroll") for (int i = 0; i < 16; ++i)                                                         \
        if (!((done >> i) & 1)) qr[i] = __builtin_amdgcn_raw_buffer_load_b128(wsr, (ADDR), 0, AUX_SC1);      \
      _Pragma("unroll") for (int i = 0; i < 16; ++i)                                                         \
        if (!((done >> i) & 1)) { if (rec_ok(qr[i], tag)) done |= 1u << i; }                                 \
      if (done == 0xffffu) break;                                                                            \
    }                                                                                                        \
  } while (0)

__global__ void __launch_bounds__(NTHR, 2) fused_kernel(Params p) {
  extern __shared__ __attribute__((aligned(16))) unsigned char lds_raw[];
  LAS unsigned char* L = (LAS unsigned char*)lds_raw;
  const int tid = threadIdx.x, lane = tid & 63;
  const int wave = __builtin_amdgcn_readfirstlane(tid >> 6);
  const int wg = blockIdx.x, xg = wg & 7, r = wg >> 3;
  const int bA = xg, chunk = r;
  const int hA = xg * 4 + (r >> 3), sl = r & 7;
  const int ft0 = (xg * 43) >> 1;
  const bool ffn = r < (((xg + 1) * 43) >> 1) - ft0;
  const int ft = ft0 + r;
  const __amdgpu_buffer_rsrc_t wsr = __builtin_amdgcn_make_buffer_rsrc((void*)p.ws, 0, WS_END, 0x00020000);
  const int l16 = lane & 15, g4 = lane >> 4;
  STAMP(0);
  const unsigned tag = *(const __attribute__((address_space(4))) unsigned*)(p.ws + WS_EPOCH) + 1u;
  v4u hmv = {0u, 0u, 0u, 0u};
  v4u qr[16];

  if (wave >= 4) {
    const int v = wave - 4;
    const u16* src0 = p.ckv + ((size_t)bA * SK + chunk * 128 + v) * DC + (lane & 48) * 8;
    unsigned lo[4];
#pragma unroll
    for (int k = 0; k < 4; ++k) lo[k] = ((lane & 15) ^ ((v << 2) | k)) * 8;
#pragma unroll
    for (int i = 0; i < NROWS; ++i) {
      GLDS(src0 + (size_t)i * 4 * DC + lo[i & 3], L + L_CKV + (i * 4 + v) * 1024, AUX_NT);
      if (i >= WIN - 1 && i < 31) __builtin_amdgcn_s_waitcnt(0x0f70 | ((WIN - 1) & 15) | (((WIN - 1) >> 4) << 14));
    }
    STAMP(1);
  } else {
    if (wave == 0 || (wave == 1 && ffn)) {
      const unsigned st = L_S + wave * 16384;
      {
        const u16* src; int stride;
        if (wave == 0) { stride = NH * HD; src = p.wq + (size_t)(lane >> 1) * stride + hA * HD + sl * 16 + (lane & 1) * 8; }
        else { stride = FF; src = ((lane & 1) ? p.wu : p.wg) + (size_t)(lane >> 1) * stride + ft * 8; }
#pragma unroll
        for (int i = 0; i < 16; ++i) GLDS(src + (size_t)i * 32 * stride, L + st + i * 1024, A_AUX);
      }
      STAMP(10);
      v4u xr[16];
      {
        const u16* xs = p.x + (l16 & 7) * DM + g4 * 8;
#pragma unroll
        for (int s = 0; s < 16; ++s) xr[s] = *(const GAS v4u*)(xs + s * 32);
      }
      if (l16 >= 8) {
#pragma unroll
        for (int s = 0; s < 16; ++s) xr[s] = (v4u){0x3f803f80u, 0x3f803f80u, 0x3f803f80u, 0x3f803f80u};
      }
      STAMP(11);
      float mean, rstd;
      {
        f32x4 g = {0.f, 0.f, 0.f, 0.f};
#pragma unroll
        for (int s = 0; s < 16; ++s) g = mfma16(xr[s], xr[s], g);
        const float dsel = (l16 & 2) ? ((l16 & 1) ? g[3] : g[2]) : ((l16 & 1) ? g[1] : g[0]);
        const float sq = u2f(__builtin_amdgcn_ds_bpermute(((l16 & 7) + 16 * ((l16 & 7) >> 2)) * 4, f2u(dsel)));
        const float sm = u2f(__builtin_amdgcn_ds_bpermute(((l16 & 7) + 32) * 4, f2u(g[0])));
        mean = sm * (1.f / DM);
        rstd = 1.f / sqrtf(fmaxf(sq * (1.f / DM) - mean * mean, 0.f) + 1e-5f);
      }
      asm volatile("" :: "v"(rstd));
      STAMP(12);
      VMWAIT(0);
      STAMP(1);
      f32x4 acc = {0.f, 0.f, 0.f, 0.f};
      {
        LAS unsigned char* a0 = L + st + g4 * 256 + l16 * 8;
#pragma unroll
        for (int s = 0; s < 16; ++s) acc = mfma16(tr_read2(a0 + s * 1024, a0 + s * 1024 + 128), xr[s], acc);
      }
      float y[4];
#pragma unroll
      for (int j = 0; j < 4; ++j) y[j] = (acc[j] - mean * dppm<0x158>(acc[j])) * rstd;
      if (wave == 0) {
        unsigned pl[4];
#pragma unroll
        for (int j = 0; j < 4; ++j) { const unsigned h = pk2(y[j], y[j]); pl[j] = pk2(y[j], y[j] - bfhi(h)); }
        const unsigned orec = WS_QG + ((hA * 8 + (l16 & 7)) * 64 + sl * 8 + g4 * 2) * 16;
        if (l16 < 8) {
          __builtin_amdgcn_raw_buffer_store_b128((v4u){pl[0], tag, pl[1], tag}, wsr, orec, 0, AUX_SC1);
          __builtin_amdgcn_raw_buffer_store_b128((v4u){pl[2], tag, pl[3], tag}, wsr, orec + 16, 0, AUX_SC1);
        }
      } else {
        float hm[4];
#pragma unroll
        for (int j = 0; j < 4; ++j) {
          auto s = __builtin_amdgcn_permlane32_swap(f2u(y[j]), f2u(y[j]), false, false);
          const float gv = u2f(s[0]), uv = u2f(s[1]);
          const float yy = 0.7978845608028654f * (gv + 0.044715f * gv * gv * gv);
          const float th = 1.f - 2.f / (__expf(2.f * yy) + 1.f);
          hm[j] = 0.5f * gv * (1.f + th) * uv;
        }
        const unsigned a = pk2(hm[0], hm[1]), b = pk2(hm[2], hm[3]);
        auto sa = __builtin_amdgcn_permlane16_swap(a, a, false, false);
        auto sb = __builtin_amdgcn_permlane16_swap(b, b, false, false);
        hmv = (v4u){sa[0], sb[0], sa[1], sb[1]};
      }
      STAMP(2);
    } else if (wave == 2) {
      v4u wf[16];
      {
        const u16* src = p.wuk + ((size_t)(sl * 64 + l16) * NH + hA) * HD + g4 * 8;
#pragma unroll
        for (int ct = 0; ct < 4; ++ct)
#pragma unroll
          for (int s = 0; s < 4; ++s) wf[ct * 4 + s] = WLOAD((const GAS v4u*)(src + (size_t)ct * 16 * NH * HD + s * 32));
      }
      const unsigned qoff = WS_QG + ((hA * 8 + (l16 & 7)) * 64 + g4 * 4) * 16;
      GATHER16(qr, WS_QG + ((hA * 8 + (lane >> 3)) * 64 + (lane & 7) * 8) * 16, 24, qoff + ((i >> 2) * 16 + (i & 3)) * 16, 1);
      STAMP(1);
      const unsigned sel = l16 < 8 ? 0x05040100u : 0x07060302u;
      v4u bq[4];
#pragma unroll
      for (int s = 0; s < 4; ++s)
#pragma unroll
        for (int k = 0; k < 4; ++k) bq[s][k] = __builtin_amdgcn_perm(qr[s * 4 + k].z, qr[s * 4 + k].x, sel);
      const float scale = 0.08838834764831845f * 1.4426950408889634f;
#pragma unroll
      for (int ct = 0; ct < 4; ++ct) {
        f32x4 acc = {0.f, 0.f, 0.f, 0.f};
#pragma unroll
        for (int s = 0; s < 4; ++s) acc = mfma16(wf[ct * 4 + s], bq[s], acc);
        float y[4];
#pragma unroll
        for (int j = 0; j < 4; ++j) y[j] = (acc[j] + dppm<0x128>(acc[j])) * scale;
        if (l16 < 8)
          __builtin_amdgcn_raw_buffer_store_b128((v4u){pk2(y[0], y[1]), tag, pk2(y[2], y[3]), tag}, wsr,
                                                 WS_QA + ((l16 * 32 + hA) * 128 + sl * 16 + ct * 4 + g4) * 16, 0, AUX_SC1);
      }
      STAMP(2);
    }
    const unsigned qoff = WS_QA + ((bA * 32 + wave * 8) * 128 + lane) * 16;
    GATHER16(qr, WS_QA + ((bA * 32 + wave * 8 + (lane >> 3)) * 128 + (lane & 7) * 16) * 16, 40, qoff + i * 1024, 4);
    STAMP(3);
  }

  v4u wo_[16], wv[2], wdn;
  {
    const u16* src = p.wo + ((size_t)hA * HD + (lane >> 3)) * DM + wave * 64 + (lane & 7) * 8;
#pragma unroll
    for (int i = 0; i < 16; ++i) wo_[i] = WLOAD((const GAS v4u*)(src + (size_t)i * 8 * DM));
    const u16* sv = p.wuv + ((size_t)(sl * 64 + (lane >> 1)) * NH + hA) * HD + wave * 16 + (lane & 1) * 8;
    wv[0] = WLOAD((const GAS v4u*)sv);
    wv[1] = WLOAD((const GAS v4u*)(sv + (size_t)32 * NH * HD));
    wdn = WLOAD((const GAS v4u*)(p.wd + (size_t)((ffn ? ft : 0) * 8 + (lane >> 3)) * DM + wave * 64 + (lane & 7) * 8));
  }
  asm volatile("" :: "s"(tag));
  BAR();
  if (wave < 4) {
#pragma unroll
    for (int i = 0; i < 16; ++i) {
      const unsigned h = wave * 8 + (i >> 1);
      const unsigned chn = lane >> 1;
      *(LAS v2u*)(L + L_QIMG + (i & 1) * 16384 + h * 512 + (((chn & 16) | ((chn & 15) ^ swz(h))) * 16) + (lane & 1) * 8) = (v2u){qr[i].x, qr[i].z};
    }
  } else {
    VMWAIT(19);
  }
  BAR();
  STAMP(4);

  const int cb = wave & 3, rh = wave >> 2;
  const int l32 = lane & 31, hf = lane >> 5;
  const unsigned tz = (hf ^ swz(l32)) * 16;
  float m, lsum;
  f32x16 o0, o1;
  {
    v4u qf[16];
    {
      const unsigned qb = L_QIMG + rh * 16384 + l32 * 512 + tz;
#pragma unroll
      for (int s = 0; s < 16; ++s) qf[s] = *(const LAS v4u*)(L + (qb ^ ((s & 7) * 32)) + (s >> 3) * 256);
    }
    f32x16 sacc;
#pragma unroll
    for (int i = 0; i < 16; ++i) sacc[i] = 0.f;
    const unsigned kb = L_CKV + (cb * 32 + l32) * 1024 + rh * 512 + tz;
    BAR();
#pragma unroll
    for (int s = 0; s < 16; ++s) {
      v4u a = *(const LAS v4u*)(L + (kb ^ ((s & 7) * 32)) + (s >> 3) * 256);
      sacc = mfma32(a, qf[s], sacc);
    }
    f32x4 k0, k1;
    if (rh) {
      *(LAS f32x4*)(L + L_SX + wave * 2048 + lane * 16) = (f32x4){sacc[0], sacc[1], sacc[2], sacc[3]};
      *(LAS f32x4*)(L + L_SX + wave * 2048 + 1024 + lane * 16) = (f32x4){sacc[4], sacc[5], sacc[6], sacc[7]};
      k0 = (f32x4){sacc[8], sacc[9], sacc[10], sacc[11]}; k1 = (f32x4){sacc[12], sacc[13], sacc[14], sacc[15]};
    } else {
      *(LAS f32x4*)(L + L_SX + wave * 2048 + lane * 16) = (f32x4){sacc[8], sacc[9], sacc[10], sacc[11]};
      *(LAS f32x4*)(L + L_SX + wave * 2048 + 1024 + lane * 16) = (f32x4){sacc[12], sacc[13], sacc[14], sacc[15]};
      k0 = (f32x4){sacc[0], sacc[1], sacc[2], sacc[3]}; k1 = (f32x4){sacc[4], sacc[5], sacc[6], sacc[7]};
    }
    if (wave == 1 && lane < 8) *(LAS v4u*)(L + L_HM + lane * 16) = hmv;
    BAR();
    k0 += *(const LAS f32x4*)(L + L_SX + (wave ^ 4) * 2048 + lane * 16);
    k1 += *(const LAS f32x4*)(L + L_SX + (wave ^ 4) * 2048 + 1024 + lane * 16);
    float mx = fmaxf(fmaxf(fmaxf(k0[0], k0[1]), fmaxf(k0[2], k0[3])), fmaxf(fmaxf(k1[0], k1[1]), fmaxf(k1[2], k1[3])));
    mx = xmax32(mx);
    if (lane < 32) *(LAS float*)(L + L_MX + lane * 32 + wave * 4) = mx;
    BAR();
    {
      const f32x4 a = *(const LAS f32x4*)(L + L_MX + l32 * 32), b = *(const LAS f32x4*)(L + L_MX + l32 * 32 + 16);
      m = fmaxf(fmaxf(fmaxf(a[0], a[1]), fmaxf(a[2], a[3])), fmaxf(fmaxf(b[0], b[1]), fmaxf(b[2], b[3])));
    }
    const unsigned pa0 = pk2(__builtin_amdgcn_exp2f(k0[0] - m), __builtin_amdgcn_exp2f(k0[1] - m));
    const unsigned pa1 = pk2(__builtin_amdgcn_exp2f(k0[2] - m), __builtin_amdgcn_exp2f(k0[3] - m));
    const unsigned pb0 = pk2(__builtin_amdgcn_exp2f(k1[0] - m), __builtin_amdgcn_exp2f(k1[1] - m));
    const unsigned pb1 = pk2(__builtin_amdgcn_exp2f(k1[2] - m), __builtin_amdgcn_exp2f(k1[3] - m));
    {
      const unsigned z = swz(l32), c0 = cb * 4 + rh * 2;
      *(LAS v2u*)(L + L_P + l32 * 256 + ((c0 ^ z) * 16) + hf * 8) = (v2u){pa0, pa1};
      *(LAS v2u*)(L + L_P + l32 * 256 + (((c0 + 1) ^ z) * 16) + hf * 8) = (v2u){pb0, pb1};
    }
    float ls = dot_ones(pb1, dot_ones(pb0, dot_ones(pa1, dot_ones(pa0, 0.f))));
    ls = xsum32(ls);
    if (lane < 32) *(LAS float*)(L + L_LS + lane * 32 + wave * 4) = ls;
    BAR();
    v4u pf[8];
    {
      const unsigned pbs = L_P + l32 * 256 + tz;
#pragma unroll
      for (int ks = 0; ks < 8; ++ks) pf[ks] = *(const LAS v4u*)(L + (pbs ^ (ks * 32)));
    }
#pragma unroll
    for (int i = 0; i < 16; ++i) { o0[i] = 0.f; o1[i] = 0.f; }
    {
      const unsigned blk = (lane >> 4) & 1, q = (lane & 15) >> 2, pp = lane & 3;
      unsigned ta[4];
#pragma unroll
      for (int ct = 0; ct < 2; ++ct)
#pragma unroll
        for (int tt = 0; tt < 2; ++tt) {
          const unsigned row = 8 * hf + 4 * tt + q;
          const unsigned chn = wave * 8 + ct * 4 + 2 * blk + (pp >> 1);
          ta[ct * 2 + tt] = L_CKV + row * 1024 + (((chn & 48) | ((chn & 15) ^ swz(row))) * 16) + 8 * (pp & 1);
        }
#pragma unroll
      for (int ks = 0; ks < 8; ++ks) {
        o0 = mfma32(tr_read2(L + ta[0] + ks * 16384, L + ta[1] + ks * 16384), pf[ks], o0);
        o1 = mfma32(tr_read2(L + ta[2] + ks * 16384, L + ta[3] + ks * 16384), pf[ks], o1);
      }
    }
    {
      const f32x4 a = *(const LAS f32x4*)(L + L_LS + l32 * 32), b = *(const LAS f32x4*)(L + L_LS + l32 * 32 + 16);
      lsum = ((a[0] + a[1]) + (a[2] + a[3])) + ((b[0] + b[1]) + (b[2] + b[3]));
    }
  }
  STAMP(5);
  const unsigned wb = L_CKV + (wave >> 1) * 256;
  const unsigned wq8 = (wave & 1) * 8;
  const unsigned r8 = lane >> 3, q8 = lane & 7;
  const unsigned sc_e = wb + r8 * 1024 + (((wq8 + q8) ^ swz(r8)) * 16);
  const unsigned sc_o = wb + (r8 + 8) * 1024 + (((wq8 + q8) ^ swz(r8 + 8)) * 16);
  {
    const float inv = __builtin_amdgcn_rcpf(lsum);
    o0 *= inv; o1 *= inv;
    const unsigned wa = wb + l32 * 1024 + (((wq8 + hf) ^ swz(l32)) * 16);
#pragma unroll
    for (int gp = 0; gp < 4; ++gp) {
      const f32x16& o = gp < 2 ? o0 : o1;
      const int b8 = (gp & 1) * 8;
      auto rx = __builtin_amdgcn_permlane32_swap(pk2(o[b8 + 0], o[b8 + 1]), pk2(o[b8 + 4], o[b8 + 5]), false, false);
      auto ry = __builtin_amdgcn_permlane32_swap(pk2(o[b8 + 2], o[b8 + 3]), pk2(o[b8 + 6], o[b8 + 7]), false, false);
      *(LAS v4u*)(L + (wa ^ (gp * 32))) = (v4u){rx[0], ry[0], rx[1], ry[1]};
    }
    LGKM0();
    const unsigned pbase = WS_PART + ((((bA * 32 + r8) * 8 + wave) * 32 + chunk) * 64 + q8 * 8) * 2;
#pragma unroll
    for (int i = 0; i < 4; ++i) {
      const v4u t = *(const LAS v4u*)(L + ((i & 1) ? sc_o : sc_e) + (i >> 1) * 16384);
      __builtin_amdgcn_raw_buffer_store_b128(t, wsr, pbase + i * (8 * 8 * 32 * 128), 0, AUX_SC1);
    }
    const unsigned lse = f2u(m + __builtin_amdgcn_logf(lsum));
    VMWAIT(0);
    if (lane < 32)
      __builtin_amdgcn_raw_buffer_store_b64((v2u){lse, tag}, wsr, WS_PF + (((bA * 32 + lane) * 8 + wave) * 32 + (chunk & 3) * 8 + (chunk >> 2)) * 8, 0, AUX_SC1);
    STAMP(6);
  }
  v4u av[2];
  f32x4 accE[4];
  {
    LGKM0();
#pragma unroll
    for (int i = 0; i < 16; ++i) *(LAS v4u*)(L + ((i & 1) ? sc_o : sc_e) + (i >> 1) * 16384) = wo_[i];
    LGKM0();
    const unsigned rr = 8 * g4 + (l16 >> 2);
    const unsigned pc = (l16 & 3) >> 1, ph = (l16 & 1) * 8;
    const unsigned t0 = wb + rr * 1024 + (((wq8 + pc) ^ swz(rr)) * 16) + ph;
    const unsigned t1 = wb + (rr + 4) * 1024 + (((wq8 + pc) ^ swz(rr + 4)) * 16) + ph;
#pragma unroll
    for (int t = 0; t < 4; ++t)
#pragma unroll
      for (int ks = 0; ks < 4; ++ks) wo_[t * 4 + ks] = tr_read2(L + (t0 ^ (t * 32)) + ks * 32768, L + (t1 ^ (t * 32)) + ks * 32768);
    LGKM0();
    {
      const unsigned rv = lane >> 1;
      const unsigned a = wb + rv * 1024 + (((wq8 + (lane & 1)) ^ swz(rv)) * 16);
      *(LAS v4u*)(L + a) = wv[0];
      *(LAS v4u*)(L + a + 32768) = wv[1];
      *(LAS v4u*)(L + sc_e + 65536) = wdn;
    }
    LGKM0();
    av[0] = tr_read2(L + t0, L + t1);
    av[1] = tr_read2(L + t0 + 32768, L + t1 + 32768);
#pragma unroll
    for (int t = 0; t < 4; ++t) accE[t] = (f32x4){0.f, 0.f, 0.f, 0.f};
    if (ffn) {
      const unsigned rd = l16 >> 2;
      const unsigned d0 = wb + 65536 + rd * 1024 + (((wq8 + pc) ^ swz(rd)) * 16) + ph;
      const unsigned d1 = wb + 65536 + (rd + 4) * 1024 + (((wq8 + pc) ^ swz(rd + 4)) * 16) + ph;
      v4u hb = *(const LAS v4u*)(L + L_HM + (l16 & 7) * 16);
      if (g4) hb = (v4u){0u, 0u, 0u, 0u};
#pragma unroll
      for (int t = 0; t < 4; ++t) accE[t] = mfma16(tr_read2(L + (d0 ^ (t * 32)), L + (d1 ^ (t * 32))), hb, accE[t]);
    }
  }
  {
    v4u fr[4];
    const unsigned fo = WS_PF + (((wave * 32 + hA) * 8 + sl) * 32 + g4 * 8) * 8;
    for (unsigned spin = 0; spin < SPIN_CAP; ++spin) {
#pragma unroll
      for (int i = 0; i < 4; ++i) fr[i] = __builtin_amdgcn_raw_buffer_load_b128(wsr, fo + i * 16, 0, AUX_SC1);
      unsigned mn = __builtin_elementwise_min(__builtin_elementwise_min(fr[0].y, fr[0].w), __builtin_elementwise_min(fr[1].y, fr[1].w));
      unsigned mx = __builtin_elementwise_max(__builtin_elementwise_max(fr[0].y, fr[0].w), __builtin_elementwise_max(fr[1].y, fr[1].w));
      mn = __builtin_elementwise_min(mn, __builtin_elementwise_min(__builtin_elementwise_min(fr[2].y, fr[2].w), __builtin_elementwise_min(fr[3].y, fr[3].w)));
      mx = __builtin_elementwise_max(mx, __builtin_elementwise_max(__builtin_elementwise_max(fr[2].y, fr[2].w), __builtin_elementwise_max(fr[3].y, fr[3].w)));
      if (__all((mn == tag) & (mx == tag))) break;
      __builtin_amdgcn_s_sleep(1);
    }
    asm volatile("" ::: "memory");
    STAMP(7);
    v2u pr[8];
    const unsigned po = WS_PART + ((((wave * 32 + hA) * 8 + sl) * 32 + g4) * 64 + l16 * 4) * 2;
#pragma unroll
    for (int u = 0; u < 8; ++u) pr[u] = __builtin_amdgcn_raw_buffer_load_b64(wsr, po + u * 512, 0, AUX_SC1);
    float ls[8];
#pragma unroll
    for (int i = 0; i < 4; ++i) { ls[2 * i] = u2f(fr[i].x); ls[2 * i + 1] = u2f(fr[i].z); }
    float M = fmaxf(fmaxf(fmaxf(ls[0], ls[1]), fmaxf(ls[2], ls[3])), fmaxf(fmaxf(ls[4], ls[5]), fmaxf(ls[6], ls[7])));
    M = xmax32(xmax16(M));
    const unsigned e4 = l16 & 3;
    const f32x2 m0 = {e4 == 0 ? 1.f : 0.f, e4 == 1 ? 1.f : 0.f}, m1 = {e4 == 2 ? 1.f : 0.f, e4 == 3 ? 1.f : 0.f};
    unsigned wa[16];
    float Ls = 0.f;
#pragma unroll
    for (int u = 0; u < 8; ++u) {
      const float w = __builtin_amdgcn_exp2f(ls[u] - M);
      const f32x2 a = (f32x2){w, w} * m0, b = (f32x2){w, w} * m1;
      wa[2 * u] = pk2(a.x, a.y); wa[2 * u + 1] = pk2(b.x, b.y);
      Ls = dot_ones(wa[2 * u] | wa[2 * u + 1], Ls);
    }
    Ls = xsum32(xsum16(Ls));
    const float inv = __builtin_amdgcn_rcpf(Ls);
    f32x4 acc = {0.f, 0.f, 0.f, 0.f};
#pragma unroll
    for (int u = 0; u < 4; ++u)
      acc = mfma16((v4u){wa[4 * u], wa[4 * u + 1], wa[4 * u + 2], wa[4 * u + 3]}, (v4u){pr[2 * u].x, pr[2 * u].y, pr[2 * u + 1].x, pr[2 * u + 1].y}, acc);
    if (lane < 16) *(LAS v2u*)(L + L_CL + wave * 144 + lane * 8) = (v2u){pk2(acc[0] * inv, acc[1] * inv), pk2(acc[2] * inv, acc[3] * inv)};
    BAR();
  }
  {
    const v4u b0 = *(const LAS v4u*)(L + L_CL + (l16 & 7) * 144 + g4 * 16), b1 = *(const LAS v4u*)(L + L_CL + (l16 & 7) * 144 + 64 + g4 * 16);
    f32x4 acc = {0.f, 0.f, 0.f, 0.f};
    acc = mfma16(av[0], b0, acc);
    acc = mfma16(av[1], b1, acc);
    if (l16 < 8) *(LAS v2u*)(L + L_CX + l16 * 272 + wave * 32 + g4 * 8) = (v2u){pk2(acc[0], acc[1]), pk2(acc[2], acc[3])};
    BAR();
  }
  {
    v4u bx[4];
#pragma unroll
    for (int ks = 0; ks < 4; ++ks) bx[ks] = *(const LAS v4u*)(L + L_CX + (l16 & 7) * 272 + ks * 64 + g4 * 16);
#pragma unroll
    for (int t = 0; t < 4; ++t) {
#pragma unroll
      for (int ks = 0; ks < 4; ++ks) accE[t] = mfma16(wo_[t * 4 + ks], bx[ks], accE[t]);
      if (l16 < 8)
        __builtin_amdgcn_raw_buffer_store_b128((v4u){pk2(accE[t][0], accE[t][1]), tag, pk2(accE[t][2], accE[t][3]), tag}, wsr,
                                               WS_OG + (((l16 * 32 + wave * 4 + t) * 256 + wg) * 4 + g4) * 16, 0, AUX_SC1);
    }
    STAMP(8);
  }
  {
    const unsigned oo = WS_OG + ((wg * 256 + wave * 32) * 4 + lane) * 16;
    v4u r0, r1;
    for (unsigned spin = 0; spin < SPIN_CAP; ++spin) {
      r0 = __builtin_amdgcn_raw_buffer_load_b128(wsr, oo, 0, AUX_SC1);
      r1 = __builtin_amdgcn_raw_buffer_load_b128(wsr, oo + 1024, 0, AUX_SC1);
      if (__all((r0.y == tag) & (r0.w == tag) & (r1.y == tag) & (r1.w == tag))) break;
      __builtin_amdgcn_s_sleep(1);
    }
    const unsigned e4 = l16 & 3;
    const unsigned one = (e4 & 1) ? 0x3f800000u : 0x00003f80u;
    const unsigned s0 = e4 < 2 ? one : 0u, s1 = e4 < 2 ? 0u : one;
    f32x4 acc = {0.f, 0.f, 0.f, 0.f};
    acc = mfma16((v4u){s0, s1, s0, s1}, (v4u){r0.x, r0.z, r1.x, r1.z}, acc);
#pragma unroll
    for (int j = 0; j < 4; ++j) { acc[j] += dppm<0x124>(acc[j]); acc[j] += dppm<0x128>(acc[j]); }
    if (lane < 4) *(LAS f32x4*)(L + L_FIN + wave * 64 + lane * 16) = acc;
    const unsigned xw = *(const GAS unsigned*)(p.x + wg * 16 + (lane & 7) * 2);
    BAR();
    if (wave == 0) {
      const f32x2 t = *(const LAS f32x2*)(L + L_FIN + (lane >> 3) * 64 + (lane & 7) * 8);
      float v0 = t.x, v1 = t.y;
      v0 += dppm<0x128>(v0); v1 += dppm<0x128>(v1);
      v0 = xsum16(v0); v1 = xsum16(v1);
      v0 = xsum32(v0); v1 = xsum32(v1);
      if (lane < 8) *(GAS unsigned*)(p.out + wg * 16 + lane * 2) = pk2(bflo(xw) + v0, bfhi(xw) + v1);
      if (wg == 0 && lane == 0) __builtin_amdgcn_raw_buffer_store_b32(tag, wsr, WS_EPOCH, 0, AUX_SC1);
    }
    STAMP(9);
  }
}

extern "C" void kernel_launch(void* const* d_in, const int* in_sizes, int n_in, void* d_out, int out_size, void* d_ws, size_t ws_size,
                              hipStream_t stream) {
  Params p;
  p.x = (const u16*)d_in[0]; p.ckv = (const u16*)d_in[1]; p.wq = (const u16*)d_in[2]; p.wuk = (const u16*)d_in[3];
  p.wuv = (const u16*)d_in[4]; p.wo = (const u16*)d_in[5]; p.wg = (const u16*)d_in[7]; p.wu = (const u16*)d_in[8]; p.wd = (const u16*)d_in[9];
  p.out = (u16*)d_out; p.ws = (unsigned char*)d_ws;
  (void)hipFuncSetAttribute((const void*)fused_kernel, hipFuncAttributeMaxDynamicSharedMemorySize, LDS_BYTES);
  fused_kernel<<<NWG, NTHR, LDS_BYTES, stream>>>(p);
}
```

```cpp
#include <hip/hip_runtime.h>
#include <hip/hip_bf16.h>
#include <stdint.h>

#define STAMPS 0
#define WIN 6
#define LDELAY 0
#define FDELAY 40

typedef unsigned v4u __attribute__((ext_vector_type(4)));
typedef unsigned v2u __attribute__((ext_vector_type(2)));
typedef float f32x2 __attribute__((ext_vector_type(2)));
typedef float f32x4 __attribute__((ext_vector_type(4)));
typedef float f32x16 __attribute__((ext_vector_type(16)));
typedef short bf16x8 __attribute__((ext_vector_type(8)));
typedef short s16x4 __attribute__((ext_vector_type(4)));
typedef __bf16 bf16x2 __attribute__((ext_vector_type(2)));
typedef unsigned short u16;
#define LAS __attribute__((address_space(3)))
#define GAS __attribute__((address_space(1)))

constexpr int NB = 8, NH = 32, SK = 4096, HD = 128, DM = 512, DC = 512, FF = 1376;
constexpr int NWG = 256, NTHR = 512;
constexpr unsigned WS_EPOCH = 0;
constexpr unsigned WS_QG = 4096;
constexpr unsigned WS_QA = WS_QG + 32 * 4 * 256 * 16;
constexpr unsigned WS_GU = WS_QA + 8 * 32 * 128 * 16;
constexpr unsigned WS_HM = WS_GU + 8 * 176 * 32 * 16;
constexpr unsigned WS_FL = WS_HM + 176 * 8 * 2 * 16;
constexpr unsigned WS_CX = WS_FL + 8 * 8 * 32 * 4 * 8;
constexpr unsigned WS_OG = WS_CX + 32 * 8 * 8 * 8 * 4 * 16;
constexpr unsigned WS_PART = WS_OG + 32 * 32 * 8 * 4 * 16;
constexpr unsigned WS_END = WS_PART + 8 * 32 * 32 * 512 * 2;
constexpr unsigned WS_STAMP = 32u << 20;
constexpr unsigned L_CKV = 0;
constexpr unsigned L_STG = 96 * 1024;
constexpr unsigned L_S = 128 * 1024;
constexpr unsigned L_PROG = L_S + 63 * 16;
constexpr unsigned L_IMG = L_S + 62 * 16;
constexpr unsigned L_FLG = L_S + 1024 + (63 ^ 4) * 16;
constexpr unsigned L_GO = L_S + 1024 + (62 ^ 4) * 16;
constexpr unsigned L_STAT = L_S + 4096;
constexpr unsigned L_UK = L_S + 16384;
constexpr unsigned L_CL = L_S + 512;
constexpr unsigned L_CX = L_S + 16384;
constexpr unsigned LDS_BYTES = 160 * 1024;
constexpr int AUX_NT = 2, AUX_SC1 = 16;
constexpr unsigned SPIN_CAP = 1u << 18;

struct Params {
  const u16 *x, *ckv, *wq, *wuk, *wuv, *wo, *wg, *wu, *wd;
  u16* out;
  unsigned char* ws;
};

__device__ __forceinline__ float u2f(unsigned u) { return __uint_as_float(u); }
__device__ __forceinline__ unsigned f2u(float f) { return __float_as_uint(f); }
__device__ __forceinline__ float bflo(unsigned w) { return u2f(w << 16); }
__device__ __forceinline__ float bfhi(unsigned w) { return u2f(w & 0xffff0000u); }
__device__ __forceinline__ unsigned pk2(float lo, float hi) {
  f32x2 f = {lo, hi};
  return __builtin_bit_cast(unsigned, __builtin_convertvector(f, bf16x2));
}
__device__ __forceinline__ float dot_ones(unsigned a, float c) {
  return __builtin_amdgcn_fdot2_f32_bf16(__builtin_bit_cast(bf16x2, a), __builtin_bit_cast(bf16x2, 0x3f803f80u), c, false);
}
__device__ __forceinline__ f32x4 mfma16(v4u a, v4u b, f32x4 c) {
  return __builtin_amdgcn_mfma_f32_16x16x32_bf16(__builtin_bit_cast(bf16x8, a), __builtin_bit_cast(bf16x8, b), c, 0, 0, 0);
}
__device__ __forceinline__ f32x16 mfma32(v4u a, v4u b, f32x16 c) {
  return __builtin_amdgcn_mfma_f32_32x32x16_bf16(__builtin_bit_cast(bf16x8, a), __builtin_bit_cast(bf16x8, b), c, 0, 0, 0);
}
__device__ __forceinline__ unsigned swz(unsigned row) { return ((row & 3) << 2) | ((row >> 2) & 3); }
#define BAR() do { asm volatile("s_waitcnt lgkmcnt(0)" ::: "memory"); __builtin_amdgcn_s_barrier(); asm volatile("" ::: "memory"); } while (0)
#define VMWAIT_(n) asm volatile("s_waitcnt vmcnt(" #n ")" ::: "memory")
#define VMWAIT(n) VMWAIT_(n)
#define LGKM0() asm volatile("s_waitcnt lgkmcnt(0)" ::: "memory")
#define GLDS(src, dst, aux) __builtin_amdgcn_global_load_lds((const GAS unsigned*)(src), (LAS unsigned*)(dst), 16, 0, (aux))
#define WLOAD(p) __builtin_nontemporal_load(p)
__device__ __forceinline__ v2u tr_read(LAS unsigned char* a) {
  return __builtin_bit_cast(v2u, __builtin_amdgcn_ds_read_tr16_b64_v4i16((LAS s16x4*)a));
}
__device__ __forceinline__ v4u tr_read2(LAS unsigned char* a, LAS unsigned char* b) {
  const v2u x = tr_read(a), y = tr_read(b);
  return (v4u){x.x, x.y, y.x, y.y};
}
template <int CTRL>
__device__ __forceinline__ unsigned dppu(unsigned v) { return __builtin_amdgcn_update_dpp(v, v, CTRL, 0xf, 0xf, false); }
template <int CTRL>
__device__ __forceinline__ float dppm(float v) { return u2f(dppu<CTRL>(f2u(v))); }
__device__ __forceinline__ float xsum16(float v) {
  auto s = __builtin_amdgcn_permlane16_swap(f2u(v), f2u(v), false, false);
  return u2f(s[0]) + u2f(s[1]);
}
__device__ __forceinline__ float xmax16(float v) {
  auto s = __builtin_amdgcn_permlane16_swap(f2u(v), f2u(v), false, false);
  return fmaxf(u2f(s[0]), u2f(s[1]));
}
__device__ __forceinline__ float xsum32(float v) {
  auto s = __builtin_amdgcn_permlane32_swap(f2u(v), f2u(v), false, false);
  return u2f(s[0]) + u2f(s[1]);
}
__device__ __forceinline__ float xmax32(float v) {
  auto s = __builtin_amdgcn_permlane32_swap(f2u(v), f2u(v), false, false);
  return fmaxf(u2f(s[0]), u2f(s[1]));
}
__device__ __forceinline__ bool rec_ok(v4u r, unsigned tag) { return __all((r.y == tag) & (r.w == tag)); }
__device__ __forceinline__ unsigned umin4(v4u a) { return __builtin_elementwise_min(__builtin_elementwise_min(a.x, a.y), __builtin_elementwise_min(a.z, a.w)); }
__device__ __forceinline__ unsigned lds_wait4(LAS unsigned char* a, unsigned n) {
  unsigned mn = 0;
  for (unsigned spin = 0; spin < (SPIN_CAP << 4); ++spin) {
    const v4u w = *(volatile LAS v4u*)a;
    mn = __builtin_amdgcn_readfirstlane(umin4(w));
    if (mn >= n) break;
    __builtin_amdgcn_s_sleep(1);
  }
  asm volatile("" ::: "memory");
  return mn;
}
__device__ __forceinline__ void lds_wait1(LAS unsigned char* a) {
  for (unsigned spin = 0; spin < (SPIN_CAP << 4); ++spin) {
    if (__builtin_amdgcn_readfirstlane(*(volatile LAS unsigned*)a)) break;
    __builtin_amdgcn_s_sleep(1);
  }
  asm volatile("" ::: "memory");
}
template <int CTRL, int RM = 0xf>
__device__ __forceinline__ float dpp0(float v) { return u2f(__builtin_amdgcn_update_dpp(0, f2u(v), CTRL, RM, 0xf, true)); }
__device__ __forceinline__ float wave_sum(float v) {
  v += dppm<0x121>(v); v += dppm<0x122>(v); v += dppm<0x124>(v); v += dppm<0x128>(v);
  return xsum32(xsum16(v));
}

#define STAMP(k) do { } while (0)

#define SENTINEL(SENT, THR, SLP)                                                                             \
  do {                                                                                                       \
    _Pragma("nounroll") for (unsigned spin = 0; spin < SPIN_CAP; ++spin) {                                   \
      const unsigned sr = __builtin_amdgcn_raw_buffer_load_b32(wsr, (SENT) + 4, 0, AUX_SC1);                 \
      if (__builtin_popcountll(__ballot(sr == tag)) >= (THR)) break;                                         \
      __builtin_amdgcn_s_sleep(SLP);                                                                         \
    }                                                                                                        \
  } while (0)
#define GATHER(N, qr, ADDR)                                                                                  \
  do {                                                                                                       \
    unsigned done = 0;                                                                                       \
    _Pragma("nounroll") for (unsigned spin = 0; spin < SPIN_CAP; ++spin) {                                   \
      _Pragma("unroll") for (int i = 0; i < N; ++i)                                                          \
        if (!((done >> i) & 1)) qr[i] = __builtin_amdgcn_raw_buffer_load_b128(wsr, (ADDR), 0, AUX_SC1);      \
      _Pragma("unroll") for (int i = 0; i < N; ++i)                                                          \
        if (!((done >> i) & 1)) { if (rec_ok(qr[i], tag)) done |= 1u << i; }                                 \
      if (done == (1u << N) - 1u) break;                                                                     \
    }                                                                                                        \
  } while (0)

__global__ void __launch_bounds__(NTHR, 2) fused_kernel(Params p) {
  extern __shared__ __attribute__((aligned(16))) unsigned char lds_raw[];
  LAS unsigned char* L = (LAS unsigned char*)lds_raw;
  const int tid = threadIdx.x, lane = tid & 63;
  const int wave = __builtin_amdgcn_readfirstlane(tid >> 6);
  const int wg = blockIdx.x, xg = wg & 7, r = wg >> 3;
  const int bA = xg, chunk = r;
  const int hA = xg * 4 + (r >> 3), sl = r & 7;
  const int hB = ((xg + 1) & 7) * 4 + (r >> 3);
  const int ft0 = (xg * 43) >> 1;
  const bool ffn2 = r < (((xg + 1) * 43) >> 1) - ft0;
  const int ft = ft0 + r;
  const bool ffn1 = r < 22;
  const int ntask = hB < 12 ? 6 : 5, task0 = hB < 12 ? 6 * hB : 12 + 5 * hB;
  const __amdgpu_buffer_rsrc_t wsr = __builtin_amdgcn_make_buffer_rsrc((void*)p.ws, 0, WS_END, 0x00020000);
  const int l16 = lane & 15, g4 = lane >> 4;
  const int l32 = lane & 31, hf = lane >> 5;
  const unsigned r8 = lane >> 3, q8 = lane & 7;
  STAMP(0);
  const unsigned tag = *(const __attribute__((address_space(4))) unsigned*)(p.ws + WS_EPOCH) + 1u;
  asm volatile("" :: "s"(tag));
  v4u wo2[2], wv2[2], wd1;
  auto tail_loads = [&]() {
    const u16* so = p.wo + ((size_t)hB * HD + wave * 16 + r8) * DM + sl * 64 + q8 * 8;
    wo2[0] = WLOAD((const GAS v4u*)so);
    wo2[1] = WLOAD((const GAS v4u*)(so + 8 * DM));
    const u16* sv = p.wuv + ((size_t)(sl * 64 + wave * 8 + r8) * NH + hA) * HD + q8 * 8;
    wv2[0] = WLOAD((const GAS v4u*)sv);
    wv2[1] = WLOAD((const GAS v4u*)(sv + 64));
    const int fr = min(wave * 8 + (int)r8, ntask * 8 - 1);
    wd1 = WLOAD((const GAS v4u*)(p.wd + (size_t)(task0 * 8 + fr) * DM + sl * 64 + q8 * 8));
  };
  f32x16 sacc;
  float m = 0.f;

  if (wave >= 4) {
    const int v = wave - 4;
    const u16* src0 = p.ckv + ((size_t)bA * SK + chunk * 128 + 4 * v + g4) * DC + ((l16 ^ ((g4 << 2) | v)) * 8);
    BAR();
    if (LDELAY) __builtin_amdgcn_s_sleep(LDELAY);
#pragma unroll
    for (int n = 0; n < 32; ++n) {
      if (n == 24) {
        for (unsigned spin = 0; spin < (SPIN_CAP << 4); ++spin) {
          const v2u w = *(volatile LAS v2u*)(L + L_FLG);
          if (__builtin_amdgcn_readfirstlane(w.x & w.y)) break;
          __builtin_amdgcn_s_sleep(1);
        }
        asm volatile("" ::: "memory");
      }
      GLDS(src0 + (size_t)(n & 7) * 16 * DC + (n >> 3) * 128, L + L_CKV + (n >> 3) * 32768 + ((n & 7) * 4 + v) * 1024, AUX_NT);
      if (n >= WIN - 1 && n < 31) {
        __builtin_amdgcn_s_waitcnt(0x0f70 | ((WIN - 1) & 15) | (((WIN - 1) >> 4) << 14));
        if (((n - WIN + 2) & 7) == 0) { asm volatile("" ::: "memory"); *(volatile LAS unsigned*)(L + L_PROG + v * 4) = (n - WIN + 2) >> 3; }
      }
    }
    STAMP(1);
    tail_loads();
    VMWAIT(5);
    *(volatile LAS unsigned*)(L + L_PROG + v * 4) = 4;
    STAMP(2);
    if (wave == 7 && ffn2) {
      v4u gr[4];
      GATHER(4, gr, WS_GU + (((2 * i + hf) * 176 + ft) * 32 + l32) * 16);
      float hm[2];
#pragma unroll
      for (int e = 0; e < 2; ++e) {
        const float gv = xsum32(u2f(e ? gr[0].z : gr[0].x) + u2f(e ? gr[1].z : gr[1].x));
        const float uv = xsum32(u2f(e ? gr[2].z : gr[2].x) + u2f(e ? gr[3].z : gr[3].x));
        const float yy = 0.7978845608028654f * (gv + 0.044715f * gv * gv * gv);
        const float th = 1.f - 2.f / (__expf(2.f * yy) + 1.f);
        hm[e] = 0.5f * gv * (1.f + th) * uv;
      }
      const unsigned a = pk2(hm[0], hm[1]), b = dppu<0xb1>(a);
      if (lane < 32 && !(lane & 1))
        __builtin_amdgcn_raw_buffer_store_b128((v4u){a, tag, b, tag}, wsr, WS_HM + (ft * 16 + (lane >> 1)) * 16, 0, AUX_SC1);
      STAMP(3);
    }
  } else {
    if (wave == 3) {
      v4u xv[8];
#pragma unroll
      for (int i = 0; i < 8; ++i) xv[i] = *(const GAS v4u*)(p.x + i * DM + lane * 8);
      if (lane < 4) {
        *(LAS unsigned*)(L + L_PROG + lane * 4) = 0; *(LAS unsigned*)(L + L_IMG + lane * 4) = 0;
        *(LAS unsigned*)(L + L_FLG + lane * 4) = (lane == 1 && !ffn1) ? 1 : 0; *(LAS unsigned*)(L + L_GO + lane * 4) = 0;
      }
      BAR();
#pragma unroll
      for (int i = 0; i < 8; ++i) {
        const float mean = wave_sum(dot_ones(xv[i].w, dot_ones(xv[i].z, dot_ones(xv[i].y, dot_ones(xv[i].x, 0.f))))) * (1.f / DM);
        float sq = 0.f;
#pragma unroll
        for (int j = 0; j < 4; ++j) { const unsigned w = xv[i][j]; const float a = bflo(w) - mean, b = bfhi(w) - mean; sq += a * a; sq += b * b; }
        const float var = wave_sum(sq) * (1.f / DM);
        if (lane == 0) *(LAS f32x2*)(L + L_STAT + i * 8) = (f32x2){mean, __builtin_amdgcn_rsqf(var + 1e-5f)};
      }
      LGKM0();
      if (lane == 0) *(volatile LAS unsigned*)(L + L_FLG + 12) = 1;
      STAMP(1);
      SENTINEL(WS_QA + ((bA * 32 + (l16 >> 1) * 4 + 3) * 128 + (6 + (l16 & 1)) * 16) * 16, 48, 4);
      if (lane == 0) *(volatile LAS unsigned*)(L + L_GO) = 1;
      STAMP(2);
    } else if (wave == 0 || (wave == 1 && ffn1)) {
      const unsigned st = L_STG + wave * 16384;
      const int kq = r & 3;
      const int t1 = xg * 22 + r, cg = (t1 % 88) >> 2, mat = t1 >= 88;
      const int kq1 = wave == 0 ? kq : (t1 & 3);
      if (wave == 1) { BAR(); if (FDELAY) __builtin_amdgcn_s_sleep(FDELAY); }
      {
        const unsigned pz = q8 ^ (((r8 & 3) << 1) | (r8 >> 2));
        const u16* src; int stride;
        if (wave == 0) { stride = NH * HD; src = p.wq + (size_t)(kq * 128 + r8) * stride + hA * HD + ((r >> 2) & 1) * 64 + pz * 8; }
        else { stride = FF; src = (mat ? p.wu : p.wg) + (size_t)(kq1 * 128 + r8) * stride + min(cg * 64 + (int)pz * 8, FF - 8); }
#pragma unroll
        for (int i = 0; i < 16; ++i) GLDS(src + (size_t)i * 8 * stride, L + st + i * 1024, AUX_NT);
      }
      v4u xr[4];
      {
        const u16* xs = p.x + (l16 & 7) * DM + kq1 * 128 + g4 * 8;
#pragma unroll
        for (int s = 0; s < 4; ++s) xr[s] = *(const GAS v4u*)(xs + s * 32);
      }
      if (wave == 0) BAR();
      if (l16 >= 8) {
#pragma unroll
        for (int s = 0; s < 4; ++s) xr[s] = (v4u){0x3f803f80u, 0x3f803f80u, 0x3f803f80u, 0x3f803f80u};
      }
      lds_wait1(L + L_FLG + 12);
      const f32x2 stt = *(const LAS f32x2*)(L + L_STAT + (l16 & 7) * 8);
      VMWAIT(0);
      STAMP(1);
      f32x4 acc[4];
      {
        const unsigned rq = l16 >> 2, pc = (l16 & 3) >> 1, ph = (l16 & 1) * 8;
        const unsigned a0 = st + (8 * g4 + rq) * 128 + ((pc ^ (rq << 1)) * 16) + ph;
        const unsigned a1 = st + (8 * g4 + rq + 4) * 128 + ((pc ^ (rq << 1) ^ 1) * 16) + ph;
        v4u af[16];
#pragma unroll
        for (int nt = 0; nt < 4; ++nt)
#pragma unroll
          for (int s = 0; s < 4; ++s) af[nt * 4 + s] = tr_read2(L + (a0 ^ (nt * 32)) + s * 4096, L + (a1 ^ (nt * 32)) + s * 4096);
#pragma unroll
        for (int nt = 0; nt < 4; ++nt) {
          acc[nt] = (f32x4){0.f, 0.f, 0.f, 0.f};
#pragma unroll
          for (int s = 0; s < 4; ++s) acc[nt] = mfma16(af[nt * 4 + s], xr[s], acc[nt]);
        }
        if (lane == 0) *(volatile LAS unsigned*)(L + L_FLG + wave * 4) = 1;
      }
#pragma unroll
      for (int nt = 0; nt < 4; ++nt)
#pragma unroll
        for (int j = 0; j < 4; ++j) acc[nt][j] = (acc[nt][j] - stt.x * dppm<0x158>(acc[nt][j])) * stt.y;
      if (wave == 0) {
#pragma unroll
        for (int np = 0; np < 2; ++np) {
          const unsigned a = pk2(acc[2 * np][0], acc[2 * np][1]), b = pk2(acc[2 * np][2], acc[2 * np][3]);
          const unsigned c = dppu<0x128>(pk2(acc[2 * np + 1][0], acc[2 * np + 1][1])), d = dppu<0x128>(pk2(acc[2 * np + 1][2], acc[2 * np + 1][3]));
          const unsigned u = ((r >> 2) & 1) * 4 + 2 * np + (l16 >> 3);
          __builtin_amdgcn_raw_buffer_store_b128((v4u){l16 < 8 ? a : c, tag, l16 < 8 ? b : d, tag}, wsr,
                                                 WS_QG + ((((hA * 4 + kq) * 4 + ((l16 & 7) >> 1)) * 8 + u) * 8 + (l16 & 1) * 4 + g4) * 16, 0, AUX_SC1);
        }
      } else {
#pragma unroll
        for (int nt = 0; nt < 4; ++nt) {
          const unsigned c = dppu<0x128>(f2u(acc[nt][2])), d = dppu<0x128>(f2u(acc[nt][3]));
          __builtin_amdgcn_raw_buffer_store_b128((v4u){l16 < 8 ? f2u(acc[nt][0]) : c, tag, l16 < 8 ? f2u(acc[nt][1]) : d, tag}, wsr,
                                                 WS_GU + ((((mat * 4 + kq1) * 176 + cg * 8 + 2 * nt + (g4 >> 1)) * 8 + (l16 & 7)) * 4 + 2 * (g4 & 1) + (l16 >> 3)) * 16, 0, AUX_SC1);
        }
      }
      STAMP(2);
    } else if (wave == 2) {
      v4u wf[16], qr[16];
      {
        const u16* src = p.wuk + ((size_t)(sl * 64 + g4) * NH + hB) * HD;
#pragma unroll
        for (int n = 0; n < 16; ++n) GLDS(src + (size_t)n * 4 * NH * HD + ((l16 ^ ((n * 4 + g4) & 15)) * 8), L + L_UK + n * 1024, AUX_NT);
      }
      BAR();
      VMWAIT(0);
      {
        const unsigned a0 = L_UK + l16 * 256 + (((g4 >> 1) ^ l16) * 16) + (g4 & 1) * 8;
#pragma unroll
        for (int ct = 0; ct < 4; ++ct)
#pragma unroll
          for (int s = 0; s < 4; ++s) {
            const v2u lo = *(const LAS v2u*)(L + (a0 ^ (s * 64)) + ct * 4096), hi = *(const LAS v2u*)(L + (a0 ^ (s * 64 + 32)) + ct * 4096);
            wf[ct * 4 + s] = (v4u){lo.x, lo.y, hi.x, hi.y};
          }
        LGKM0();
        if (lane == 0) *(volatile LAS unsigned*)(L + L_FLG + 8) = 1;
      }
      STAMP(12);
      SENTINEL(WS_QG + (((hB * 4 + (q8 & 3)) * 4) * 8 + (q8 >> 2) * 4) * 8 * 16, 16, 1);
      const unsigned qoff = WS_QG + ((((hB * 4 + (l16 >> 3)) * 4 + ((l16 & 7) >> 1)) * 8) * 8 + (l16 & 1) * 4 + g4) * 16;
      GATHER(16, qr, qoff + (i >> 3) * 8192 + (i & 7) * 128);
      STAMP(1);
      const float scale = 0.08838834764831845f * 1.4426950408889634f;
#pragma unroll
      for (int cp = 0; cp < 2; ++cp) {
        f32x4 a0 = {0.f, 0.f, 0.f, 0.f}, a1 = {0.f, 0.f, 0.f, 0.f};
#pragma unroll
        for (int k = 0; k < 8; ++k) {
          const v4u bq = {qr[2 * k].x, qr[2 * k].z, qr[2 * k + 1].x, qr[2 * k + 1].z};
          a0 = mfma16(wf[cp * 8 + (k & 3)], bq, a0); a1 = mfma16(wf[cp * 8 + 4 + (k & 3)], bq, a1);
        }
        float y[4];
#pragma unroll
        for (int j = 0; j < 4; ++j) {
          const float keep = l16 < 8 ? a0[j] : a1[j], give = l16 < 8 ? a1[j] : a0[j];
          y[j] = (keep + dppm<0x128>(give)) * scale;
        }
        __builtin_amdgcn_raw_buffer_store_b128((v4u){pk2(y[0], y[1]), tag, pk2(y[2], y[3]), tag}, wsr,
                                               WS_QA + (((l16 & 7) * 32 + hB) * 128 + sl * 16 + cp * 8 + (l16 >> 3) * 4 + g4) * 16, 0, AUX_SC1);
      }
      STAMP(2);
    } else {
      BAR();
    }
    v4u ql;
    {
      v4u qr[18];
      const unsigned qoff = WS_QA + ((bA * 32 + wave * 8) * 128 + lane) * 16;
      const unsigned qoff2 = WS_QA + ((bA * 32 + (lane & 1)) * 128 + 124 + hf * 2) * 16;
      lds_wait1(L + L_GO);
      GATHER(18, qr, (i < 16 ? qoff + i * 1024 : qoff2 + (i - 16) * 16));
      STAMP(3);
      lds_wait4(L + L_FLG, 1);
#pragma unroll
      for (int i = 0; i < 16; ++i) {
        const unsigned h = wave * 8 + (i >> 1);
        const unsigned chn = (i & 1) * 32 + (lane >> 1);
        if (!((i == 1 || i == 3) && wave == 0 && lane >= 60))
          *(LAS v2u*)(L + L_S + h * 1024 + (((chn & 48) | ((chn & 15) ^ swz(h))) * 16) + (lane & 1) * 8) = (v2u){qr[i].x, qr[i].z};
      }
      ql = (v4u){qr[16].x, qr[16].z, qr[17].x, qr[17].z};
      LGKM0();
      if (lane == 0) *(volatile LAS unsigned*)(L + L_IMG + wave * 4) = 1;
    }
    tail_loads();
    lds_wait4(L + L_IMG, 1);
    STAMP(4);
    {
#pragma unroll
      for (int i = 0; i < 16; ++i) sacc[i] = 0.f;
      const unsigned tz = (hf ^ swz(l32)) * 16;
      const unsigned qb = L_S + l32 * 1024 + tz;
      const unsigned kb = L_CKV + (wave * 32 + l32) * 256 + tz;
      v4u qf[2][8], kf[2][8];
      unsigned have = lds_wait4(L + L_PROG, 1);
#pragma unroll
      for (int s = 0; s < 8; ++s) { kf[0][s] = *(const LAS v4u*)(L + (kb ^ (s * 32))); qf[0][s] = *(const LAS v4u*)(L + (qb ^ (s * 32))); }
#pragma unroll
      for (int blk = 0; blk < 4; ++blk) {
        if (blk < 3) {
          if (have < (unsigned)blk + 2) {
#pragma unroll
            for (int s = 0; s < 8; ++s) sacc = mfma32(kf[blk & 1][s], qf[blk & 1][s], sacc);
            have = lds_wait4(L + L_PROG, blk + 2);
#pragma unroll
            for (int s = 0; s < 8; ++s) {
              kf[(blk + 1) & 1][s] = *(const LAS v4u*)(L + (kb ^ (s * 32)) + (blk + 1) * 32768);
              qf[(blk + 1) & 1][s] = *(const LAS v4u*)(L + (qb ^ (s * 32)) + (blk + 1) * 256);
            }
          } else {
#pragma unroll
            for (int s = 0; s < 8; ++s) {
              kf[(blk + 1) & 1][s] = *(const LAS v4u*)(L + (kb ^ (s * 32)) + (blk + 1) * 32768);
              qf[(blk + 1) & 1][s] = *(const LAS v4u*)(L + (qb ^ (s * 32)) + (blk + 1) * 256);
              sacc = mfma32(kf[blk & 1][s], qf[blk & 1][s], sacc);
            }
          }
          if (blk == 0 && lane == 0) *(volatile LAS unsigned*)(L + L_IMG + wave * 4) = 2;
        } else {
          if (l32 < 2) qf[1][7] = ql;
#pragma unroll
          for (int s = 0; s < 8; ++s) sacc = mfma32(kf[1][s], qf[1][s], sacc);
        }
      }
    }
    STAMP(5);
    float mx = fmaxf(fmaxf(fmaxf(sacc[0], sacc[1]), fmaxf(sacc[2], sacc[3])), fmaxf(fmaxf(sacc[4], sacc[5]), fmaxf(sacc[6], sacc[7])));
    mx = fmaxf(mx, fmaxf(fmaxf(fmaxf(sacc[8], sacc[9]), fmaxf(sacc[10], sacc[11])), fmaxf(fmaxf(sacc[12], sacc[13]), fmaxf(sacc[14], sacc[15]))));
    mx = xmax32(mx);
    lds_wait4(L + L_IMG, 2);
    if (lane < 32) *(LAS float*)(L + L_S + lane * 1024 + swz(lane) * 16 + wave * 4) = mx;
  }
  BAR();
  const unsigned zl = swz(l32);
  if (wave < 4) {
    const f32x4 a = *(const LAS f32x4*)(L + L_S + l32 * 1024 + zl * 16);
    m = fmaxf(fmaxf(a[0], a[1]), fmaxf(a[2], a[3]));
    float ls = 0.f;
#pragma unroll
    for (int q = 0; q < 4; ++q) {
      const unsigned p0 = pk2(__builtin_amdgcn_exp2f(sacc[4 * q] - m), __builtin_amdgcn_exp2f(sacc[4 * q + 1] - m));
      const unsigned p1 = pk2(__builtin_amdgcn_exp2f(sacc[4 * q + 2] - m), __builtin_amdgcn_exp2f(sacc[4 * q + 3] - m));
      *(LAS v2u*)(L + L_S + l32 * 1024 + 256 + (((wave * 4 + q) ^ zl) * 16) + hf * 8) = (v2u){p0, p1};
      ls = dot_ones(p1, dot_ones(p0, ls));
    }
    ls = xsum32(ls);
    if (lane < 32) *(LAS float*)(L + L_S + lane * 1024 + 512 + zl * 16 + wave * 4) = ls;
  }
  BAR();
  const unsigned wb = L_CKV + (wave >> 1) * 32768;
  const unsigned wq8 = (wave & 1) * 8;
  f32x16 o0, o1;
  float lsum;
  {
    v4u pf[8];
    {
      const unsigned pbs = L_S + l32 * 1024 + 256 + (hf ^ zl) * 16;
#pragma unroll
      for (int ks = 0; ks < 8; ++ks) pf[ks] = *(const LAS v4u*)(L + (pbs ^ (ks * 32)));
    }
#pragma unroll
    for (int i = 0; i < 16; ++i) { o0[i] = 0.f; o1[i] = 0.f; }
    const unsigned blk = (lane >> 4) & 1, q = (lane & 15) >> 2, pp = lane & 3;
    unsigned ta[2];
#pragma unroll
    for (int tt = 0; tt < 2; ++tt) {
      const unsigned row = 8 * hf + 4 * tt + q;
      ta[tt] = wb + row * 256 + (((wq8 + 2 * blk + (pp >> 1)) ^ swz(row)) * 16) + 8 * (pp & 1);
    }
#pragma unroll
    for (int ks = 0; ks < 8; ++ks) {
      o0 = mfma32(tr_read2(L + ta[0] + ks * 4096, L + ta[1] + ks * 4096), pf[ks], o0);
      o1 = mfma32(tr_read2(L + (ta[0] ^ 64) + ks * 4096, L + (ta[1] ^ 64) + ks * 4096), pf[ks], o1);
    }
    const f32x4 a = *(const LAS f32x4*)(L + L_S + l32 * 1024 + 512 + zl * 16);
    lsum = (a[0] + a[1]) + (a[2] + a[3]);
  }
  STAMP(6);
  const unsigned sc_e = wb + r8 * 256 + (((wq8 + q8) ^ swz(r8)) * 16);
  const unsigned sc_o = wb + (r8 + 8) * 256 + (((wq8 + q8) ^ swz(r8 + 8)) * 16);
  {
    const float inv = __builtin_amdgcn_rcpf(lsum);
    o0 *= inv; o1 *= inv;
    const unsigned wa = wb + l32 * 256 + (((wq8 + hf) ^ zl) * 16);
#pragma unroll
    for (int gp = 0; gp < 4; ++gp) {
      const f32x16& o = gp < 2 ? o0 : o1;
      const int b8 = (gp & 1) * 8;
      auto rx = __builtin_amdgcn_permlane32_swap(pk2(o[b8 + 0], o[b8 + 1]), pk2(o[b8 + 4], o[b8 + 5]), false, false);
      auto ry = __builtin_amdgcn_permlane32_swap(pk2(o[b8 + 2], o[b8 + 3]), pk2(o[b8 + 6], o[b8 + 7]), false, false);
      *(LAS v4u*)(L + (wa ^ (gp * 32))) = (v4u){rx[0], ry[0], rx[1], ry[1]};
    }
    LGKM0();
    const unsigned pbase = WS_PART + ((((bA * 32 + r8) * 8 + wave) * 32 + chunk) * 64 + q8 * 8) * 2;
#pragma unroll
    for (int i = 0; i < 4; ++i) {
      const v4u t = *(const LAS v4u*)(L + ((i & 1) ? sc_o : sc_e) + (i >> 1) * 4096);
      __builtin_amdgcn_raw_buffer_store_b128(t, wsr, pbase + i * (8 * 8 * 32 * 128), 0, AUX_SC1);
    }
    *(LAS v4u*)(L + sc_e + 8192) = wo2[0];
    *(LAS v4u*)(L + sc_o + 8192) = wo2[1];
    *(LAS v4u*)(L + sc_e + 12288) = wv2[0];
    *(LAS v4u*)(L + sc_o + 12288) = wv2[1];
    *(LAS v4u*)(L + sc_e + 16384) = wd1;
    VMWAIT(0);
    BAR();
    if (wave == 0 && lane < 32)
      __builtin_amdgcn_raw_buffer_store_b64((v2u){f2u(m + __builtin_amdgcn_logf(lsum)), tag}, wsr,
                                            WS_FL + ((((lane >> 2) * 8 + bA) * 32 + chunk) * 4 + (lane & 3)) * 8, 0, AUX_SC1);
    STAMP(7);
  }
  const unsigned pc = (l16 & 3) >> 1, ph = (l16 & 1) * 8, rq = l16 >> 2;
  v4u av[2], ao[4];
  f32x4 accE = {0.f, 0.f, 0.f, 0.f};
  {
    const unsigned i8 = (wave >> 2) * 8, pcs = (g4 & 1) * 8 + 2 * (wave & 3) + pc;
    const unsigned b0 = (g4 >> 1) * 32768 + ph;
    const unsigned v0 = b0 + (48 + i8 + rq) * 256 + ((pcs ^ swz(i8 + rq)) * 16), v1 = b0 + (52 + i8 + rq) * 256 + ((pcs ^ swz(i8 + rq + 4)) * 16);
    av[0] = tr_read2(L + v0, L + v1);
    av[1] = tr_read2(L + v0 + 65536, L + v1 + 65536);
    if (wave < 4) {
      v4u hr[4];
      const unsigned rin = 8 * (g4 & 1) + rq, pco = (g4 >> 1) * 8 + 2 * wave + pc;
      const unsigned o0a = (32 + rin) * 256 + ((pco ^ swz(rin)) * 16) + ph, o1a = (36 + rin) * 256 + ((pco ^ swz(rin + 4)) * 16) + ph;
#pragma unroll
      for (int ks = 0; ks < 4; ++ks) ao[ks] = tr_read2(L + o0a + ks * 32768, L + o1a + ks * 32768);
      const unsigned d0 = b0 + (64 + rq) * 256 + ((pcs ^ swz(rq)) * 16), d1 = b0 + (68 + rq) * 256 + ((pcs ^ swz(rq + 4)) * 16);
      const v4u ad0 = tr_read2(L + d0, L + d1), ad1 = tr_read2(L + d0 + 65536, L + d1 + 65536);
      const bool on = 4 + g4 < ntask;
      const unsigned h0 = WS_HM + ((task0 + g4) * 8 + (l16 & 7)) * 32, h1 = WS_HM + ((task0 + (on ? 4 + g4 : 0)) * 8 + (l16 & 7)) * 32;
      for (unsigned spin = 0; spin < SPIN_CAP; ++spin) {
        hr[0] = __builtin_amdgcn_raw_buffer_load_b128(wsr, h0, 0, AUX_SC1);
        hr[1] = __builtin_amdgcn_raw_buffer_load_b128(wsr, h0 + 16, 0, AUX_SC1);
        hr[2] = __builtin_amdgcn_raw_buffer_load_b128(wsr, h1, 0, AUX_SC1);
        hr[3] = __builtin_amdgcn_raw_buffer_load_b128(wsr, h1 + 16, 0, AUX_SC1);
        if (__all((hr[0].y == tag) & (hr[0].w == tag) & (hr[1].y == tag) & (hr[1].w == tag) & (hr[2].y == tag) & (hr[2].w == tag) & (hr[3].y == tag) & (hr[3].w == tag))) break;
        __builtin_amdgcn_s_sleep(1);
      }
      accE = mfma16(ad0, (v4u){hr[0].x, hr[0].z, hr[1].x, hr[1].z}, accE);
      accE = mfma16(ad1, on ? (v4u){hr[2].x, hr[2].z, hr[3].x, hr[3].z} : (v4u){0u, 0u, 0u, 0u}, accE);
    }
  }
  {
    v2u fr;
    const unsigned fo = WS_FL + (((xg * 8 + wave) * 32 + l32) * 4 + (hA & 3)) * 8;
    for (unsigned spin = 0; spin < SPIN_CAP; ++spin) {
      fr = __builtin_amdgcn_raw_buffer_load_b64(wsr, fo, 0, AUX_SC1);
      if (__all(fr.y == tag)) break;
      __builtin_amdgcn_s_sleep(1);
    }
    asm volatile("" ::: "memory");
    STAMP(8);
    v2u pr[8];
    const unsigned po = WS_PART + ((((wave * 32 + hA) * 8 + sl) * 32 + g4) * 64 + l16 * 4) * 2;
#pragma unroll
    for (int u = 0; u < 8; ++u) pr[u] = __builtin_amdgcn_raw_buffer_load_b64(wsr, po + u * 512, 0, AUX_SC1);
    const float lse = u2f(fr.x);
    float M = lse;
    M = fmaxf(M, dppm<0x121>(M)); M = fmaxf(M, dppm<0x122>(M)); M = fmaxf(M, dppm<0x124>(M)); M = fmaxf(M, dppm<0x128>(M));
    M = xmax16(M);
    const float wj = bflo(pk2(__builtin_amdgcn_exp2f(lse - M), 0.f));
    float Ls = wj;
    Ls += dppm<0x121>(Ls); Ls += dppm<0x122>(Ls); Ls += dppm<0x124>(Ls); Ls += dppm<0x128>(Ls);
    Ls = xsum16(Ls);
    const float inv = __builtin_amdgcn_rcpf(Ls);
    const unsigned e4 = l16 & 3;
    const unsigned sh = (e4 & 1) ? 0u : 16u, k0 = e4 < 2 ? ~0u : 0u;
    unsigned wa[16];
#pragma unroll
    for (int u = 0; u < 8; ++u) {
      const unsigned w = (unsigned)__builtin_amdgcn_ds_bpermute((g4 + 4 * u) * 4, f2u(wj)) >> sh;
      wa[2 * u] = w & k0; wa[2 * u + 1] = w & ~k0;
    }
    f32x4 acc = {0.f, 0.f, 0.f, 0.f};
#pragma unroll
    for (int u = 0; u < 4; ++u)
      acc = mfma16((v4u){wa[4 * u], wa[4 * u + 1], wa[4 * u + 2], wa[4 * u + 3]}, (v4u){pr[2 * u].x, pr[2 * u].y, pr[2 * u + 1].x, pr[2 * u + 1].y}, acc);
    if (lane < 16) *(LAS v2u*)(L + L_CL + wave * 1040 + lane * 8) = (v2u){pk2(acc[0] * inv, acc[1] * inv), pk2(acc[2] * inv, acc[3] * inv)};
    BAR();
  }
  const unsigned one = (l16 & 1) ? 0x3f800000u : 0x00003f80u;
  const unsigned s0 = (l16 & 2) ? 0u : one, s1 = (l16 & 2) ? one : 0u;
  {
    const v4u b0 = *(const LAS v4u*)(L + L_CL + (l16 & 7) * 1040 + g4 * 16), b1 = *(const LAS v4u*)(L + L_CL + (l16 & 7) * 1040 + 64 + g4 * 16);
    f32x4 acc = {0.f, 0.f, 0.f, 0.f};
    acc = mfma16(av[0], b0, acc);
    acc = mfma16(av[1], b1, acc);
    if (l16 < 8)
      __builtin_amdgcn_raw_buffer_store_b128((v4u){pk2(acc[0], acc[1]), tag, pk2(acc[2], acc[3]), tag}, wsr,
                                             WS_CX + ((((hA * 8 + wave) * 8 + sl) * 8 + l16) * 4 + g4) * 16, 0, AUX_SC1);
    STAMP(9);
    v4u cr[4];
    const unsigned co = WS_CX + ((hB * 8 + wave) * 256 + lane) * 16;
    for (unsigned spin = 0; spin < SPIN_CAP; ++spin) {
#pragma unroll
      for (int i = 0; i < 4; ++i) cr[i] = __builtin_amdgcn_raw_buffer_load_b128(wsr, co + i * 1024, 0, AUX_SC1);
      if (__all((cr[0].y == tag) & (cr[0].w == tag) & (cr[1].y == tag) & (cr[1].w == tag) & (cr[2].y == tag) & (cr[2].w == tag) & (cr[3].y == tag) & (cr[3].w == tag))) break;
      __builtin_amdgcn_s_sleep(1);
    }
    STAMP(10);
    {
      const bool on = ((l16 >> 2) & 1) == (g4 & 1);
      const unsigned t0 = on ? s0 : 0u, t1 = on ? s1 : 0u;
      f32x4 a = {0.f, 0.f, 0.f, 0.f};
      a = mfma16((v4u){t0, t1, t0, t1}, (v4u){cr[0].x, cr[0].z, cr[1].x, cr[1].z}, a);
      a = mfma16((v4u){t0, t1, t0, t1}, (v4u){cr[2].x, cr[2].z, cr[3].x, cr[3].z}, a);
      if (lane < 32) *(LAS v2u*)(L + L_CX + (lane >> 2) * 272 + wave * 32 + (lane & 3) * 8) = (v2u){pk2(a[0], a[1]), pk2(a[2], a[3])};
    }
    BAR();
  }
  if (wave < 4) {
#pragma unroll
    for (int ks = 0; ks < 4; ++ks) accE = mfma16(ao[ks], *(const LAS v4u*)(L + L_CX + (l16 & 7) * 272 + ks * 64 + g4 * 16), accE);
    if (l16 < 8)
      __builtin_amdgcn_raw_buffer_store_b128((v4u){pk2(accE[0], accE[1]), tag, pk2(accE[2], accE[3]), tag}, wsr,
                                             WS_OG + ((((sl * 4 + wave) * 32 + hB) * 8 + l16) * 4 + g4) * 16, 0, AUX_SC1);
    STAMP(11);
  } else if (wave == 4) {
    const unsigned oo = WS_OG + ((((wg & 31) * 32 + (lane >> 2)) * 8 + (wg >> 5)) * 4 + (lane & 3)) * 16;
    const v2u xw = *(const GAS v2u*)(p.x + wg * 16 + (lane & 3) * 4);
    v4u r0, r1;
    for (unsigned spin = 0; spin < SPIN_CAP; ++spin) {
      r0 = __builtin_amdgcn_raw_buffer_load_b128(wsr, oo, 0, AUX_SC1);
      r1 = __builtin_amdgcn_raw_buffer_load_b128(wsr, oo + 16 * 512, 0, AUX_SC1);
      if (__all((r0.y == tag) & (r0.w == tag) & (r1.y == tag) & (r1.w == tag))) break;
      __builtin_amdgcn_s_sleep(1);
    }
    f32x4 acc = {0.f, 0.f, 0.f, 0.f};
    acc = mfma16((v4u){s0, s1, s0, s1}, (v4u){r0.x, r0.z, r1.x, r1.z}, acc);
#pragma unroll
    for (int j = 0; j < 4; ++j) { acc[j] += dppm<0x124>(acc[j]); acc[j] += dppm<0x128>(acc[j]); }
    if (lane < 4)
      *(GAS v2u*)(p.out + wg * 16 + lane * 4) = (v2u){pk2(bflo(xw.x) + acc[0], bfhi(xw.x) + acc[1]), pk2(bflo(xw.y) + acc[2], bfhi(xw.y) + acc[3])};
    if (wg == 0 && lane == 0) __builtin_amdgcn_raw_buffer_store_b32(tag, wsr, WS_EPOCH, 0, AUX_SC1);
    STAMP(11);
  }
}

extern "C" void kernel_launch(void* const* d_in, const int* in_sizes, int n_in, void* d_out, int out_size, void* d_ws, size_t ws_size,
                              hipStream_t stream) {
  Params p;
  p.x = (const u16*)d_in[0]; p.ckv = (const u16*)d_in[1]; p.wq = (const u16*)d_in[2]; p.wuk = (const u16*)d_in[3];
  p.wuv = (const u16*)d_in[4]; p.wo = (const u16*)d_in[5]; p.wg = (const u16*)d_in[7]; p.wu = (const u16*)d_in[8]; p.wd = (const u16*)d_in[9];
  p.out = (u16*)d_out; p.ws = (unsigned char*)d_ws;
  (void)hipFuncSetAttribute((const void*)fused_kernel, hipFuncAttributeMaxDynamicSharedMemorySize, LDS_BYTES);
  fused_kernel<<<NWG, NTHR, LDS_BYTES, stream>>>(p);
}
```

```cpp
#include <hip/hip_runtime.h>
#include <hip/hip_bf16.h>
#include <stdint.h>

#define STAMPS 0
#define CODE_LINES 224
#define PRETLB 1
#define WIN 6
#define LDELAY 0
#define FDELAY 40

typedef unsigned v4u __attribute__((ext_vector_type(4)));
typedef unsigned v2u __attribute__((ext_vector_type(2)));
typedef float f32x2 __attribute__((ext_vector_type(2)));
typedef float f32x4 __attribute__((ext_vector_type(4)));
typedef float f32x16 __attribute__((ext_vector_type(16)));
typedef short bf16x8 __attribute__((ext_vector_type(8)));
typedef short s16x4 __attribute__((ext_vector_type(4)));
typedef __bf16 bf16x2 __attribute__((ext_vector_type(2)));
typedef unsigned short u16;
#define LAS __attribute__((address_space(3)))
#define GAS __attribute__((address_space(1)))

constexpr int NB = 8, NH = 32, SK = 4096, HD = 128, DM = 512, DC = 512, FF = 1376;
constexpr int NWG = 256, NTHR = 512;
constexpr unsigned WS_EPOCH = 0;
constexpr unsigned WS_QG = 4096;
constexpr unsigned WS_QA = WS_QG + 32 * 4 * 256 * 16;
constexpr unsigned WS_GU = WS_QA + 8 * 32 * 128 * 16;
constexpr unsigned WS_HM = WS_GU + 8 * 176 * 32 * 16;
constexpr unsigned WS_FL = WS_HM + 176 * 8 * 2 * 16;
constexpr unsigned WS_CX = WS_FL + 8 * 8 * 32 * 4 * 8;
constexpr unsigned WS_OG = WS_CX + 32 * 8 * 8 * 8 * 4 * 16;
constexpr unsigned WS_PART = WS_OG + 32 * 32 * 8 * 4 * 16;
constexpr unsigned WS_END = WS_PART + 8 * 32 * 32 * 512 * 2;
constexpr unsigned WS_STAMP = 32u << 20;
constexpr unsigned L_CKV = 0;
constexpr unsigned L_STG = 96 * 1024;
constexpr unsigned L_S = 128 * 1024;
constexpr unsigned L_PROG = L_S + 63 * 16;
constexpr unsigned L_IMG = L_S + 62 * 16;
constexpr unsigned L_FLG = L_S + 1024 + (63 ^ 4) * 16;
constexpr unsigned L_GO = L_S + 1024 + (62 ^ 4) * 16;
constexpr unsigned L_STAT = L_S + 4096;
constexpr unsigned L_UK = L_S + 16384;
constexpr unsigned L_CL = L_S + 512;
constexpr unsigned L_CX = L_S + 16384;
constexpr unsigned LDS_BYTES = 160 * 1024;
constexpr int AUX_NT = 2, AUX_SC1 = 16;
constexpr unsigned SPIN_CAP = 1u << 18;

struct Params {
  const u16 *x, *ckv, *wq, *wuk, *wuv, *wo, *wg, *wu, *wd;
  u16* out;
  unsigned char* ws;
};

__device__ __forceinline__ float u2f(unsigned u) { return __uint_as_float(u); }
__device__ __forceinline__ unsigned f2u(float f) { return __float_as_uint(f); }
__device__ __forceinline__ float bflo(unsigned w) { return u2f(w << 16); }
__device__ __forceinline__ float bfhi(unsigned w) { return u2f(w & 0xffff0000u); }
__device__ __forceinline__ unsigned pk2(float lo, float hi) {
  f32x2 f = {lo, hi};
  return __builtin_bit_cast(unsigned, __builtin_convertvector(f, bf16x2));
}
__device__ __forceinline__ float dot_ones(unsigned a, float c) {
  return __builtin_amdgcn_fdot2_f32_bf16(__builtin_bit_cast(bf16x2, a), __builtin_bit_cast(bf16x2, 0x3f803f80u), c, false);
}
__device__ __forceinline__ f32x4 mfma16(v4u a, v4u b, f32x4 c) {
  return __builtin_amdgcn_mfma_f32_16x16x32_bf16(__builtin_bit_cast(bf16x8, a), __builtin_bit_cast(bf16x8, b), c, 0, 0, 0);
}
__device__ __forceinline__ f32x16 mfma32(v4u a, v4u b, f32x16 c) {
  return __builtin_amdgcn_mfma_f32_32x32x16_bf16(__builtin_bit_cast(bf16x8, a), __builtin_bit_cast(bf16x8, b), c, 0, 0, 0);
}
__device__ __forceinline__ unsigned swz(unsigned row) { return ((row & 3) << 2) | ((row >> 2) & 3); }
#define BAR() do { asm volatile("s_waitcnt lgkmcnt(0)" ::: "memory"); __builtin_amdgcn_s_barrier(); asm volatile("" ::: "memory"); } while (0)
#define VMWAIT_(n) asm volatile("s_waitcnt vmcnt(" #n ")" ::: "memory")
#define VMWAIT(n) VMWAIT_(n)
#define LGKM0() asm volatile("s_waitcnt lgkmcnt(0)" ::: "memory")
#define GLDS(src, dst, aux) __builtin_amdgcn_global_load_lds((const GAS unsigned*)(src), (LAS unsigned*)(dst), 16, 0, (aux))
#define WLOAD(p) __builtin_nontemporal_load(p)
__device__ __forceinline__ v2u tr_read(LAS unsigned char* a) {
  return __builtin_bit_cast(v2u, __builtin_amdgcn_ds_read_tr16_b64_v4i16((LAS s16x4*)a));
}
__device__ __forceinline__ v4u tr_read2(LAS unsigned char* a, LAS unsigned char* b) {
  const v2u x = tr_read(a), y = tr_read(b);
  return (v4u){x.x, x.y, y.x, y.y};
}
template <int CTRL>
__device__ __forceinline__ unsigned dppu(unsigned v) { return __builtin_amdgcn_update_dpp(v, v, CTRL, 0xf, 0xf, false); }
template <int CTRL>
__device__ __forceinline__ float dppm(float v) { return u2f(dppu<CTRL>(f2u(v))); }
__device__ __forceinline__ float xsum16(float v) {
  auto s = __builtin_amdgcn_permlane16_swap(f2u(v), f2u(v), false, false);
  return u2f(s[0]) + u2f(s[1]);
}
__device__ __forceinline__ float xmax16(float v) {
  auto s = __builtin_amdgcn_permlane16_swap(f2u(v), f2u(v), false, false);
  return fmaxf(u2f(s[0]), u2f(s[1]));
}
__device__ __forceinline__ float xsum32(float v) {
  auto s = __builtin_amdgcn_permlane32_swap(f2u(v), f2u(v), false, false);
  return u2f(s[0]) + u2f(s[1]);
}
__device__ __forceinline__ float xmax32(float v) {
  auto s = __builtin_amdgcn_permlane32_swap(f2u(v), f2u(v), false, false);
  return fmaxf(u2f(s[0]), u2f(s[1]));
}
__device__ __forceinline__ bool rec_ok(v4u r, unsigned tag) { return __all((r.y == tag) & (r.w == tag)); }
__device__ __forceinline__ unsigned umin4(v4u a) { return __builtin_elementwise_min(__builtin_elementwise_min(a.x, a.y), __builtin_elementwise_min(a.z, a.w)); }
__device__ __forceinline__ unsigned lds_wait4(LAS unsigned char* a, unsigned n) {
  unsigned mn = 0;
  for (unsigned spin = 0; spin < (SPIN_CAP << 4); ++spin) {
    const v4u w = *(volatile LAS v4u*)a;
    mn = __builtin_amdgcn_readfirstlane(umin4(w));
    if (mn >= n) break;
    __builtin_amdgcn_s_sleep(1);
  }
  asm volatile("" ::: "memory");
  return mn;
}
__device__ __forceinline__ void lds_wait1(LAS unsigned char* a) {
  for (unsigned spin = 0; spin < (SPIN_CAP << 4); ++spin) {
    if (__builtin_amdgcn_readfirstlane(*(volatile LAS unsigned*)a)) break;
    __builtin_amdgcn_s_sleep(1);
  }
  asm volatile("" ::: "memory");
}
template <int CTRL, int RM = 0xf>
__device__ __forceinline__ float dpp0(float v) { return u2f(__builtin_amdgcn_update_dpp(0, f2u(v), CTRL, RM, 0xf, true)); }
__device__ __forceinline__ float wave_sum(float v) {
  v += dppm<0x121>(v); v += dppm<0x122>(v); v += dppm<0x124>(v); v += dppm<0x128>(v);
  return xsum32(xsum16(v));
}

#define STAMP(k) do { } while (0)

#define SENTINEL(SENT, THR, SLP)                                                                             \
  do {                                                                                                       \
    _Pragma("nounroll") for (unsigned spin = 0; spin < SPIN_CAP; ++spin) {                                   \
      const unsigned sr = __builtin_amdgcn_raw_buffer_load_b32(wsr, (SENT) + 4, 0, AUX_SC1);                 \
      if (__builtin_popcountll(__ballot(sr == tag)) >= (THR)) break;                                         \
      __builtin_amdgcn_s_sleep(SLP);                                                                         \
    }                                                                                                        \
  } while (0)
#define GATHER(N, qr, ADDR)                                                                                  \
  do {                                                                                                       \
    unsigned done = 0;                                                                                       \
    _Pragma("nounroll") for (unsigned spin = 0; spin < SPIN_CAP; ++spin) {                                   \
      _Pragma("unroll") for (int i = 0; i < N; ++i)                                                          \
        if (!((done >> i) & 1)) qr[i] = __builtin_amdgcn_raw_buffer_load_b128(wsr, (ADDR), 0, AUX_SC1);      \
      _Pragma("unroll") for (int i = 0; i < N; ++i)                                                          \
        if (!((done >> i) & 1)) { if (rec_ok(qr[i], tag)) done |= 1u << i; }                                 \
      if (done == (1u << N) - 1u) break;                                                                     \
    }                                                                                                        \
  } while (0)

__global__ void __launch_bounds__(NTHR, 2) fused_kernel(Params p) {
  extern __shared__ __attribute__((aligned(16))) unsigned char lds_raw[];
  LAS unsigned char* L = (LAS unsigned char*)lds_raw;
  const int tid = threadIdx.x, lane = tid & 63;
  const int wave = __builtin_amdgcn_readfirstlane(tid >> 6);
  const int wg = blockIdx.x, xg = wg & 7, r = wg >> 3;
  const int bA = xg, chunk = r;
  const int hA = xg * 4 + (r >> 3), sl = r & 7;
  const int hB = ((xg + 1) & 7) * 4 + (r >> 3);
  const int ft0 = (xg * 43) >> 1;
  const bool ffn2 = r < (((xg + 1) * 43) >> 1) - ft0;
  const int ft = ft0 + r;
  const bool ffn1 = r < 22;
  const int ntask = hB < 12 ? 6 : 5, task0 = hB < 12 ? 6 * hB : 12 + 5 * hB;
  const __amdgpu_buffer_rsrc_t wsr = __builtin_amdgcn_make_buffer_rsrc((void*)p.ws, 0, WS_END, 0x00020000);
  const int l16 = lane & 15, g4 = lane >> 4;
  const int l32 = lane & 31, hf = lane >> 5;
  const unsigned r8 = lane >> 3, q8 = lane & 7;
  STAMP(0);
  unsigned long long pc0;
  asm volatile("s_getpc_b64 %0" : "=s"(pc0));
  const unsigned tag = *(const __attribute__((address_space(4))) unsigned*)(p.ws + WS_EPOCH) + 1u;
  asm volatile("" :: "s"(tag));
  v4u wo2[2], wv2[2], wd1;
  auto tail_loads = [&]() {
    const u16* so = p.wo + ((size_t)hB * HD + wave * 16 + r8) * DM + sl * 64 + q8 * 8;
    wo2[0] = WLOAD((const GAS v4u*)so);
    wo2[1] = WLOAD((const GAS v4u*)(so + 8 * DM));
    const u16* sv = p.wuv + ((size_t)(sl * 64 + wave * 8 + r8) * NH + hA) * HD + q8 * 8;
    wv2[0] = WLOAD((const GAS v4u*)sv);
    wv2[1] = WLOAD((const GAS v4u*)(sv + 64));
    const int fr = min(wave * 8 + (int)r8, ntask * 8 - 1);
    wd1 = WLOAD((const GAS v4u*)(p.wd + (size_t)(task0 * 8 + fr) * DM + sl * 64 + q8 * 8));
  };
  f32x16 sacc;
  float m = 0.f;

  if (wave >= 4) {
    const int v = wave - 4;
    const u16* src0 = p.ckv + ((size_t)bA * SK + chunk * 128 + 4 * v + g4) * DC + ((l16 ^ ((g4 << 2) | v)) * 8);
    BAR();
    if (LDELAY) __builtin_amdgcn_s_sleep(LDELAY);
#pragma unroll
    for (int n = 0; n < 32; ++n) {
      if (n == 24) {
        for (unsigned spin = 0; spin < (SPIN_CAP << 4); ++spin) {
          const v2u w = *(volatile LAS v2u*)(L + L_FLG);
          if (__builtin_amdgcn_readfirstlane(w.x & w.y)) break;
          __builtin_amdgcn_s_sleep(1);
        }
        asm volatile("" ::: "memory");
      }
      GLDS(src0 + (size_t)(n & 7) * 16 * DC + (n >> 3) * 128, L + L_CKV + (n >> 3) * 32768 + ((n & 7) * 4 + v) * 1024, AUX_NT);
      if (n >= WIN - 1 && n < 31) {
        __builtin_amdgcn_s_waitcnt(0x0f70 | ((WIN - 1) & 15) | (((WIN - 1) >> 4) << 14));
        if (((n - WIN + 2) & 7) == 0) { asm volatile("" ::: "memory"); *(volatile LAS unsigned*)(L + L_PROG + v * 4) = (n - WIN + 2) >> 3; }
      }
    }
    STAMP(1);
    tail_loads();
    VMWAIT(5);
    *(volatile LAS unsigned*)(L + L_PROG + v * 4) = 4;
    STAMP(2);
    if (wave == 7 && ffn2) {
      v4u gr[4];
      GATHER(4, gr, WS_GU + (((2 * i + hf) * 176 + ft) * 32 + l32) * 16);
      float hm[2];
#pragma unroll
      for (int e = 0; e < 2; ++e) {
        const float gv = xsum32(u2f(e ? gr[0].z : gr[0].x) + u2f(e ? gr[1].z : gr[1].x));
        const float uv = xsum32(u2f(e ? gr[2].z : gr[2].x) + u2f(e ? gr[3].z : gr[3].x));
        const float yy = 0.7978845608028654f * (gv + 0.044715f * gv * gv * gv);
        const float th = 1.f - 2.f / (__expf(2.f * yy) + 1.f);
        hm[e] = 0.5f * gv * (1.f + th) * uv;
      }
      const unsigned a = pk2(hm[0], hm[1]), b = dppu<0xb1>(a);
      if (lane < 32 && !(lane & 1))
        __builtin_amdgcn_raw_buffer_store_b128((v4u){a, tag, b, tag}, wsr, WS_HM + (ft * 16 + (lane >> 1)) * 16, 0, AUX_SC1);
      STAMP(3);
    }
  } else {
    if (wave == 3) {
      v4u xv[8];
#pragma unroll
      for (int i = 0; i < 8; ++i) xv[i] = *(const GAS v4u*)(p.x + i * DM + lane * 8);
      unsigned pf0;
      {
        const unsigned ln = min((unsigned)((r & 3) * 64 + lane), (unsigned)CODE_LINES - 1u);
        pf0 = *(const GAS unsigned*)((pc0 & ~127ull) + ln * 128);
      }
      unsigned pf1 = 0;
      {
        const unsigned char* a;
        if (lane < 6) a = p.ws + lane * (2u << 20) + 1024;
        else if (lane < 8) a = (const unsigned char*)p.wuv + (lane - 6) * (2u << 20);
        else if (lane < 10) a = (const unsigned char*)p.wo + (lane - 8) * (2u << 20);
        else if (lane < 11) a = (const unsigned char*)p.wd;
        else a = (const unsigned char*)p.out;
        if (lane < 12) pf1 = *(const GAS unsigned*)a;
      }
      if (lane < 4) {
        *(LAS unsigned*)(L + L_PROG + lane * 4) = 0; *(LAS unsigned*)(L + L_IMG + lane * 4) = 0;
        *(LAS unsigned*)(L + L_FLG + lane * 4) = (lane == 1 && !ffn1) ? 1 : 0; *(LAS unsigned*)(L + L_GO + lane * 4) = 0;
      }
      BAR();
#pragma unroll
      for (int i = 0; i < 8; ++i) {
        const float mean = wave_sum(dot_ones(xv[i].w, dot_ones(xv[i].z, dot_ones(xv[i].y, dot_ones(xv[i].x, 0.f))))) * (1.f / DM);
        float sq = 0.f;
#pragma unroll
        for (int j = 0; j < 4; ++j) { const unsigned w = xv[i][j]; const float a = bflo(w) - mean, b = bfhi(w) - mean; sq += a * a; sq += b * b; }
        const float var = wave_sum(sq) * (1.f / DM);
        if (lane == 0) *(LAS f32x2*)(L + L_STAT + i * 8) = (f32x2){mean, __builtin_amdgcn_rsqf(var + 1e-5f)};
      }
      LGKM0();
      if (lane == 0) *(volatile LAS unsigned*)(L + L_FLG + 12) = 1;
      STAMP(1);
      asm volatile("" :: "v"(pf0));
      asm volatile("" :: "v"(pf1));
      SENTINEL(WS_QA + ((bA * 32 + (l16 >> 1) * 4 + 3) * 128 + (6 + (l16 & 1)) * 16) * 16, 48, 4);
      if (lane == 0) *(volatile LAS unsigned*)(L + L_GO) = 1;
      STAMP(2);
    } else if (wave == 0 || (wave == 1 && ffn1)) {
      const unsigned st = L_STG + wave * 16384;
      const int kq = r & 3;
      const int t1 = xg * 22 + r, cg = (t1 % 88) >> 2, mat = t1 >= 88;
      const int kq1 = wave == 0 ? kq : (t1 & 3);
      if (wave == 1) { BAR(); if (FDELAY) __builtin_amdgcn_s_sleep(FDELAY); }
      {
        const unsigned pz = q8 ^ (((r8 & 3) << 1) | (r8 >> 2));
        const u16* src; int stride;
        if (wave == 0) { stride = NH * HD; src = p.wq + (size_t)(kq * 128 + r8) * stride + hA * HD + ((r >> 2) & 1) * 64 + pz * 8; }
        else { stride = FF; src = (mat ? p.wu : p.wg) + (size_t)(kq1 * 128 + r8) * stride + min(cg * 64 + (int)pz * 8, FF - 8); }
#pragma unroll
        for (int i = 0; i < 16; ++i) GLDS(src + (size_t)i * 8 * stride, L + st + i * 1024, AUX_NT);
      }
      v4u xr[4];
      {
        const u16* xs = p.x + (l16 & 7) * DM + kq1 * 128 + g4 * 8;
#pragma unroll
        for (int s = 0; s < 4; ++s) xr[s] = *(const GAS v4u*)(xs + s * 32);
      }
      if (wave == 0) BAR();
      if (l16 >= 8) {
#pragma unroll
        for (int s = 0; s < 4; ++s) xr[s] = (v4u){0x3f803f80u, 0x3f803f80u, 0x3f803f80u, 0x3f803f80u};
      }
      lds_wait1(L + L_FLG + 12);
      const f32x2 stt = *(const LAS f32x2*)(L + L_STAT + (l16 & 7) * 8);
      VMWAIT(0);
      STAMP(1);
      f32x4 acc[4];
      {
        const unsigned rq = l16 >> 2, pc = (l16 & 3) >> 1, ph = (l16 & 1) * 8;
        const unsigned a0 = st + (8 * g4 + rq) * 128 + ((pc ^ (rq << 1)) * 16) + ph;
        const unsigned a1 = st + (8 * g4 + rq + 4) * 128 + ((pc ^ (rq << 1) ^ 1) * 16) + ph;
        v4u af[16];
#pragma unroll
        for (int nt = 0; nt < 4; ++nt)
#pragma unroll
          for (int s = 0; s < 4; ++s) af[nt * 4 + s] = tr_read2(L + (a0 ^ (nt * 32)) + s * 4096, L + (a1 ^ (nt * 32)) + s * 4096);
#pragma unroll
        for (int nt = 0; nt < 4; ++nt) {
          acc[nt] = (f32x4){0.f, 0.f, 0.f, 0.f};
#pragma unroll
          for (int s = 0; s < 4; ++s) acc[nt] = mfma16(af[nt * 4 + s], xr[s], acc[nt]);
        }
        if (lane == 0) *(volatile LAS unsigned*)(L + L_FLG + wave * 4) = 1;
      }
#pragma unroll
      for (int nt = 0; nt < 4; ++nt)
#pragma unroll
        for (int j = 0; j < 4; ++j) acc[nt][j] = (acc[nt][j] - stt.x * dppm<0x158>(acc[nt][j])) * stt.y;
      if (wave == 0) {
#pragma unroll
        for (int np = 0; np < 2; ++np) {
          const unsigned a = pk2(acc[2 * np][0], acc[2 * np][1]), b = pk2(acc[2 * np][2], acc[2 * np][3]);
          const unsigned c = dppu<0x128>(pk2(acc[2 * np + 1][0], acc[2 * np + 1][1])), d = dppu<0x128>(pk2(acc[2 * np + 1][2], acc[2 * np + 1][3]));
          const unsigned u = ((r >> 2) & 1) * 4 + 2 * np + (l16 >> 3);
          __builtin_amdgcn_raw_buffer_store_b128((v4u){l16 < 8 ? a : c, tag, l16 < 8 ? b : d, tag}, wsr,
                                                 WS_QG + ((((hA * 4 + kq) * 4 + ((l16 & 7) >> 1)) * 8 + u) * 8 + (l16 & 1) * 4 + g4) * 16, 0, AUX_SC1);
        }
      } else {
#pragma unroll
        for (int nt = 0; nt < 4; ++nt) {
          const unsigned c = dppu<0x128>(f2u(acc[nt][2])), d = dppu<0x128>(f2u(acc[nt][3]));
          __builtin_amdgcn_raw_buffer_store_b128((v4u){l16 < 8 ? f2u(acc[nt][0]) : c, tag, l16 < 8 ? f2u(acc[nt][1]) : d, tag}, wsr,
                                                 WS_GU + ((((mat * 4 + kq1) * 176 + cg * 8 + 2 * nt + (g4 >> 1)) * 8 + (l16 & 7)) * 4 + 2 * (g4 & 1) + (l16 >> 3)) * 16, 0, AUX_SC1);
        }
      }
      STAMP(2);
    } else if (wave == 2) {
      v4u wf[16], qr[16];
      {
        const u16* src = p.wuk + ((size_t)(sl * 64 + g4) * NH + hB) * HD;
#pragma unroll
        for (int n = 0; n < 16; ++n) GLDS(src + (size_t)n * 4 * NH * HD + ((l16 ^ ((n * 4 + g4) & 15)) * 8), L + L_UK + n * 1024, AUX_NT);
      }
      BAR();
      VMWAIT(0);
      {
        const unsigned a0 = L_UK + l16 * 256 + (((g4 >> 1) ^ l16) * 16) + (g4 & 1) * 8;
#pragma unroll
        for (int ct = 0; ct < 4; ++ct)
#pragma unroll
          for (int s = 0; s < 4; ++s) {
            const v2u lo = *(const LAS v2u*)(L + (a0 ^ (s * 64)) + ct * 4096), hi = *(const LAS v2u*)(L + (a0 ^ (s * 64 + 32)) + ct * 4096);
            wf[ct * 4 + s] = (v4u){lo.x, lo.y, hi.x, hi.y};
          }
        LGKM0();
        if (lane == 0) *(volatile LAS unsigned*)(L + L_FLG + 8) = 1;
      }
      STAMP(12);
      SENTINEL(WS_QG + (((hB * 4 + (q8 & 3)) * 4) * 8 + (q8 >> 2) * 4) * 8 * 16, 16, 1);
      const unsigned qoff = WS_QG + ((((hB * 4 + (l16 >> 3)) * 4 + ((l16 & 7) >> 1)) * 8) * 8 + (l16 & 1) * 4 + g4) * 16;
      GATHER(16, qr, qoff + (i >> 3) * 8192 + (i & 7) * 128);
      STAMP(1);
      const float scale = 0.08838834764831845f * 1.4426950408889634f;
#pragma unroll
      for (int cp = 0; cp < 2; ++cp) {
        f32x4 a0 = {0.f, 0.f, 0.f, 0.f}, a1 = {0.f, 0.f, 0.f, 0.f};
#pragma unroll
        for (int k = 0; k < 8; ++k) {
          const v4u bq = {qr[2 * k].x, qr[2 * k].z, qr[2 * k + 1].x, qr[2 * k + 1].z};
          a0 = mfma16(wf[cp * 8 + (k & 3)], bq, a0); a1 = mfma16(wf[cp * 8 + 4 + (k & 3)], bq, a1);
        }
        float y[4];
#pragma unroll
        for (int j = 0; j < 4; ++j) {
          const float keep = l16 < 8 ? a0[j] : a1[j], give = l16 < 8 ? a1[j] : a0[j];
          y[j] = (keep + dppm<0x128>(give)) * scale;
        }
        __builtin_amdgcn_raw_buffer_store_b128((v4u){pk2(y[0], y[1]), tag, pk2(y[2], y[3]), tag}, wsr,
                                               WS_QA + (((l16 & 7) * 32 + hB) * 128 + sl * 16 + cp * 8 + (l16 >> 3) * 4 + g4) * 16, 0, AUX_SC1);
      }
      STAMP(2);
    } else {
      BAR();
    }
    v4u ql;
    {
      v4u qr[18];
      const unsigned qoff = WS_QA + ((bA * 32 + wave * 8) * 128 + lane) * 16;
      const unsigned qoff2 = WS_QA + ((bA * 32 + (lane & 1)) * 128 + 124 + hf * 2) * 16;
      lds_wait1(L + L_GO);
      GATHER(18, qr, (i < 16 ? qoff + i * 1024 : qoff2 + (i - 16) * 16));
      STAMP(3);
      lds_wait4(L + L_FLG, 1);
#pragma unroll
      for (int i = 0; i < 16; ++i) {
        const unsigned h = wave * 8 + (i >> 1);
        const unsigned chn = (i & 1) * 32 + (lane >> 1);
        if (!((i == 1 || i == 3) && wave == 0 && lane >= 60))
          *(LAS v2u*)(L + L_S + h * 1024 + (((chn & 48) | ((chn & 15) ^ swz(h))) * 16) + (lane & 1) * 8) = (v2u){qr[i].x, qr[i].z};
      }
      ql = (v4u){qr[16].x, qr[16].z, qr[17].x, qr[17].z};
      LGKM0();
      if (lane == 0) *(volatile LAS unsigned*)(L + L_IMG + wave * 4) = 1;
    }
    tail_loads();
    lds_wait4(L + L_IMG, 1);
    STAMP(4);
    {
#pragma unroll
      for (int i = 0; i < 16; ++i) sacc[i] = 0.f;
      const unsigned tz = (hf ^ swz(l32)) * 16;
      const unsigned qb = L_S + l32 * 1024 + tz;
      const unsigned kb = L_CKV + (wave * 32 + l32) * 256 + tz;
      v4u qf[2][8], kf[2][8];
      unsigned have = lds_wait4(L + L_PROG, 1);
#pragma unroll
      for (int s = 0; s < 8; ++s) { kf[0][s] = *(const LAS v4u*)(L + (kb ^ (s * 32))); qf[0][s] = *(const LAS v4u*)(L + (qb ^ (s * 32))); }
#pragma unroll
      for (int blk = 0; blk < 4; ++blk) {
        if (blk < 3) {
          if (have < (unsigned)blk + 2) {
#pragma unroll
            for (int s = 0; s < 8; ++s) sacc = mfma32(kf[blk & 1][s], qf[blk & 1][s], sacc);
            have = lds_wait4(L + L_PROG, blk + 2);
#pragma unroll
            for (int s = 0; s < 8; ++s) {
              kf[(blk + 1) & 1][s] = *(const LAS v4u*)(L + (kb ^ (s * 32)) + (blk + 1) * 32768);
              qf[(blk + 1) & 1][s] = *(const LAS v4u*)(L + (qb ^ (s * 32)) + (blk + 1) * 256);
            }
          } else {
#pragma unroll
            for (int s = 0; s < 8; ++s) {
              kf[(blk + 1) & 1][s] = *(const LAS v4u*)(L + (kb ^ (s * 32)) + (blk + 1) * 32768);
              qf[(blk + 1) & 1][s] = *(const LAS v4u*)(L + (qb ^ (s * 32)) + (blk + 1) * 256);
              sacc = mfma32(kf[blk & 1][s], qf[blk & 1][s], sacc);
            }
          }
          if (blk == 0 && lane == 0) *(volatile LAS unsigned*)(L + L_IMG + wave * 4) = 2;
        } else {
          if (l32 < 2) qf[1][7] = ql;
#pragma unroll
          for (int s = 0; s < 8; ++s) sacc = mfma32(kf[1][s], qf[1][s], sacc);
        }
      }
    }
    STAMP(5);
    float mx = fmaxf(fmaxf(fmaxf(sacc[0], sacc[1]), fmaxf(sacc[2], sacc[3])), fmaxf(fmaxf(sacc[4], sacc[5]), fmaxf(sacc[6], sacc[7])));
    mx = fmaxf(mx, fmaxf(fmaxf(fmaxf(sacc[8], sacc[9]), fmaxf(sacc[10], sacc[11])), fmaxf(fmaxf(sacc[12], sacc[13]), fmaxf(sacc[14], sacc[15]))));
    mx = xmax32(mx);
    lds_wait4(L + L_IMG, 2);
    if (lane < 32) *(LAS float*)(L + L_S + lane * 1024 + swz(lane) * 16 + wave * 4) = mx;
  }
  BAR();
  const unsigned zl = swz(l32);
  if (wave < 4) {
    const f32x4 a = *(const LAS f32x4*)(L + L_S + l32 * 1024 + zl * 16);
    m = fmaxf(fmaxf(a[0], a[1]), fmaxf(a[2], a[3]));
    float ls = 0.f;
#pragma unroll
    for (int q = 0; q < 4; ++q) {
      const unsigned p0 = pk2(__builtin_amdgcn_exp2f(sacc[4 * q] - m), __builtin_amdgcn_exp2f(sacc[4 * q + 1] - m));
      const unsigned p1 = pk2(__builtin_amdgcn_exp2f(sacc[4 * q + 2] - m), __builtin_amdgcn_exp2f(sacc[4 * q + 3] - m));
      *(LAS v2u*)(L + L_S + l32 * 1024 + 256 + (((wave * 4 + q) ^ zl) * 16) + hf * 8) = (v2u){p0, p1};
      ls = dot_ones(p1, dot_ones(p0, ls));
    }
    ls = xsum32(ls);
    if (lane < 32) *(LAS float*)(L + L_S + lane * 1024 + 512 + zl * 16 + wave * 4) = ls;
  }
  BAR();
  const unsigned wb = L_CKV + (wave >> 1) * 32768;
  const unsigned wq8 = (wave & 1) * 8;
  f32x16 o0, o1;
  float lsum;
  {
    v4u pf[8];
    {
      const unsigned pbs = L_S + l32 * 1024 + 256 + (hf ^ zl) * 16;
#pragma unroll
      for (int ks = 0; ks < 8; ++ks) pf[ks] = *(const LAS v4u*)(L + (pbs ^ (ks * 32)));
    }
#pragma unroll
    for (int i = 0; i < 16; ++i) { o0[i] = 0.f; o1[i] = 0.f; }
    const unsigned blk = (lane >> 4) & 1, q = (lane & 15) >> 2, pp = lane & 3;
    unsigned ta[2];
#pragma unroll
    for (int tt = 0; tt < 2; ++tt) {
      const unsigned row = 8 * hf + 4 * tt + q;
      ta[tt] = wb + row * 256 + (((wq8 + 2 * blk + (pp >> 1)) ^ swz(row)) * 16) + 8 * (pp & 1);
    }
#pragma unroll
    for (int ks = 0; ks < 8; ++ks) {
      o0 = mfma32(tr_read2(L + ta[0] + ks * 4096, L + ta[1] + ks * 4096), pf[ks], o0);
      o1 = mfma32(tr_read2(L + (ta[0] ^ 64) + ks * 4096, L + (ta[1] ^ 64) + ks * 4096), pf[ks], o1);
    }
    const f32x4 a = *(const LAS f32x4*)(L + L_S + l32 * 1024 + 512 + zl * 16);
    lsum = (a[0] + a[1]) + (a[2] + a[3]);
  }
  STAMP(6);
  const unsigned sc_e = wb + r8 * 256 + (((wq8 + q8) ^ swz(r8)) * 16);
  const unsigned sc_o = wb + (r8 + 8) * 256 + (((wq8 + q8) ^ swz(r8 + 8)) * 16);
  {
    const float inv = __builtin_amdgcn_rcpf(lsum);
    o0 *= inv; o1 *= inv;
    const unsigned wa = wb + l32 * 256 + (((wq8 + hf) ^ zl) * 16);
#pragma unroll
    for (int gp = 0; gp < 4; ++gp) {
      const f32x16& o = gp < 2 ? o0 : o1;
      const int b8 = (gp & 1) * 8;
      auto rx = __builtin_amdgcn_permlane32_swap(pk2(o[b8 + 0], o[b8 + 1]), pk2(o[b8 + 4], o[b8 + 5]), false, false);
      auto ry = __builtin_amdgcn_permlane32_swap(pk2(o[b8 + 2], o[b8 + 3]), pk2(o[b8 + 6], o[b8 + 7]), false, false);
      *(LAS v4u*)(L + (wa ^ (gp * 32))) = (v4u){rx[0], ry[0], rx[1], ry[1]};
    }
    LGKM0();
    const unsigned pbase = WS_PART + ((((bA * 32 + r8) * 8 + wave) * 32 + chunk) * 64 + q8 * 8) * 2;
#pragma unroll
    for (int i = 0; i < 4; ++i) {
      const v4u t = *(const LAS v4u*)(L + ((i & 1) ? sc_o : sc_e) + (i >> 1) * 4096);
      __builtin_amdgcn_raw_buffer_store_b128(t, wsr, pbase + i * (8 * 8 * 32 * 128), 0, AUX_SC1);
    }
    *(LAS v4u*)(L + sc_e + 8192) = wo2[0];
    *(LAS v4u*)(L + sc_o + 8192) = wo2[1];
    *(LAS v4u*)(L + sc_e + 12288) = wv2[0];
    *(LAS v4u*)(L + sc_o + 12288) = wv2[1];
    *(LAS v4u*)(L + sc_e + 16384) = wd1;
    VMWAIT(0);
    BAR();
    if (wave == 0 && lane < 32)
      __builtin_amdgcn_raw_buffer_store_b64((v2u){f2u(m + __builtin_amdgcn_logf(lsum)), tag}, wsr,
                                            WS_FL + ((((lane >> 2) * 8 + bA) * 32 + chunk) * 4 + (lane & 3)) * 8, 0, AUX_SC1);
    STAMP(7);
  }
  const unsigned pc = (l16 & 3) >> 1, ph = (l16 & 1) * 8, rq = l16 >> 2;
  v4u av[2], ao[4];
  f32x4 accE = {0.f, 0.f, 0.f, 0.f};
  {
    const unsigned i8 = (wave >> 2) * 8, pcs = (g4 & 1) * 8 + 2 * (wave & 3) + pc;
    const unsigned b0 = (g4 >> 1) * 32768 + ph;
    const unsigned v0 = b0 + (48 + i8 + rq) * 256 + ((pcs ^ swz(i8 + rq)) * 16), v1 = b0 + (52 + i8 + rq) * 256 + ((pcs ^ swz(i8 + rq + 4)) * 16);
    av[0] = tr_read2(L + v0, L + v1);
    av[1] = tr_read2(L + v0 + 65536, L + v1 + 65536);
    if (wave < 4) {
      v4u hr[4];
      const unsigned rin = 8 * (g4 & 1) + rq, pco = (g4 >> 1) * 8 + 2 * wave + pc;
      const unsigned o0a = (32 + rin) * 256 + ((pco ^ swz(rin)) * 16) + ph, o1a = (36 + rin) * 256 + ((pco ^ swz(rin + 4)) * 16) + ph;
#pragma unroll
      for (int ks = 0; ks < 4; ++ks) ao[ks] = tr_read2(L + o0a + ks * 32768, L + o1a + ks * 32768);
      const unsigned d0 = b0 + (64 + rq) * 256 + ((pcs ^ swz(rq)) * 16), d1 = b0 + (68 + rq) * 256 + ((pcs ^ swz(rq + 4)) * 16);
      const v4u ad0 = tr_read2(L + d0, L + d1), ad1 = tr_read2(L + d0 + 65536, L + d1 + 65536);
      const bool on = 4 + g4 < ntask;
      const unsigned h0 = WS_HM + ((task0 + g4) * 8 + (l16 & 7)) * 32, h1 = WS_HM + ((task0 + (on ? 4 + g4 : 0)) * 8 + (l16 & 7)) * 32;
      for (unsigned spin = 0; spin < SPIN_CAP; ++spin) {
        hr[0] = __builtin_amdgcn_raw_buffer_load_b128(wsr, h0, 0, AUX_SC1);
        hr[1] = __builtin_amdgcn_raw_buffer_load_b128(wsr, h0 + 16, 0, AUX_SC1);
        hr[2] = __builtin_amdgcn_raw_buffer_load_b128(wsr, h1, 0, AUX_SC1);
        hr[3] = __builtin_amdgcn_raw_buffer_load_b128(wsr, h1 + 16, 0, AUX_SC1);
        if (__all((hr[0].y == tag) & (hr[0].w == tag) & (hr[1].y == tag) & (hr[1].w == tag) & (hr[2].y == tag) & (hr[2].w == tag) & (hr[3].y == tag) & (hr[3].w == tag))) break;
        __builtin_amdgcn_s_sleep(1);
      }
      accE = mfma16(ad0, (v4u){hr[0].x, hr[0].z, hr[1].x, hr[1].z}, accE);
      accE = mfma16(ad1, on ? (v4u){hr[2].x, hr[2].z, hr[3].x, hr[3].z} : (v4u){0u, 0u, 0u, 0u}, accE);
    }
  }
  {
    v2u fr;
    const unsigned fo = WS_FL + (((xg * 8 + wave) * 32 + l32) * 4 + (hA & 3)) * 8;
    for (unsigned spin = 0; spin < SPIN_CAP; ++spin) {
      fr = __builtin_amdgcn_raw_buffer_load_b64(wsr, fo, 0, AUX_SC1);
      if (__all(fr.y == tag)) break;
      __builtin_amdgcn_s_sleep(1);
    }
    asm volatile("" ::: "memory");
    STAMP(8);
    v2u pr[8];
    const unsigned po = WS_PART + ((((wave * 32 + hA) * 8 + sl) * 32 + g4) * 64 + l16 * 4) * 2;
#pragma unroll
    for (int u = 0; u < 8; ++u) pr[u] = __builtin_amdgcn_raw_buffer_load_b64(wsr, po + u * 512, 0, AUX_SC1);
    const float lse = u2f(fr.x);
    float M = lse;
    M = fmaxf(M, dppm<0x121>(M)); M = fmaxf(M, dppm<0x122>(M)); M = fmaxf(M, dppm<0x124>(M)); M = fmaxf(M, dppm<0x128>(M));
    M = xmax16(M);
    const float wj = bflo(pk2(__builtin_amdgcn_exp2f(lse - M), 0.f));
    float Ls = wj;
    Ls += dppm<0x121>(Ls); Ls += dppm<0x122>(Ls); Ls += dppm<0x124>(Ls); Ls += dppm<0x128>(Ls);
    Ls = xsum16(Ls);
    const float inv = __builtin_amdgcn_rcpf(Ls);
    const unsigned e4 = l16 & 3;
    const unsigned sh = (e4 & 1) ? 0u : 16u, k0 = e4 < 2 ? ~0u : 0u;
    unsigned wa[16];
#pragma unroll
    for (int u = 0; u < 8; ++u) {
      const unsigned w = (unsigned)__builtin_amdgcn_ds_bpermute((g4 + 4 * u) * 4, f2u(wj)) >> sh;
      wa[2 * u] = w & k0; wa[2 * u + 1] = w & ~k0;
    }
    f32x4 acc = {0.f, 0.f, 0.f, 0.f};
#pragma unroll
    for (int u = 0; u < 4; ++u)
      acc = mfma16((v4u){wa[4 * u], wa[4 * u + 1], wa[4 * u + 2], wa[4 * u + 3]}, (v4u){pr[2 * u].x, pr[2 * u].y, pr[2 * u + 1].x, pr[2 * u + 1].y}, acc);
    if (lane < 16) *(LAS v2u*)(L + L_CL + wave * 1040 + lane * 8) = (v2u){pk2(acc[0] * inv, acc[1] * inv), pk2(acc[2] * inv, acc[3] * inv)};
    BAR();
  }
  const unsigned one = (l16 & 1) ? 0x3f800000u : 0x00003f80u;
  const unsigned s0 = (l16 & 2) ? 0u : one, s1 = (l16 & 2) ? one : 0u;
  {
    const v4u b0 = *(const LAS v4u*)(L + L_CL + (l16 & 7) * 1040 + g4 * 16), b1 = *(const LAS v4u*)(L + L_CL + (l16 & 7) * 1040 + 64 + g4 * 16);
    f32x4 acc = {0.f, 0.f, 0.f, 0.f};
    acc = mfma16(av[0], b0, acc);
    acc = mfma16(av[1], b1, acc);
    if (l16 < 8)
      __builtin_amdgcn_raw_buffer_store_b128((v4u){pk2(acc[0], acc[1]), tag, pk2(acc[2], acc[3]), tag}, wsr,
                                             WS_CX + ((((hA * 8 + wave) * 8 + sl) * 8 + l16) * 4 + g4) * 16, 0, AUX_SC1);
    STAMP(9);
    v4u cr[4];
    const unsigned co = WS_CX + ((hB * 8 + wave) * 256 + lane) * 16;
    for (unsigned spin = 0; spin < SPIN_CAP; ++spin) {
#pragma unroll
      for (int i = 0; i < 4; ++i) cr[i] = __builtin_amdgcn_raw_buffer_load_b128(wsr, co + i * 1024, 0, AUX_SC1);
      if (__all((cr[0].y == tag) & (cr[0].w == tag) & (cr[1].y == tag) & (cr[1].w == tag) & (cr[2].y == tag) & (cr[2].w == tag) & (cr[3].y == tag) & (cr[3].w == tag))) break;
      __builtin_amdgcn_s_sleep(1);
    }
    STAMP(10);
    {
      const bool on = ((l16 >> 2) & 1) == (g4 & 1);
      const unsigned t0 = on ? s0 : 0u, t1 = on ? s1 : 0u;
      f32x4 a = {0.f, 0.f, 0.f, 0.f};
      a = mfma16((v4u){t0, t1, t0, t1}, (v4u){cr[0].x, cr[0].z, cr[1].x, cr[1].z}, a);
      a = mfma16((v4u){t0, t1, t0, t1}, (v4u){cr[2].x, cr[2].z, cr[3].x, cr[3].z}, a);
      if (lane < 32) *(LAS v2u*)(L + L_CX + (lane >> 2) * 272 + wave * 32 + (lane & 3) * 8) = (v2u){pk2(a[0], a[1]), pk2(a[2], a[3])};
    }
    BAR();
  }
  if (wave < 4) {
#pragma unroll
    for (int ks = 0; ks < 4; ++ks) accE = mfma16(ao[ks], *(const LAS v4u*)(L + L_CX + (l16 & 7) * 272 + ks * 64 + g4 * 16), accE);
    if (l16 < 8)
      __builtin_amdgcn_raw_buffer_store_b128((v4u){pk2(accE[0], accE[1]), tag, pk2(accE[2], accE[3]), tag}, wsr,
                                             WS_OG + ((((sl * 4 + wave) * 32 + hB) * 8 + l16) * 4 + g4) * 16, 0, AUX_SC1);
    STAMP(11);
  } else if (wave == 4) {
    const unsigned oo = WS_OG + ((((wg & 31) * 32 + (lane >> 2)) * 8 + (wg >> 5)) * 4 + (lane & 3)) * 16;
    const v2u xw = *(const GAS v2u*)(p.x + wg * 16 + (lane & 3) * 4);
    v4u r0, r1;
    for (unsigned spin = 0; spin < SPIN_CAP; ++spin) {
      r0 = __builtin_amdgcn_raw_buffer_load_b128(wsr, oo, 0, AUX_SC1);
      r1 = __builtin_amdgcn_raw_buffer_load_b128(wsr, oo + 16 * 512, 0, AUX_SC1);
      if (__all((r0.y == tag) & (r0.w == tag) & (r1.y == tag) & (r1.w == tag))) break;
      __builtin_amdgcn_s_sleep(1);
    }
    f32x4 acc = {0.f, 0.f, 0.f, 0.f};
    acc = mfma16((v4u){s0, s1, s0, s1}, (v4u){r0.x, r0.z, r1.x, r1.z}, acc);
#pragma unroll
    for (int j = 0; j < 4; ++j) { acc[j] += dppm<0x124>(acc[j]); acc[j] += dppm<0x128>(acc[j]); }
    if (lane < 4)
      *(GAS v2u*)(p.out + wg * 16 + lane * 4) = (v2u){pk2(bflo(xw.x) + acc[0], bfhi(xw.x) + acc[1]), pk2(bflo(xw.y) + acc[2], bfhi(xw.y) + acc[3])};
    if (wg == 0 && lane == 0) __builtin_amdgcn_raw_buffer_store_b32(tag, wsr, WS_EPOCH, 0, AUX_SC1);
    STAMP(11);
  }
}

extern "C" void kernel_launch(void* const* d_in, const int* in_sizes, int n_in, void* d_out, int out_size, void* d_ws, size_t ws_size,
                              hipStream_t stream) {
  Params p;
  p.x = (const u16*)d_in[0]; p.ckv = (const u16*)d_in[1]; p.wq = (const u16*)d_in[2]; p.wuk = (const u16*)d_in[3];
  p.wuv = (const u16*)d_in[4]; p.wo = (const u16*)d_in[5]; p.wg = (const u16*)d_in[7]; p.wu = (const u16*)d_in[8]; p.wd = (const u16*)d_in[9];
  p.out = (u16*)d_out; p.ws = (unsigned char*)d_ws;
  (void)hipFuncSetAttribute((const void*)fused_kernel, hipFuncAttributeMaxDynamicSharedMemorySize, LDS_BYTES);
  fused_kernel<<<NWG, NTHR, LDS_BYTES, stream>>>(p);
}
```

```cpp
#define DK 1
#define DW 3
#include <hip/hip_runtime.h>
#include <hip/hip_bf16.h>
#include <stdint.h>

#define STAMPS 0
#define CODE_LINES 224
#define PRETLB 1
#define WIN 6
#define LDELAY 0
#define FDELAY 40

typedef unsigned v4u __attribute__((ext_vector_type(4)));
typedef unsigned v2u __attribute__((ext_vector_type(2)));
typedef float f32x2 __attribute__((ext_vector_type(2)));
typedef float f32x4 __attribute__((ext_vector_type(4)));
typedef float f32x16 __attribute__((ext_vector_type(16)));
typedef short bf16x8 __attribute__((ext_vector_type(8)));
typedef short s16x4 __attribute__((ext_vector_type(4)));
typedef __bf16 bf16x2 __attribute__((ext_vector_type(2)));
typedef unsigned short u16;
#define LAS __attribute__((address_space(3)))
#define GAS __attribute__((address_space(1)))

constexpr int NB = 8, NH = 32, SK = 4096, HD = 128, DM = 512, DC = 512, FF = 1376;
constexpr int NWG = 256, NTHR = 512;
constexpr unsigned WS_EPOCH = 0;
constexpr unsigned WS_QG = 4096;
constexpr unsigned WS_QA = WS_QG + 32 * 4 * 256 * 16;
constexpr unsigned WS_GU = WS_QA + 8 * 32 * 128 * 16;
constexpr unsigned WS_HM = WS_GU + 8 * 176 * 32 * 16;
constexpr unsigned WS_FL = WS_HM + 176 * 8 * 2 * 16;
constexpr unsigned WS_CX = WS_FL + 8 * 8 * 32 * 4 * 8;
constexpr unsigned WS_OG = WS_CX + 32 * 8 * 8 * 8 * 4 * 16;
constexpr unsigned WS_PART = WS_OG + 32 * 32 * 8 * 4 * 16;
constexpr unsigned WS_END = WS_PART + 8 * 32 * 32 * 512 * 2;
constexpr unsigned WS_STAMP = 32u << 20;
constexpr unsigned L_CKV = 0;
constexpr unsigned L_STG = 96 * 1024;
constexpr unsigned L_S = 128 * 1024;
constexpr unsigned L_PROG = L_S + 63 * 16;
constexpr unsigned L_IMG = L_S + 62 * 16;
constexpr unsigned L_FLG = L_S + 1024 + (63 ^ 4) * 16;
constexpr unsigned L_GO = L_S + 1024 + (62 ^ 4) * 16;
constexpr unsigned L_STAT = L_S + 4096;
constexpr unsigned L_UK = L_S + 16384;
constexpr unsigned L_CL = L_S + 512;
constexpr unsigned L_CX = L_S + 16384;
constexpr unsigned LDS_BYTES = 160 * 1024;
constexpr int AUX_NT = 2, AUX_SC1 = 16;
constexpr unsigned SPIN_CAP = 1u << 18;

struct Params {
  const u16 *x, *ckv, *wq, *wuk, *wuv, *wo, *wg, *wu, *wd;
  u16* out;
  unsigned char* ws;
};

__device__ __forceinline__ float u2f(unsigned u) { return __uint_as_float(u); }
__device__ __forceinline__ unsigned f2u(float f) { return __float_as_uint(f); }
__device__ __forceinline__ float bflo(unsigned w) { return u2f(w << 16); }
__device__ __forceinline__ float bfhi(unsigned w) { return u2f(w & 0xffff0000u); }
__device__ __forceinline__ unsigned pk2(float lo, float hi) {
  f32x2 f = {lo, hi};
  return __builtin_bit_cast(unsigned, __builtin_convertvector(f, bf16x2));
}
__device__ __forceinline__ float dot_ones(unsigned a, float c) {
  return __builtin_amdgcn_fdot2_f32_bf16(__builtin_bit_cast(bf16x2, a), __builtin_bit_cast(bf16x2, 0x3f803f80u), c, false);
}
__device__ __forceinline__ f32x4 mfma16(v4u a, v4u b, f32x4 c) {
  return __builtin_amdgcn_mfma_f32_16x16x32_bf16(__builtin_bit_cast(bf16x8, a), __builtin_bit_cast(bf16x8, b), c, 0, 0, 0);
}
__device__ __forceinline__ f32x16 mfma32(v4u a, v4u b, f32x16 c) {
  return __builtin_amdgcn_mfma_f32_32x32x16_bf16(__builtin_bit_cast(bf16x8, a), __builtin_bit_cast(bf16x8, b), c, 0, 0, 0);
}
__device__ __forceinline__ unsigned swz(unsigned row) { return ((row & 3) << 2) | ((row >> 2) & 3); }
#define BAR() do { asm volatile("s_waitcnt lgkmcnt(0)" ::: "memory"); __builtin_amdgcn_s_barrier(); asm volatile("" ::: "memory"); } while (0)
#define VMWAIT_(n) asm volatile("s_waitcnt vmcnt(" #n ")" ::: "memory")
#define VMWAIT(n) VMWAIT_(n)
#define LGKM0() asm volatile("s_waitcnt lgkmcnt(0)" ::: "memory")
#define GLDS(src, dst, aux) __builtin_amdgcn_global_load_lds((const GAS unsigned*)(src), (LAS unsigned*)(dst), 16, 0, (aux))
#define WLOAD(p) __builtin_nontemporal_load(p)
__device__ __forceinline__ v2u tr_read(LAS unsigned char* a) {
  return __builtin_bit_cast(v2u, __builtin_amdgcn_ds_read_tr16_b64_v4i16((LAS s16x4*)a));
}
__device__ __forceinline__ v4u tr_read2(LAS unsigned char* a, LAS unsigned char* b) {
  const v2u x = tr_read(a), y = tr_read(b);
  return (v4u){x.x, x.y, y.x, y.y};
}
template <int CTRL>
__device__ __forceinline__ unsigned dppu(unsigned v) { return __builtin_amdgcn_update_dpp(v, v, CTRL, 0xf, 0xf, false); }
template <int CTRL>
__device__ __forceinline__ float dppm(float v) { return u2f(dppu<CTRL>(f2u(v))); }
__device__ __forceinline__ float xsum16(float v) {
  auto s = __builtin_amdgcn_permlane16_swap(f2u(v), f2u(v), false, false);
  return u2f(s[0]) + u2f(s[1]);
}
__device__ __forceinline__ float xmax16(float v) {
  auto s = __builtin_amdgcn_permlane16_swap(f2u(v), f2u(v), false, false);
  return fmaxf(u2f(s[0]), u2f(s[1]));
}
__device__ __forceinline__ float xsum32(float v) {
  auto s = __builtin_amdgcn_permlane32_swap(f2u(v), f2u(v), false, false);
  return u2f(s[0]) + u2f(s[1]);
}
__device__ __forceinline__ float xmax32(float v) {
  auto s = __builtin_amdgcn_permlane32_swap(f2u(v), f2u(v), false, false);
  return fmaxf(u2f(s[0]), u2f(s[1]));
}
__device__ __forceinline__ bool rec_ok(v4u r, unsigned tag) { return __all((r.y == tag) & (r.w == tag)); }
__device__ __forceinline__ unsigned umin4(v4u a) { return __builtin_elementwise_min(__builtin_elementwise_min(a.x, a.y), __builtin_elementwise_min(a.z, a.w)); }
__device__ __forceinline__ unsigned lds_wait4(LAS unsigned char* a, unsigned n) {
  unsigned mn = 0;
  for (unsigned spin = 0; spin < (SPIN_CAP << 4); ++spin) {
    const v4u w = *(volatile LAS v4u*)a;
    mn = __builtin_amdgcn_readfirstlane(umin4(w));
    if (mn >= n) break;
    __builtin_amdgcn_s_sleep(1);
  }
  asm volatile("" ::: "memory");
  return mn;
}
__device__ __forceinline__ void lds_wait1(LAS unsigned char* a) {
  for (unsigned spin = 0; spin < (SPIN_CAP << 4); ++spin) {
    if (__builtin_amdgcn_readfirstlane(*(volatile LAS unsigned*)a)) break;
    __builtin_amdgcn_s_sleep(1);
  }
  asm volatile("" ::: "memory");
}
template <int CTRL, int RM = 0xf>
__device__ __forceinline__ float dpp0(float v) { return u2f(__builtin_amdgcn_update_dpp(0, f2u(v), CTRL, RM, 0xf, true)); }
__device__ __forceinline__ float wave_sum(float v) {
  v += dppm<0x121>(v); v += dppm<0x122>(v); v += dppm<0x124>(v); v += dppm<0x128>(v);
  return xsum32(xsum16(v));
}

#define STAMP(k) do { if ((k) == 0) dt0 = (unsigned)__builtin_amdgcn_s_memrealtime(); if ((k) == DK && wave == DW && lane == 0) *(volatile LAS unsigned*)(L + L_GO + 8) = (unsigned)__builtin_amdgcn_s_memrealtime(); } while (0)

#define SENTINEL(SENT, THR, SLP)                                                                             \
  do {                                                                                                       \
    _Pragma("nounroll") for (unsigned spin = 0; spin < SPIN_CAP; ++spin) {                                   \
      const unsigned sr = __builtin_amdgcn_raw_buffer_load_b32(wsr, (SENT) + 4, 0, AUX_SC1);                 \
      if (__builtin_popcountll(__ballot(sr == tag)) >= (THR)) break;                                         \
      __builtin_amdgcn_s_sleep(SLP);                                                                         \
    }                                                                                                        \
  } while (0)
#define GATHER(N, qr, ADDR)                                                                                  \
  do {                                                                                                       \
    unsigned done = 0;                                                                                       \
    _Pragma("nounroll") for (unsigned spin = 0; spin < SPIN_CAP; ++spin) {                                   \
      _Pragma("unroll") for (int i = 0; i < N; ++i)                                                          \
        if (!((done >> i) & 1)) qr[i] = __builtin_amdgcn_raw_buffer_load_b128(wsr, (ADDR), 0, AUX_SC1);      \
      _Pragma("unroll") for (int i = 0; i < N; ++i)                                                          \
        if (!((done >> i) & 1)) { if (rec_ok(qr[i], tag)) done |= 1u << i; }                                 \
      if (done == (1u << N) - 1u) break;                                                                     \
    }                                                                                                        \
  } while (0)

__global__ void __launch_bounds__(NTHR, 2) fused_kernel(Params p) {
  extern __shared__ __attribute__((aligned(16))) unsigned char lds_raw[];
  LAS unsigned char* L = (LAS unsigned char*)lds_raw;
  const int tid = threadIdx.x, lane = tid & 63;
  const int wave = __builtin_amdgcn_readfirstlane(tid >> 6);
  const int wg = blockIdx.x, xg = wg & 7, r = wg >> 3;
  const int bA = xg, chunk = r;
  const int hA = xg * 4 + (r >> 3), sl = r & 7;
  const int hB = ((xg + 1) & 7) * 4 + (r >> 3);
  const int ft0 = (xg * 43) >> 1;
  const bool ffn2 = r < (((xg + 1) * 43) >> 1) - ft0;
  const int ft = ft0 + r;
  const bool ffn1 = r < 22;
  const int ntask = hB < 12 ? 6 : 5, task0 = hB < 12 ? 6 * hB : 12 + 5 * hB;
  const __amdgpu_buffer_rsrc_t wsr = __builtin_amdgcn_make_buffer_rsrc((void*)p.ws, 0, WS_END, 0x00020000);
  const int l16 = lane & 15, g4 = lane >> 4;
  const int l32 = lane & 31, hf = lane >> 5;
  const unsigned r8 = lane >> 3, q8 = lane & 7;
  unsigned dt0;
  STAMP(0);
  unsigned long long pc0;
  asm volatile("s_getpc_b64 %0" : "=s"(pc0));
  const unsigned tag = *(const __attribute__((address_space(4))) unsigned*)(p.ws + WS_EPOCH) + 1u;
  asm volatile("" :: "s"(tag));
  v4u wo2[2], wv2[2], wd1;
  auto tail_loads = [&]() {
    const u16* so = p.wo + ((size_t)hB * HD + wave * 16 + r8) * DM + sl * 64 + q8 * 8;
    wo2[0] = WLOAD((const GAS v4u*)so);
    wo2[1] = WLOAD((const GAS v4u*)(so + 8 * DM));
    const u16* sv = p.wuv + ((size_t)(sl * 64 + wave * 8 + r8) * NH + hA) * HD + q8 * 8;
    wv2[0] = WLOAD((const GAS v4u*)sv);
    wv2[1] = WLOAD((const GAS v4u*)(sv + 64));
    const int fr = min(wave * 8 + (int)r8, ntask * 8 - 1);
    wd1 = WLOAD((const GAS v4u*)(p.wd + (size_t)(task0 * 8 + fr) * DM + sl * 64 + q8 * 8));
  };
  f32x16 sacc;
  float m = 0.f;

  if (wave >= 4) {
    const int v = wave - 4;
    const u16* src0 = p.ckv + ((size_t)bA * SK + chunk * 128 + 4 * v + g4) * DC + ((l16 ^ ((g4 << 2) | v)) * 8);
    BAR();
    if (LDELAY) __builtin_amdgcn_s_sleep(LDELAY);
#pragma unroll
    for (int n = 0; n < 32; ++n) {
      if (n == 24) {
        for (unsigned spin = 0; spin < (SPIN_CAP << 4); ++spin) {
          const v2u w = *(volatile LAS v2u*)(L + L_FLG);
          if (__builtin_amdgcn_readfirstlane(w.x & w.y)) break;
          __builtin_amdgcn_s_sleep(1);
        }
        asm volatile("" ::: "memory");
      }
      GLDS(src0 + (size_t)(n & 7) * 16 * DC + (n >> 3) * 128, L + L_CKV + (n >> 3) * 32768 + ((n & 7) * 4 + v) * 1024, AUX_NT);
      if (n >= WIN - 1 && n < 31) {
        __builtin_amdgcn_s_waitcnt(0x0f70 | ((WIN - 1) & 15) | (((WIN - 1) >> 4) << 14));
        if (((n - WIN + 2) & 7) == 0) { asm volatile("" ::: "memory"); *(volatile LAS unsigned*)(L + L_PROG + v * 4) = (n - WIN + 2) >> 3; }
      }
    }
    STAMP(1);
    tail_loads();
    VMWAIT(5);
    *(volatile LAS unsigned*)(L + L_PROG + v * 4) = 4;
    STAMP(2);
    if (wave == 7 && ffn2) {
      v4u gr[4];
      GATHER(4, gr, WS_GU + (((2 * i + hf) * 176 + ft) * 32 + l32) * 16);
      float hm[2];
#pragma unroll
      for (int e = 0; e < 2; ++e) {
        const float gv = xsum32(u2f(e ? gr[0].z : gr[0].x) + u2f(e ? gr[1].z : gr[1].x));
        const float uv = xsum32(u2f(e ? gr[2].z : gr[2].x) + u2f(e ? gr[3].z : gr[3].x));
        const float yy = 0.7978845608028654f * (gv + 0.044715f * gv * gv * gv);
        const float th = 1.f - 2.f / (__expf(2.f * yy) + 1.f);
        hm[e] = 0.5f * gv * (1.f + th) * uv;
      }
      const unsigned a = pk2(hm[0], hm[1]), b = dppu<0xb1>(a);
      if (lane < 32 && !(lane & 1))
        __builtin_amdgcn_raw_buffer_store_b128((v4u){a, tag, b, tag}, wsr, WS_HM + (ft * 16 + (lane >> 1)) * 16, 0, AUX_SC1);
      STAMP(3);
    }
  } else {
    if (wave == 3) {
      v4u xv[8];
#pragma unroll
      for (int i = 0; i < 8; ++i) xv[i] = *(const GAS v4u*)(p.x + i * DM + lane * 8);
      unsigned pf0;
      {
        const unsigned ln = min((unsigned)((r & 3) * 64 + lane), (unsigned)CODE_LINES - 1u);
        pf0 = *(const GAS unsigned*)((pc0 & ~127ull) + ln * 128);
      }
      unsigned pf1 = 0;
      {
        const unsigned char* a;
        if (lane < 6) a = p.ws + lane * (2u << 20) + 1024;
        else if (lane < 8) a = (const unsigned char*)p.wuv + (lane - 6) * (2u << 20);
        else if (lane < 10) a = (const unsigned char*)p.wo + (lane - 8) * (2u << 20);
        else if (lane < 11) a = (const unsigned char*)p.wd;
        else a = (const unsigned char*)p.out;
        if (lane < 12) pf1 = *(const GAS unsigned*)a;
      }
      if (lane < 4) {
        *(LAS unsigned*)(L + L_PROG + lane * 4) = 0; *(LAS unsigned*)(L + L_IMG + lane * 4) = 0;
        *(LAS unsigned*)(L + L_FLG + lane * 4) = (lane == 1 && !ffn1) ? 1 : 0; *(LAS unsigned*)(L + L_GO + lane * 4) = 0;
      }
      BAR();
#pragma unroll
      for (int i = 0; i < 8; ++i) {
        const float mean = wave_sum(dot_ones(xv[i].w, dot_ones(xv[i].z, dot_ones(xv[i].y, dot_ones(xv[i].x, 0.f))))) * (1.f / DM);
        float sq = 0.f;
#pragma unroll
        for (int j = 0; j < 4; ++j) { const unsigned w = xv[i][j]; const float a = bflo(w) - mean, b = bfhi(w) - mean; sq += a * a; sq += b * b; }
        const float var = wave_sum(sq) * (1.f / DM);
        if (lane == 0) *(LAS f32x2*)(L + L_STAT + i * 8) = (f32x2){mean, __builtin_amdgcn_rsqf(var + 1e-5f)};
      }
      LGKM0();
      if (lane == 0) *(volatile LAS unsigned*)(L + L_FLG + 12) = 1;
      STAMP(1);
      asm volatile("" :: "v"(pf0));
      asm volatile("" :: "v"(pf1));
      SENTINEL(WS_QA + ((bA * 32 + (l16 >> 1) * 4 + 3) * 128 + (6 + (l16 & 1)) * 16) * 16, 48, 4);
      if (lane == 0) *(volatile LAS unsigned*)(L + L_GO) = 1;
      STAMP(2);
    } else if (wave == 0 || (wave == 1 && ffn1)) {
      const unsigned st = L_STG + wave * 16384;
      const int kq = r & 3;
      const int t1 = xg * 22 + r, cg = (t1 % 88) >> 2, mat = t1 >= 88;
      const int kq1 = wave == 0 ? kq : (t1 & 3);
      if (wave == 1) { BAR(); if (FDELAY) __builtin_amdgcn_s_sleep(FDELAY); }
      {
        const unsigned pz = q8 ^ (((r8 & 3) << 1) | (r8 >> 2));
        const u16* src; int stride;
        if (wave == 0) { stride = NH * HD; src = p.wq + (size_t)(kq * 128 + r8) * stride + hA * HD + ((r >> 2) & 1) * 64 + pz * 8; }
        else { stride = FF; src = (mat ? p.wu : p.wg) + (size_t)(kq1 * 128 + r8) * stride + min(cg * 64 + (int)pz * 8, FF - 8); }
#pragma unroll
        for (int i = 0; i < 16; ++i) GLDS(src + (size_t)i * 8 * stride, L + st + i * 1024, AUX_NT);
      }
      v4u xr[4];
      {
        const u16* xs = p.x + (l16 & 7) * DM + kq1 * 128 + g4 * 8;
#pragma unroll
        for (int s = 0; s < 4; ++s) xr[s] = *(const GAS v4u*)(xs + s * 32);
      }
      if (wave == 0) BAR();
      if (l16 >= 8) {
#pragma unroll
        for (int s = 0; s < 4; ++s) xr[s] = (v4u){0x3f803f80u, 0x3f803f80u, 0x3f803f80u, 0x3f803f80u};
      }
      lds_wait1(L + L_FLG + 12);
      const f32x2 stt = *(const LAS f32x2*)(L + L_STAT + (l16 & 7) * 8);
      VMWAIT(0);
      STAMP(1);
      f32x4 acc[4];
      {
        const unsigned rq = l16 >> 2, pc = (l16 & 3) >> 1, ph = (l16 & 1) * 8;
        const unsigned a0 = st + (8 * g4 + rq) * 128 + ((pc ^ (rq << 1)) * 16) + ph;
        const unsigned a1 = st + (8 * g4 + rq + 4) * 128 + ((pc ^ (rq << 1) ^ 1) * 16) + ph;
        v4u af[16];
#pragma unroll
        for (int nt = 0; nt < 4; ++nt)
#pragma unroll
          for (int s = 0; s < 4; ++s) af[nt * 4 + s] = tr_read2(L + (a0 ^ (nt * 32)) + s * 4096, L + (a1 ^ (nt * 32)) + s * 4096);
#pragma unroll
        for (int nt = 0; nt < 4; ++nt) {
          acc[nt] = (f32x4){0.f, 0.f, 0.f, 0.f};
#pragma unroll
          for (int s = 0; s < 4; ++s) acc[nt] = mfma16(af[nt * 4 + s], xr[s], acc[nt]);
        }
        if (lane == 0) *(volatile LAS unsigned*)(L + L_FLG + wave * 4) = 1;
      }
#pragma unroll
      for (int nt = 0; nt < 4; ++nt)
#pragma unroll
        for (int j = 0; j < 4; ++j) acc[nt][j] = (acc[nt][j] - stt.x * dppm<0x158>(acc[nt][j])) * stt.y;
      if (wave == 0) {
#pragma unroll
        for (int np = 0; np < 2; ++np) {
          const unsigned a = pk2(acc[2 * np][0], acc[2 * np][1]), b = pk2(acc[2 * np][2], acc[2 * np][3]);
          const unsigned c = dppu<0x128>(pk2(acc[2 * np + 1][0], acc[2 * np + 1][1])), d = dppu<0x128>(pk2(acc[2 * np + 1][2], acc[2 * np + 1][3]));
          const unsigned u = ((r >> 2) & 1) * 4 + 2 * np + (l16 >> 3);
          __builtin_amdgcn_raw_buffer_store_b128((v4u){l16 < 8 ? a : c, tag, l16 < 8 ? b : d, tag}, wsr,
                                                 WS_QG + ((((hA * 4 + kq) * 4 + ((l16 & 7) >> 1)) * 8 + u) * 8 + (l16 & 1) * 4 + g4) * 16, 0, AUX_SC1);
        }
      } else {
#pragma unroll
        for (int nt = 0; nt < 4; ++nt) {
          const unsigned c = dppu<0x128>(f2u(acc[nt][2])), d = dppu<0x128>(f2u(acc[nt][3]));
          __builtin_amdgcn_raw_buffer_store_b128((v4u){l16 < 8 ? f2u(acc[nt][0]) : c, tag, l16 < 8 ? f2u(acc[nt][1]) : d, tag}, wsr,
                                                 WS_GU + ((((mat * 4 + kq1) * 176 + cg * 8 + 2 * nt + (g4 >> 1)) * 8 + (l16 & 7)) * 4 + 2 * (g4 & 1) + (l16 >> 3)) * 16, 0, AUX_SC1);
        }
      }
      STAMP(2);
    } else if (wave == 2) {
      v4u wf[16], qr[16];
      {
        const u16* src = p.wuk + ((size_t)(sl * 64 + g4) * NH + hB) * HD;
#pragma unroll
        for (int n = 0; n < 16; ++n) GLDS(src + (size_t)n * 4 * NH * HD + ((l16 ^ ((n * 4 + g4) & 15)) * 8), L + L_UK + n * 1024, AUX_NT);
      }
      BAR();
      VMWAIT(0);
      {
        const unsigned a0 = L_UK + l16 * 256 + (((g4 >> 1) ^ l16) * 16) + (g4 & 1) * 8;
#pragma unroll
        for (int ct = 0; ct < 4; ++ct)
#pragma unroll
          for (int s = 0; s < 4; ++s) {
            const v2u lo = *(const LAS v2u*)(L + (a0 ^ (s * 64)) + ct * 4096), hi = *(const LAS v2u*)(L + (a0 ^ (s * 64 + 32)) + ct * 4096);
            wf[ct * 4 + s] = (v4u){lo.x, lo.y, hi.x, hi.y};
          }
        LGKM0();
        if (lane == 0) *(volatile LAS unsigned*)(L + L_FLG + 8) = 1;
      }
      STAMP(12);
      SENTINEL(WS_QG + (((hB * 4 + (q8 & 3)) * 4) * 8 + (q8 >> 2) * 4) * 8 * 16, 16, 1);
      const unsigned qoff = WS_QG + ((((hB * 4 + (l16 >> 3)) * 4 + ((l16 & 7) >> 1)) * 8) * 8 + (l16 & 1) * 4 + g4) * 16;
      GATHER(16, qr, qoff + (i >> 3) * 8192 + (i & 7) * 128);
      STAMP(1);
      const float scale = 0.08838834764831845f * 1.4426950408889634f;
#pragma unroll
      for (int cp = 0; cp < 2; ++cp) {
        f32x4 a0 = {0.f, 0.f, 0.f, 0.f}, a1 = {0.f, 0.f, 0.f, 0.f};
#pragma unroll
        for (int k = 0; k < 8; ++k) {
          const v4u bq = {qr[2 * k].x, qr[2 * k].z, qr[2 * k + 1].x, qr[2 * k + 1].z};
          a0 = mfma16(wf[cp * 8 + (k & 3)], bq, a0); a1 = mfma16(wf[cp * 8 + 4 + (k & 3)], bq, a1);
        }
        float y[4];
#pragma unroll
        for (int j = 0; j < 4; ++j) {
          const float keep = l16 < 8 ? a0[j] : a1[j], give = l16 < 8 ? a1[j] : a0[j];
          y[j] = (keep + dppm<0x128>(give)) * scale;
        }
        __builtin_amdgcn_raw_buffer_store_b128((v4u){pk2(y[0], y[1]), tag, pk2(y[2], y[3]), tag}, wsr,
                                               WS_QA + (((l16 & 7) * 32 + hB) * 128 + sl * 16 + cp * 8 + (l16 >> 3) * 4 + g4) * 16, 0, AUX_SC1);
      }
      STAMP(2);
    } else {
      BAR();
    }
    v4u ql;
    {
      v4u qr[18];
      const unsigned qoff = WS_QA + ((bA * 32 + wave * 8) * 128 + lane) * 16;
      const unsigned qoff2 = WS_QA + ((bA * 32 + (lane & 1)) * 128 + 124 + hf * 2) * 16;
      lds_wait1(L + L_GO);
      GATHER(18, qr, (i < 16 ? qoff + i * 1024 : qoff2 + (i - 16) * 16));
      STAMP(3);
      lds_wait4(L + L_FLG, 1);
#pragma unroll
      for (int i = 0; i < 16; ++i) {
        const unsigned h = wave * 8 + (i >> 1);
        const unsigned chn = (i & 1) * 32 + (lane >> 1);
        if (!((i == 1 || i == 3) && wave == 0 && lane >= 60))
          *(LAS v2u*)(L + L_S + h * 1024 + (((chn & 48) | ((chn & 15) ^ swz(h))) * 16) + (lane & 1) * 8) = (v2u){qr[i].x, qr[i].z};
      }
      ql = (v4u){qr[16].x, qr[16].z, qr[17].x, qr[17].z};
      LGKM0();
      if (lane == 0) *(volatile LAS unsigned*)(L + L_IMG + wave * 4) = 1;
    }
    tail_loads();
    lds_wait4(L + L_IMG, 1);
    STAMP(4);
    {
#pragma unroll
      for (int i = 0; i < 16; ++i) sacc[i] = 0.f;
      const unsigned tz = (hf ^ swz(l32)) * 16;
      const unsigned qb = L_S + l32 * 1024 + tz;
      const unsigned kb = L_CKV + (wave * 32 + l32) * 256 + tz;
      v4u qf[2][8], kf[2][8];
      unsigned have = lds_wait4(L + L_PROG, 1);
#pragma unroll
      for (int s = 0; s < 8; ++s) { kf[0][s] = *(const LAS v4u*)(L + (kb ^ (s * 32))); qf[0][s] = *(const LAS v4u*)(L + (qb ^ (s * 32))); }
#pragma unroll
      for (int blk = 0; blk < 4; ++blk) {
        if (blk < 3) {
          if (have < (unsigned)blk + 2) {
#pragma unroll
            for (int s = 0; s < 8; ++s) sacc = mfma32(kf[blk & 1][s], qf[blk & 1][s], sacc);
            have = lds_wait4(L + L_PROG, blk + 2);
#pragma unroll
            for (int s = 0; s < 8; ++s) {
              kf[(blk + 1) & 1][s] = *(const LAS v4u*)(L + (kb ^ (s * 32)) + (blk + 1) * 32768);
              qf[(blk + 1) & 1][s] = *(const LAS v4u*)(L + (qb ^ (s * 32)) + (blk + 1) * 256);
            }
          } else {
#pragma unroll
            for (int s = 0; s < 8; ++s) {
              kf[(blk + 1) & 1][s] = *(const LAS v4u*)(L + (kb ^ (s * 32)) + (blk + 1) * 32768);
              qf[(blk + 1) & 1][s] = *(const LAS v4u*)(L + (qb ^ (s * 32)) + (blk + 1) * 256);
              sacc = mfma32(kf[blk & 1][s], qf[blk & 1][s], sacc);
            }
          }
          if (blk == 0 && lane == 0) *(volatile LAS unsigned*)(L + L_IMG + wave * 4) = 2;
        } else {
          if (l32 < 2) qf[1][7] = ql;
#pragma unroll
          for (int s = 0; s < 8; ++s) sacc = mfma32(kf[1][s], qf[1][s], sacc);
        }
      }
    }
    STAMP(5);
    float mx = fmaxf(fmaxf(fmaxf(sacc[0], sacc[1]), fmaxf(sacc[2], sacc[3])), fmaxf(fmaxf(sacc[4], sacc[5]), fmaxf(sacc[6], sacc[7])));
    mx = fmaxf(mx, fmaxf(fmaxf(fmaxf(sacc[8], sacc[9]), fmaxf(sacc[10], sacc[11])), fmaxf(fmaxf(sacc[12], sacc[13]), fmaxf(sacc[14], sacc[15]))));
    mx = xmax32(mx);
    lds_wait4(L + L_IMG, 2);
    if (lane < 32) *(LAS float*)(L + L_S + lane * 1024 + swz(lane) * 16 + wave * 4) = mx;
  }
  BAR();
  const unsigned zl = swz(l32);
  if (wave < 4) {
    const f32x4 a = *(const LAS f32x4*)(L + L_S + l32 * 1024 + zl * 16);
    m = fmaxf(fmaxf(a[0], a[1]), fmaxf(a[2], a[3]));
    float ls = 0.f;
#pragma unroll
    for (int q = 0; q < 4; ++q) {
      const unsigned p0 = pk2(__builtin_amdgcn_exp2f(sacc[4 * q] - m), __builtin_amdgcn_exp2f(sacc[4 * q + 1] - m));
      const unsigned p1 = pk2(__builtin_amdgcn_exp2f(sacc[4 * q + 2] - m), __builtin_amdgcn_exp2f(sacc[4 * q + 3] - m));
      *(LAS v2u*)(L + L_S + l32 * 1024 + 256 + (((wave * 4 + q) ^ zl) * 16) + hf * 8) = (v2u){p0, p1};
      ls = dot_ones(p1, dot_ones(p0, ls));
    }
    ls = xsum32(ls);
    if (lane < 32) *(LAS float*)(L + L_S + lane * 1024 + 512 + zl * 16 + wave * 4) = ls;
  }
  BAR();
  const unsigned wb = L_CKV + (wave >> 1) * 32768;
  const unsigned wq8 = (wave & 1) * 8;
  f32x16 o0, o1;
  float lsum;
  {
    v4u pf[8];
    {
      const unsigned pbs = L_S + l32 * 1024 + 256 + (hf ^ zl) * 16;
#pragma unroll
      for (int ks = 0; ks < 8; ++ks) pf[ks] = *(const LAS v4u*)(L + (pbs ^ (ks * 32)));
    }
#pragma unroll
    for (int i = 0; i < 16; ++i) { o0[i] = 0.f; o1[i] = 0.f; }
    const unsigned blk = (lane >> 4) & 1, q = (lane & 15) >> 2, pp = lane & 3;
    unsigned ta[2];
#pragma unroll
    for (int tt = 0; tt < 2; ++tt) {
      const unsigned row = 8 * hf + 4 * tt + q;
      ta[tt] = wb + row * 256 + (((wq8 + 2 * blk + (pp >> 1)) ^ swz(row)) * 16) + 8 * (pp & 1);
    }
#pragma unroll
    for (int ks = 0; ks < 8; ++ks) {
      o0 = mfma32(tr_read2(L + ta[0] + ks * 4096, L + ta[1] + ks * 4096), pf[ks], o0);
      o1 = mfma32(tr_read2(L + (ta[0] ^ 64) + ks * 4096, L + (ta[1] ^ 64) + ks * 4096), pf[ks], o1);
    }
    const f32x4 a = *(const LAS f32x4*)(L + L_S + l32 * 1024 + 512 + zl * 16);
    lsum = (a[0] + a[1]) + (a[2] + a[3]);
  }
  STAMP(6);
  const unsigned sc_e = wb + r8 * 256 + (((wq8 + q8) ^ swz(r8)) * 16);
  const unsigned sc_o = wb + (r8 + 8) * 256 + (((wq8 + q8) ^ swz(r8 + 8)) * 16);
  {
    const float inv = __builtin_amdgcn_rcpf(lsum);
    o0 *= inv; o1 *= inv;
    const unsigned wa = wb + l32 * 256 + (((wq8 + hf) ^ zl) * 16);
#pragma unroll
    for (int gp = 0; gp < 4; ++gp) {
      const f32x16& o = gp < 2 ? o0 : o1;
      const int b8 = (gp & 1) * 8;
      auto rx = __builtin_amdgcn_permlane32_swap(pk2(o[b8 + 0], o[b8 + 1]), pk2(o[b8 + 4], o[b8 + 5]), false, false);
      auto ry = __builtin_amdgcn_permlane32_swap(pk2(o[b8 + 2], o[b8 + 3]), pk2(o[b8 + 6], o[b8 + 7]), false, false);
      *(LAS v4u*)(L + (wa ^ (gp * 32))) = (v4u){rx[0], ry[0], rx[1], ry[1]};
    }
    LGKM0();
    const unsigned pbase = WS_PART + ((((bA * 32 + r8) * 8 + wave) * 32 + chunk) * 64 + q8 * 8) * 2;
#pragma unroll
    for (int i = 0; i < 4; ++i) {
      const v4u t = *(const LAS v4u*)(L + ((i & 1) ? sc_o : sc_e) + (i >> 1) * 4096);
      __builtin_amdgcn_raw_buffer_store_b128(t, wsr, pbase + i * (8 * 8 * 32 * 128), 0, AUX_SC1);
    }
    *(LAS v4u*)(L + sc_e + 8192) = wo2[0];
    *(LAS v4u*)(L + sc_o + 8192) = wo2[1];
    *(LAS v4u*)(L + sc_e + 12288) = wv2[0];
    *(LAS v4u*)(L + sc_o + 12288) = wv2[1];
    *(LAS v4u*)(L + sc_e + 16384) = wd1;
    VMWAIT(0);
    BAR();
    if (wave == 0 && lane < 32)
      __builtin_amdgcn_raw_buffer_store_b64((v2u){f2u(m + __builtin_amdgcn_logf(lsum)), tag}, wsr,
                                            WS_FL + ((((lane >> 2) * 8 + bA) * 32 + chunk) * 4 + (lane & 3)) * 8, 0, AUX_SC1);
    STAMP(7);
  }
  const unsigned pc = (l16 & 3) >> 1, ph = (l16 & 1) * 8, rq = l16 >> 2;
  v4u av[2], ao[4];
  f32x4 accE = {0.f, 0.f, 0.f, 0.f};
  {
    const unsigned i8 = (wave >> 2) * 8, pcs = (g4 & 1) * 8 + 2 * (wave & 3) + pc;
    const unsigned b0 = (g4 >> 1) * 32768 + ph;
    const unsigned v0 = b0 + (48 + i8 + rq) * 256 + ((pcs ^ swz(i8 + rq)) * 16), v1 = b0 + (52 + i8 + rq) * 256 + ((pcs ^ swz(i8 + rq + 4)) * 16);
    av[0] = tr_read2(L + v0, L + v1);
    av[1] = tr_read2(L + v0 + 65536, L + v1 + 65536);
    if (wave < 4) {
      v4u hr[4];
      const unsigned rin = 8 * (g4 & 1) + rq, pco = (g4 >> 1) * 8 + 2 * wave + pc;
      const unsigned o0a = (32 + rin) * 256 + ((pco ^ swz(rin)) * 16) + ph, o1a = (36 + rin) * 256 + ((pco ^ swz(rin + 4)) * 16) + ph;
#pragma unroll
      for (int ks = 0; ks < 4; ++ks) ao[ks] = tr_read2(L + o0a + ks * 32768, L + o1a + ks * 32768);
      const unsigned d0 = b0 + (64 + rq) * 256 + ((pcs ^ swz(rq)) * 16), d1 = b0 + (68 + rq) * 256 + ((pcs ^ swz(rq + 4)) * 16);
      const v4u ad0 = tr_read2(L + d0, L + d1), ad1 = tr_read2(L + d0 + 65536, L + d1 + 65536);
      const bool on = 4 + g4 < ntask;
      const unsigned h0 = WS_HM + ((task0 + g4) * 8 + (l16 & 7)) * 32, h1 = WS_HM + ((task0 + (on ? 4 + g4 : 0)) * 8 + (l16 & 7)) * 32;
      for (unsigned spin = 0; spin < SPIN_CAP; ++spin) {
        hr[0] = __builtin_amdgcn_raw_buffer_load_b128(wsr, h0, 0, AUX_SC1);
        hr[1] = __builtin_amdgcn_raw_buffer_load_b128(wsr, h0 + 16, 0, AUX_SC1);
        hr[2] = __builtin_amdgcn_raw_buffer_load_b128(wsr, h1, 0, AUX_SC1);
        hr[3] = __builtin_amdgcn_raw_buffer_load_b128(wsr, h1 + 16, 0, AUX_SC1);
        if (__all((hr[0].y == tag) & (hr[0].w == tag) & (hr[1].y == tag) & (hr[1].w == tag) & (hr[2].y == tag) & (hr[2].w == tag) & (hr[3].y == tag) & (hr[3].w == tag))) break;
        __builtin_amdgcn_s_sleep(1);
      }
      accE = mfma16(ad0, (v4u){hr[0].x, hr[0].z, hr[1].x, hr[1].z}, accE);
      accE = mfma16(ad1, on ? (v4u){hr[2].x, hr[2].z, hr[3].x, hr[3].z} : (v4u){0u, 0u, 0u, 0u}, accE);
    }
  }
  {
    v2u fr;
    const unsigned fo = WS_FL + (((xg * 8 + wave) * 32 + l32) * 4 + (hA & 3)) * 8;
    for (unsigned spin = 0; spin < SPIN_CAP; ++spin) {
      fr = __builtin_amdgcn_raw_buffer_load_b64(wsr, fo, 0, AUX_SC1);
      if (__all(fr.y == tag)) break;
      __builtin_amdgcn_s_sleep(1);
    }
    asm volatile("" ::: "memory");
    STAMP(8);
    v2u pr[8];
    const unsigned po = WS_PART + ((((wave * 32 + hA) * 8 + sl) * 32 + g4) * 64 + l16 * 4) * 2;
#pragma unroll
    for (int u = 0; u < 8; ++u) pr[u] = __builtin_amdgcn_raw_buffer_load_b64(wsr, po + u * 512, 0, AUX_SC1);
    const float lse = u2f(fr.x);
    float M = lse;
    M = fmaxf(M, dppm<0x121>(M)); M = fmaxf(M, dppm<0x122>(M)); M = fmaxf(M, dppm<0x124>(M)); M = fmaxf(M, dppm<0x128>(M));
    M = xmax16(M);
    const float wj = bflo(pk2(__builtin_amdgcn_exp2f(lse - M), 0.f));
    float Ls = wj;
    Ls += dppm<0x121>(Ls); Ls += dppm<0x122>(Ls); Ls += dppm<0x124>(Ls); Ls += dppm<0x128>(Ls);
    Ls = xsum16(Ls);
    const float inv = __builtin_amdgcn_rcpf(Ls);
    const unsigned e4 = l16 & 3;
    const unsigned sh = (e4 & 1) ? 0u : 16u, k0 = e4 < 2 ? ~0u : 0u;
    unsigned wa[16];
#pragma unroll
    for (int u = 0; u < 8; ++u) {
      const unsigned w = (unsigned)__builtin_amdgcn_ds_bpermute((g4 + 4 * u) * 4, f2u(wj)) >> sh;
      wa[2 * u] = w & k0; wa[2 * u + 1] = w & ~k0;
    }
    f32x4 acc = {0.f, 0.f, 0.f, 0.f};
#pragma unroll
    for (int u = 0; u < 4; ++u)
      acc = mfma16((v4u){wa[4 * u], wa[4 * u + 1], wa[4 * u + 2], wa[4 * u + 3]}, (v4u){pr[2 * u].x, pr[2 * u].y, pr[2 * u + 1].x, pr[2 * u + 1].y}, acc);
    if (lane < 16) *(LAS v2u*)(L + L_CL + wave * 1040 + lane * 8) = (v2u){pk2(acc[0] * inv, acc[1] * inv), pk2(acc[2] * inv, acc[3] * inv)};
    BAR();
  }
  const unsigned one = (l16 & 1) ? 0x3f800000u : 0x00003f80u;
  const unsigned s0 = (l16 & 2) ? 0u : one, s1 = (l16 & 2) ? one : 0u;
  {
    const v4u b0 = *(const LAS v4u*)(L + L_CL + (l16 & 7) * 1040 + g4 * 16), b1 = *(const LAS v4u*)(L + L_CL + (l16 & 7) * 1040 + 64 + g4 * 16);
    f32x4 acc = {0.f, 0.f, 0.f, 0.f};
    acc = mfma16(av[0], b0, acc);
    acc = mfma16(av[1], b1, acc);
    if (l16 < 8)
      __builtin_amdgcn_raw_buffer_store_b128((v4u){pk2(acc[0], acc[1]), tag, pk2(acc[2], acc[3]), tag}, wsr,
                                             WS_CX + ((((hA * 8 + wave) * 8 + sl) * 8 + l16) * 4 + g4) * 16, 0, AUX_SC1);
    STAMP(9);
    v4u cr[4];
    const unsigned co = WS_CX + ((hB * 8 + wave) * 256 + lane) * 16;
    for (unsigned spin = 0; spin < SPIN_CAP; ++spin) {
#pragma unroll
      for (int i = 0; i < 4; ++i) cr[i] = __builtin_amdgcn_raw_buffer_load_b128(wsr, co + i * 1024, 0, AUX_SC1);
      if (__all((cr[0].y == tag) & (cr[0].w == tag) & (cr[1].y == tag) & (cr[1].w == tag) & (cr[2].y == tag) & (cr[2].w == tag) & (cr[3].y == tag) & (cr[3].w == tag))) break;
      __builtin_amdgcn_s_sleep(1);
    }
    STAMP(10);
    {
      const bool on = ((l16 >> 2) & 1) == (g4 & 1);
      const unsigned t0 = on ? s0 : 0u, t1 = on ? s1 : 0u;
      f32x4 a = {0.f, 0.f, 0.f, 0.f};
      a = mfma16((v4u){t0, t1, t0, t1}, (v4u){cr[0].x, cr[0].z, cr[1].x, cr[1].z}, a);
      a = mfma16((v4u){t0, t1, t0, t1}, (v4u){cr[2].x, cr[2].z, cr[3].x, cr[3].z}, a);
      if (lane < 32) *(LAS v2u*)(L + L_CX + (lane >> 2) * 272 + wave * 32 + (lane & 3) * 8) = (v2u){pk2(a[0], a[1]), pk2(a[2], a[3])};
    }
    BAR();
  }
  if (wave < 4) {
#pragma unroll
    for (int ks = 0; ks < 4; ++ks) accE = mfma16(ao[ks], *(const LAS v4u*)(L + L_CX + (l16 & 7) * 272 + ks * 64 + g4 * 16), accE);
    if (l16 < 8)
      __builtin_amdgcn_raw_buffer_store_b128((v4u){pk2(accE[0], accE[1]), tag, pk2(accE[2], accE[3]), tag}, wsr,
                                             WS_OG + ((((sl * 4 + wave) * 32 + hB) * 8 + l16) * 4 + g4) * 16, 0, AUX_SC1);
    STAMP(11);
  } else if (wave == 4) {
    const unsigned oo = WS_OG + ((((wg & 31) * 32 + (lane >> 2)) * 8 + (wg >> 5)) * 4 + (lane & 3)) * 16;
    const v2u xw = *(const GAS v2u*)(p.x + wg * 16 + (lane & 3) * 4);
    v4u r0, r1;
    for (unsigned spin = 0; spin < SPIN_CAP; ++spin) {
      r0 = __builtin_amdgcn_raw_buffer_load_b128(wsr, oo, 0, AUX_SC1);
      r1 = __builtin_amdgcn_raw_buffer_load_b128(wsr, oo + 16 * 512, 0, AUX_SC1);
      if (__all((r0.y == tag) & (r0.w == tag) & (r1.y == tag) & (r1.w == tag))) break;
      __builtin_amdgcn_s_sleep(1);
    }
    f32x4 acc = {0.f, 0.f, 0.f, 0.f};
    acc = mfma16((v4u){s0, s1, s0, s1}, (v4u){r0.x, r0.z, r1.x, r1.z}, acc);
#pragma unroll
    for (int j = 0; j < 4; ++j) { acc[j] += dppm<0x124>(acc[j]); acc[j] += dppm<0x128>(acc[j]); }
    if (lane < 4)
      *(GAS v2u*)(p.out + wg * 16 + lane * 4) = (v2u){pk2(bflo(xw.x) + acc[0], bfhi(xw.x) + acc[1]), pk2(bflo(xw.y) + acc[2], bfhi(xw.y) + acc[3])};
    if (wg == 0 && lane == 0) __builtin_amdgcn_raw_buffer_store_b32(tag, wsr, WS_EPOCH, 0, AUX_SC1);
    STAMP(11);
    {
      LGKM0();
      const unsigned ts = __builtin_amdgcn_readfirstlane(*(volatile LAS unsigned*)(L + L_GO + 8));
      const unsigned now = (unsigned)__builtin_amdgcn_s_memrealtime();
      unsigned dur = ts - dt0;
      if (dur > 5000) dur = 0;
      for (unsigned spin = 0; spin < 1000000; ++spin) { if ((unsigned)__builtin_amdgcn_s_memrealtime() - now >= dur) break; }
    }
  }
}

extern "C" void kernel_launch(void* const* d_in, const int* in_sizes, int n_in, void* d_out, int out_size, void* d_ws, size_t ws_size,
                              hipStream_t stream) {
  Params p;
  p.x = (const u16*)d_in[0]; p.ckv = (const u16*)d_in[1]; p.wq = (const u16*)d_in[2]; p.wuk = (const u16*)d_in[3];
  p.wuv = (const u16*)d_in[4]; p.wo = (const u16*)d_in[5]; p.wg = (const u16*)d_in[7]; p.wu = (const u16*)d_in[8]; p.wd = (const u16*)d_in[9];
  p.out = (u16*)d_out; p.ws = (unsigned char*)d_ws;
  (void)hipFuncSetAttribute((const void*)fused_kernel, hipFuncAttributeMaxDynamicSharedMemorySize, LDS_BYTES);
  fused_kernel<<<NWG, NTHR, LDS_BYTES, stream>>>(p);
}
```

```cpp
#include <hip/hip_runtime.h>
#include <hip/hip_bf16.h>
#include <stdint.h>

#define STAMPS 0
#define CODE_LINES 160
#define WINS 16
#define WINF 20
#define WIN0 6
#define ADELAY 120
#define PDELAY 60
#define CDELAY 8
#define ODELAY 8
#define SDELAY 0
#define WMAX (WINS > WINF ? WINS : WINF)

typedef unsigned v4u __attribute__((ext_vector_type(4)));
typedef unsigned v2u __attribute__((ext_vector_type(2)));
typedef float f32x2 __attribute__((ext_vector_type(2)));
typedef float f32x4 __attribute__((ext_vector_type(4)));
typedef float f32x16 __attribute__((ext_vector_type(16)));
typedef short bf16x8 __attribute__((ext_vector_type(8)));
typedef short s16x4 __attribute__((ext_vector_type(4)));
typedef unsigned short u16x2 __attribute__((ext_vector_type(2)));
typedef __bf16 bf16x2 __attribute__((ext_vector_type(2)));
typedef unsigned short u16;
#define LAS __attribute__((address_space(3)))
#define GAS __attribute__((address_space(1)))

constexpr int NB = 8, NH = 32, SK = 4096, HD = 128, DM = 512, DC = 512, FF = 1376;
constexpr int NWG = 256, NTHR = 512;
constexpr unsigned WS_EPOCH = 0;
constexpr unsigned WS_QF = 4096;
constexpr unsigned WS_PF = WS_QF + 65536;
constexpr unsigned WS_QA = WS_PF + 65536;
constexpr unsigned WS_GU = WS_QA + 2 * 8 * 32 * 1024;
constexpr unsigned WS_HM = WS_GU + 8 * 176 * 32 * 16;
constexpr unsigned WS_FL = WS_HM + 176 * 8 * 2 * 16;
constexpr unsigned WS_CX = WS_FL + 8 * 8 * 32 * 4 * 16;
constexpr unsigned WS_OG = WS_CX + 32 * 8 * 8 * 8 * 4 * 16;
constexpr unsigned WS_PART = 4u << 20;
constexpr unsigned WS_END = WS_PART + (8u << 20);
constexpr unsigned WS_STAMP = 32u << 20;
static_assert(WS_OG + 32 * 32 * 8 * 4 * 16 <= WS_PART, "ws map");
constexpr unsigned L_CKV = 0;
constexpr unsigned L_S = 128 * 1024;
constexpr unsigned L_SY = L_S + 24576 + 224;
constexpr unsigned L_QP = L_S;
constexpr unsigned L_LN = L_S + 8192;
constexpr unsigned L_DUMP = L_S + 9216;
constexpr unsigned L_X = L_S + 16384;
constexpr unsigned L_P = L_S;
constexpr unsigned L_ST = L_S + 8192;
constexpr unsigned L_CL = L_S + 16384;
constexpr unsigned L_CX = L_S + 16384 + 2048;
constexpr unsigned LDS_BYTES = 160 * 1024;
constexpr int AUX_NT = 2, AUX_SC1 = 16;
constexpr unsigned SPIN_CAP = 1u << 18;

struct Params {
  const u16 *x, *ckv, *wq, *wuk, *wuv, *wo, *wg, *wu, *wd;
  u16* out;
  unsigned char* ws;
};

__device__ __forceinline__ float u2f(unsigned u) { return __uint_as_float(u); }
__device__ __forceinline__ unsigned f2u(float f) { return __float_as_uint(f); }
__device__ __forceinline__ float bflo(unsigned w) { return u2f(w << 16); }
__device__ __forceinline__ float bfhi(unsigned w) { return u2f(w & 0xffff0000u); }
__device__ __forceinline__ unsigned pk2(float lo, float hi) {
  f32x2 f = {lo, hi};
  return __builtin_bit_cast(unsigned, __builtin_convertvector(f, bf16x2));
}
__device__ __forceinline__ float dot_ones(unsigned a, float c) {
  return __builtin_amdgcn_fdot2_f32_bf16(__builtin_bit_cast(bf16x2, a), __builtin_bit_cast(bf16x2, 0x3f803f80u), c, false);
}
__device__ __forceinline__ unsigned pksubsat(unsigned a, unsigned b) {
  return __builtin_bit_cast(unsigned, __builtin_elementwise_sub_sat(__builtin_bit_cast(u16x2, a), __builtin_bit_cast(u16x2, b)));
}
__device__ __forceinline__ f32x4 mfma16(v4u a, v4u b, f32x4 c) {
  return __builtin_amdgcn_mfma_f32_16x16x32_bf16(__builtin_bit_cast(bf16x8, a), __builtin_bit_cast(bf16x8, b), c, 0, 0, 0);
}
__device__ __forceinline__ f32x16 mfma32(v4u a, v4u b, f32x16 c) {
  return __builtin_amdgcn_mfma_f32_32x32x16_bf16(__builtin_bit_cast(bf16x8, a), __builtin_bit_cast(bf16x8, b), c, 0, 0, 0);
}
__device__ __forceinline__ unsigned swz(unsigned row) { return ((row & 3) << 2) | ((row >> 2) & 3); }
#define BAR() do { asm volatile("s_waitcnt lgkmcnt(0)" ::: "memory"); __builtin_amdgcn_s_barrier(); asm volatile("" ::: "memory"); } while (0)
#define VMWAIT(n) do { asm volatile("" ::: "memory"); __builtin_amdgcn_s_waitcnt(0x0f70 | ((n) & 15) | (((n) >> 4) << 14)); asm volatile("" ::: "memory"); } while (0)
#define LGKM0() asm volatile("s_waitcnt lgkmcnt(0)" ::: "memory")
#define GLDS_(OP, sbase, voff, dst) asm volatile("s_mov_b32 m0, %2\n\ts_nop 0\n\t" OP :: "v"((unsigned)(voff)), "s"((const void*)(sbase)), "s"((unsigned)(dst)) : "memory", "m0")
#define GLDS(sbase, voff, dst) GLDS_("global_load_lds_dwordx4 %0, %1 nt", sbase, voff, dst)
#define GLDSX(sbase, voff, dst) GLDS_("global_load_lds_dwordx4 %0, %1", sbase, voff, dst)
#define GLDS4(sbase, voff, dst) GLDS_("global_load_lds_dword %0, %1 nt", sbase, voff, dst)
#define WLOAD(p) __builtin_nontemporal_load(p)
#define LBYTE(off, v) (*(volatile LAS unsigned char*)(L + L_SY + (off)) = (v))
__device__ __forceinline__ v2u tr_read(LAS unsigned char* a) {
  return __builtin_bit_cast(v2u, __builtin_amdgcn_ds_read_tr16_b64_v4i16((LAS s16x4*)a));
}
__device__ __forceinline__ v4u tr_read2(LAS unsigned char* a, LAS unsigned char* b) {
  const v2u x = tr_read(a), y = tr_read(b);
  return (v4u){x.x, x.y, y.x, y.y};
}
template <int CTRL>
__device__ __forceinline__ unsigned dppu(unsigned v) { return __builtin_amdgcn_update_dpp(v, v, CTRL, 0xf, 0xf, false); }
template <int CTRL>
__device__ __forceinline__ float dppm(float v) { return u2f(dppu<CTRL>(f2u(v))); }
__device__ __forceinline__ float xsum16(float v) {
  auto s = __builtin_amdgcn_permlane16_swap(f2u(v), f2u(v), false, false);
  return u2f(s[0]) + u2f(s[1]);
}
__device__ __forceinline__ float xmax16(float v) {
  auto s = __builtin_amdgcn_permlane16_swap(f2u(v), f2u(v), false, false);
  return fmaxf(u2f(s[0]), u2f(s[1]));
}
__device__ __forceinline__ float xsum32(float v) {
  auto s = __builtin_amdgcn_permlane32_swap(f2u(v), f2u(v), false, false);
  return u2f(s[0]) + u2f(s[1]);
}
__device__ __forceinline__ float xmax32(float v) {
  auto s = __builtin_amdgcn_permlane32_swap(f2u(v), f2u(v), false, false);
  return fmaxf(u2f(s[0]), u2f(s[1]));
}
__device__ __forceinline__ bool rec_ok(v4u r, unsigned tag) { return __all((r.y == tag) & (r.w == tag)); }
__device__ __forceinline__ bool flg_ok(v4u r, unsigned tag) { return __all((r.x == tag) & (r.y == tag) & (r.z == tag) & (r.w == tag)); }
__device__ __forceinline__ unsigned lds_wait(LAS unsigned char* a, unsigned mask) {
  unsigned w = 0;
#pragma nounroll
  for (unsigned spin = 0; spin < (SPIN_CAP << 4); ++spin) {
    w = __builtin_amdgcn_readfirstlane(*(volatile LAS unsigned*)a);
    if ((w & mask) == mask) break;
    __builtin_amdgcn_s_sleep(1);
  }
  asm volatile("" ::: "memory");
  return w;
}

__device__ __forceinline__ unsigned long long lds_peek64(LAS unsigned char* a) {
  const v2u w = *(volatile LAS v2u*)a;
  return (unsigned long long)__builtin_amdgcn_readfirstlane(w.x) | ((unsigned long long)__builtin_amdgcn_readfirstlane(w.y) << 32);
}
__device__ __forceinline__ unsigned long long lds_wait64(LAS unsigned char* a, unsigned long long mask) {
  unsigned long long w = 0;
#pragma nounroll
  for (unsigned spin = 0; spin < (SPIN_CAP << 4); ++spin) {
    w = lds_peek64(a);
    if ((w & mask) == mask) break;
    __builtin_amdgcn_s_sleep(1);
  }
  asm volatile("" ::: "memory");
  return w;
}
__device__ __forceinline__ unsigned lds_ticket(LAS unsigned char* a, int lane) {
  unsigned t = 0;
  if (lane == 0) t = __hip_atomic_fetch_add((LAS unsigned*)a, 1u, __ATOMIC_RELAXED, __HIP_MEMORY_SCOPE_WORKGROUP);
  return __builtin_amdgcn_readfirstlane(t);
}
__device__ __forceinline__ unsigned addbf(unsigned a, unsigned b, unsigned k) {
  const float lo = __builtin_amdgcn_fdot2_f32_bf16(__builtin_bit_cast(bf16x2, b), __builtin_bit_cast(bf16x2, k), bflo(a), false);
  const float hi = __builtin_amdgcn_fdot2_f32_bf16(__builtin_bit_cast(bf16x2, b), __builtin_bit_cast(bf16x2, k << 16), bfhi(a), false);
  return pk2(lo, hi);
}
__device__ __forceinline__ v4u addbf4(v4u a, v4u b, unsigned k) { return (v4u){addbf(a.x, b.x, k), addbf(a.y, b.y, k), addbf(a.z, b.z, k), addbf(a.w, b.w, k)}; }

#define STAMP(k) do { } while (0)

#define GLOAD(ADDR) __builtin_amdgcn_raw_buffer_load_b128(wsr, (ADDR), 0, AUX_SC1)
#define GFINISH(N, qr, ADDR, SLP)                                                                            \
  do {                                                                                                       \
    unsigned done = 0;                                                                                       \
    _Pragma("nounroll") for (unsigned spin = 0; spin < SPIN_CAP; ++spin) {                                   \
      _Pragma("unroll") for (int i = 0; i < N; ++i)                                                          \
        if (!((done >> i) & 1)) { if (rec_ok(qr[i], tag)) done |= 1u << i; }                                 \
      if (done == (1u << N) - 1u) break;                                                                     \
      if (SLP) __builtin_amdgcn_s_sleep(SLP);                                                                \
      _Pragma("unroll") for (int i = 0; i < N; ++i)                                                          \
        if (!((done >> i) & 1)) qr[i] = GLOAD(ADDR);                                                         \
    }                                                                                                        \
  } while (0)
#define GSTART(N, qr, ADDR) do { _Pragma("unroll") for (int i = 0; i < N; ++i) qr[i] = GLOAD(ADDR); } while (0)

__global__ void __launch_bounds__(NTHR, 2) fused_kernel(Params p) {
  extern __shared__ __attribute__((aligned(16))) unsigned char lds_raw[];
  LAS unsigned char* L = (LAS unsigned char*)lds_raw;
  const int tid = threadIdx.x, lane = tid & 63;
  const int wave = __builtin_amdgcn_readfirstlane(tid >> 6);
  const int wg = blockIdx.x, xg = wg & 7, r = wg >> 3;
  const int bA = xg, chunk = r;
  const int hA = xg * 4 + (r >> 3), sl = r & 7;
  const int hB = ((xg + 1) & 7) * 4 + (r >> 3);
  const bool clsF = r & 1;
  const int sidx = (r >> 1) * 8 + xg;
#define RP 0
  const int rf = (r + RP) & 31;
  const int hF = xg * 4 + (rf >> 3), dh = (rf >> 2) & 1, ch = (rf >> 1) & 1;
  const int ntask = hB < 12 ? 6 : 5, task0 = hB < 12 ? 6 * hB : 12 + 5 * hB;
  const __amdgpu_buffer_rsrc_t wsr = __builtin_amdgcn_make_buffer_rsrc((void*)p.ws, 0, WS_END, 0x00020000);
  const int l16 = lane & 15, g4 = lane >> 4;
  const int l32 = lane & 31, hf = lane >> 5;
  const unsigned r8 = lane >> 3, q8 = lane & 7;
  STAMP(0);
  unsigned long long pc0;
  asm volatile("s_getpc_b64 %0" : "=s"(pc0));
  unsigned tag, tagv;
#define TAGLOAD() tagv = __builtin_amdgcn_raw_buffer_load_b32(wsr, WS_EPOCH, 0, 0)
#define TAGSET() tag = __builtin_amdgcn_readfirstlane(tagv) + 1u
  if (wave == 5) {
#pragma unroll
    for (int i = 0; i < 8; ++i) GLDSX(p.x + i * DM, (lane ^ (2 * i)) * 16, L_X + i * 1024);
    STAMP(4);
  } else if (wave == 4) {
    if (lane < 8) *(LAS unsigned*)(L + L_SY + lane * 4) = (lane == 3 && !clsF) ? (sidx >= 48 ? 0x07070700 : 0x07070000) : ((lane == 0 && !clsF) ? 0x01000000 : 0);
  } else if (wave == 6) {
    GLDS4((const void*)(pc0 & ~127ull), min((unsigned)((r & 3) * 64 + lane), (unsigned)CODE_LINES - 1u) * 128, L_DUMP + 2048);
  } else if (wave == 7) {
    GLDS4(p.ws, lane < 4 ? WS_PART + lane * (2u << 20) : (lane == 4 ? 2048 : WS_OG), L_DUMP + 2304);
    GLDS4(p.wuv + ((size_t)(sl * 64) * NH + hA) * HD, 0, L_DUMP + 2304);
    GLDS4(p.wo + (size_t)hB * HD * DM, 0, L_DUMP + 2304);
    GLDS4(p.wd, 0, L_DUMP + 2304);
    GLDS4(p.out, 0, L_DUMP + 2304);
  } else if (!clsF) {
    const size_t po = (size_t)((r >> 1) * 32 + wave * 8) * (NH * HD) + xg * 512;
    GLDS4(p.wq + po, r8 * 8192 + q8 * 128, L_DUMP + wave * 512);
    GLDS4(p.wuk + po, r8 * 8192 + q8 * 128, L_DUMP + wave * 512 + 256);
  }
  v4u tw[10];
  auto tail_loads = [&]() {
#pragma unroll
    for (int e = 0; e < 2; ++e) {
      const int u = (wave & 3) + 4 * e;
      const u16* so = p.wo + ((size_t)hB * HD + u * 16 + r8) * DM + sl * 64 + q8 * 8;
      tw[5 * e + 0] = WLOAD((const GAS v4u*)so);
      tw[5 * e + 1] = WLOAD((const GAS v4u*)(so + 8 * DM));
      const u16* sv = p.wuv + ((size_t)(sl * 64 + u * 8 + r8) * NH + hA) * HD + q8 * 8;
      tw[5 * e + 2] = WLOAD((const GAS v4u*)sv);
      tw[5 * e + 3] = WLOAD((const GAS v4u*)(sv + 64));
      const int fr = min(u * 8 + (int)r8, ntask * 8 - 1);
      tw[5 * e + 4] = WLOAD((const GAS v4u*)(p.wd + (size_t)(task0 * 8 + fr) * DM + sl * 64 + q8 * 8));
    }
  };
  const bool hon = 4 + g4 < ntask;
  const unsigned h0 = WS_HM + ((task0 + g4) * 8 + (l16 & 7)) * 32, h1 = WS_HM + ((task0 + (hon ? 4 + g4 : 0)) * 8 + (l16 & 7)) * 32;
  const unsigned zl = swz(l32);

  if (wave >= 4) {
    bool early = false;
    if (wave >= 6) {
      const int v = wave - 6;
      const u16* srcb = p.ckv + ((size_t)bA * SK + chunk * 128 + 4 * v) * DC;
      const unsigned vo0 = g4 * 1024 + (l16 ^ ((g4 << 2) | v)) * 16, vo1 = g4 * 1024 + (l16 ^ ((g4 << 2) | (2 + v))) * 16;
      BAR();
      int mode = clsF ? 0 : 1;
      lds_wait(L + L_SY + 12, 0x01010101);
      lds_wait(L + L_SY, 0x01000000);
      if (SDELAY && !clsF) __builtin_amdgcn_s_sleep(SDELAY);
      STAMP(3);
#pragma nounroll
      for (int blk = 0; blk < 4; ++blk) {
        if (blk == 1 && clsF) lds_wait(L + L_SY + 12, 0x02020202);
        if (blk == 3) lds_wait(L + L_SY + 12, 0x04040404);
#pragma unroll
        for (int n = 0; n < 16; ++n) {
          GLDS(srcb + n * 8 * DC + blk * 128, (n & 1) ? vo1 : vo0, L_CKV + blk * 32768 + (n * 2 + v) * 1024);
          if (mode == 0) {
            VMWAIT(WIN0 - 1);
            if (__builtin_amdgcn_readfirstlane(*(volatile LAS unsigned*)(L + L_SY + 20)) & 0x10000) mode = 2;
          } else if (mode == 1) VMWAIT(WINS - 1);
          else VMWAIT(WINF - 1);
          if (n == ((WMAX - 2) & 15) && blk > (WMAX - 2) / 16) LBYTE(v, (1 << (blk - (WMAX - 2) / 16)) - 1);
        }
      }
      STAMP(1);
      early = __builtin_amdgcn_readfirstlane(*(volatile LAS unsigned*)(L + L_SY + 4)) == 0x01010101u;
      if (!early) {
        VMWAIT(16); LBYTE(v, 7);
        VMWAIT(0); LBYTE(v, 15);
        STAMP(2);
      }
    } else if (wave == 4) {
      BAR();
      TAGLOAD();
      if (clsF) {
        const u16* src = p.wuk + ((size_t)(ch * 256) * NH + hF) * HD + dh * 64;
        lds_wait(L + L_SY + 12, 0x01010101);
#pragma unroll
        for (int n = 0; n < 32; ++n) GLDS(src + (size_t)n * 8 * NH * HD, r8 * 8192 + (q8 ^ r8) * 16, 98304 + n * 1024);
        LBYTE(3, 1);
      }
      VMWAIT(0);
      if (clsF) LBYTE(3, 3);
      STAMP(3);
      TAGSET();
      lds_wait(L + L_SY + 12, 0x04040404);
      if (clsF) lds_wait(L + L_SY + 20, 0x10000);
      else __builtin_amdgcn_s_sleep(PDELAY);
      STAMP(1);
#pragma nounroll
      for (unsigned spin = 0; spin < SPIN_CAP; ++spin) {
        if (flg_ok(GLOAD(WS_QF + xg * 4096 + (l32 >> 1) * 256 + (lane & 1) * 16), tag)) break;
        __builtin_amdgcn_s_sleep(1);
      }
      asm volatile("" ::: "memory");
      LBYTE(21, 1);
      STAMP(2);
    } else {
      BAR();
      VMWAIT(0);
      LBYTE(2, 1);
      STAMP(1);
      {
        const unsigned xb = L_X + (l16 & 7) * 1024;
        const v4u ones = {0x3f803f80u, 0x3f803f80u, 0x3f803f80u, 0x3f803f80u};
        v4u xs[8];
#pragma unroll
        for (int t = 0; t < 8; ++t) xs[t] = *(const LAS v4u*)(L + xb + (((8 * t + 4 * (l16 >> 3) + g4) ^ (2 * (l16 & 7))) * 16));
        f32x4 G = {0.f, 0.f, 0.f, 0.f}, S = {0.f, 0.f, 0.f, 0.f};
#pragma unroll
        for (int t = 0; t < 8; ++t) { G = mfma16(xs[t], xs[t], G); S = mfma16(ones, xs[t], S); }
        float dg = (l16 & 2) ? ((l16 & 1) ? G[3] : G[2]) : ((l16 & 1) ? G[1] : G[0]);
        dg = g4 == (l16 >> 2) ? dg : 0.f;
        dg = xsum32(xsum16(dg));
        dg += dppm<0x128>(dg);
        const float s0 = S[0];
        const float mean = (s0 + dppm<0x128>(s0)) * (1.f / DM);
        const float rstd = __builtin_amdgcn_rsqf(fmaxf(dg * (1.f / DM) - mean * mean, 0.f) + 1e-5f);
        if (lane < 8) *(LAS v2u*)(L + L_LN + lane * 8) = (v2u){f2u(mean), f2u(rstd)};
        LGKM0();
        LBYTE(2, 3);
      }
      STAMP(3);
      TAGLOAD();
      TAGSET();
      if (!clsF && sidx < 86) {
        __builtin_amdgcn_s_sleep(ADELAY);
#pragma nounroll
        for (int ft = 2 * sidx; ft < 2 * sidx + 2; ++ft) {
          v4u gr[4];
          GSTART(4, gr, WS_GU + (((2 * i + hf) * 176 + ft) * 32 + l32) * 16);
          GFINISH(4, gr, WS_GU + (((2 * i + hf) * 176 + ft) * 32 + l32) * 16, 8);
          float hm[2];
#pragma unroll
          for (int e = 0; e < 2; ++e) {
            const float gv = xsum32(u2f(e ? gr[0].z : gr[0].x) + u2f(e ? gr[1].z : gr[1].x));
            const float uv = xsum32(u2f(e ? gr[2].z : gr[2].x) + u2f(e ? gr[3].z : gr[3].x));
            const float yy = 0.7978845608028654f * (gv + 0.044715f * gv * gv * gv);
            const float th = 1.f - 2.f / (__expf(2.f * yy) + 1.f);
            hm[e] = 0.5f * gv * (1.f + th) * uv;
          }
          const unsigned a2 = pk2(hm[0], hm[1]), b2 = dppu<0xb1>(a2);
          if (lane < 32 && !(lane & 1))
            __builtin_amdgcn_raw_buffer_store_b128((v4u){a2, tag, b2, tag}, wsr, WS_HM + (ft * 16 + (lane >> 1)) * 16, 0, AUX_SC1);
        }
        STAMP(2);
      }
    }
    if (!early) lds_wait64(L + L_SY, 0x0101010100000404ull);
    tail_loads();
    if (early) { VMWAIT(26); LBYTE(wave - 6, 7); }
    VMWAIT(10);
    if (early) { LBYTE(wave - 6, 15); STAMP(2); }
    if (wave >= 6) TAGLOAD();
  } else {
    if (clsF || wave == 0 || (wave == 1 && sidx < 48)) {
      const unsigned st = (clsF ? 32768 : 98304) + wave * 16384;
      const int t1 = wave * 128 + sidx, cg = (t1 % 88) >> 2, mat = t1 >= 88;
      const int kq1 = clsF ? wave : (t1 & 3);
      TAGLOAD();
      {
        const unsigned pz = q8 ^ (((r8 & 3) << 1) | (r8 >> 2));
        const u16* src; int stride; unsigned vo;
        if (clsF) { stride = NH * HD; src = p.wq + (size_t)(kq1 * 128) * stride + hF * HD + dh * 64; vo = r8 * 8192 + pz * 16; }
        else { stride = FF; src = (mat ? p.wu : p.wg) + (size_t)(kq1 * 128) * stride; vo = (r8 * FF + min(cg * 64 + (int)pz * 8, FF - 8)) * 2; }
#pragma unroll
        for (int i = 0; i < 16; ++i) {
          if (i == 8) BAR();
          GLDS(src + i * 8 * stride, vo, st + i * 1024);
        }
      }
      LBYTE(12 + wave, 1);
      lds_wait(L + L_SY, 0x010000);
      STAMP(15);
      v4u xr[4];
#pragma unroll
      for (int s = 0; s < 4; ++s) {
        xr[s] = *(const LAS v4u*)(L + L_X + (l16 & 7) * 1024 + (((16 * kq1 + 4 * s + g4) ^ (2 * (l16 & 7))) * 16));
        if (l16 >= 8) xr[s] = (v4u){0x3f803f80u, 0x3f803f80u, 0x3f803f80u, 0x3f803f80u};
      }
      f32x4 acc[4];
      {
        const unsigned rq = l16 >> 2, pc = (l16 & 3) >> 1, ph = (l16 & 1) * 8;
        const unsigned a0 = st + (8 * g4 + rq) * 128 + ((pc ^ (rq << 1)) * 16) + ph;
        const unsigned a1 = st + (8 * g4 + rq + 4) * 128 + ((pc ^ (rq << 1) ^ 1) * 16) + ph;
#pragma unroll
        for (int nt = 0; nt < 4; ++nt) acc[nt] = (f32x4){0.f, 0.f, 0.f, 0.f};
#pragma unroll
        for (int s = 0; s < 4; ++s) {
          if (s == 0) VMWAIT(12); else if (s == 1) VMWAIT(8); else if (s == 2) VMWAIT(4); else VMWAIT(0);
          if (s == 3) STAMP(1);
          v4u af[4];
#pragma unroll
          for (int nt = 0; nt < 4; ++nt) af[nt] = tr_read2(L + (a0 ^ (nt * 32)) + s * 4096, L + (a1 ^ (nt * 32)) + s * 4096);
#pragma unroll
          for (int nt = 0; nt < 4; ++nt) acc[nt] = mfma16(af[nt], xr[s], acc[nt]);
        }
      }
      lds_wait(L + L_SY, 0x020000);
      const v2u ln = *(const LAS v2u*)(L + L_LN + (l16 & 7) * 8);
      const float mean = u2f(ln.x), rstd = u2f(ln.y);
      LBYTE(12 + wave, clsF ? 3 : 7);
#pragma unroll
      for (int nt = 0; nt < 4; ++nt)
#pragma unroll
        for (int j = 0; j < 4; ++j) acc[nt][j] = (acc[nt][j] - mean * dppm<0x158>(acc[nt][j])) * rstd;
      if (!clsF) {
        TAGSET();
#pragma unroll
        for (int nt = 0; nt < 4; ++nt) {
          const unsigned c = dppu<0x128>(f2u(acc[nt][2])), d = dppu<0x128>(f2u(acc[nt][3]));
          __builtin_amdgcn_raw_buffer_store_b128((v4u){l16 < 8 ? f2u(acc[nt][0]) : c, tag, l16 < 8 ? f2u(acc[nt][1]) : d, tag}, wsr,
                                                 WS_GU + ((((mat * 4 + kq1) * 176 + cg * 8 + 2 * nt + (g4 >> 1)) * 8 + (l16 & 7)) * 4 + 2 * (g4 & 1) + (l16 >> 3)) * 16, 0, AUX_SC1);
        }
        STAMP(2);
      } else {
        if (l16 < 8) {
#pragma unroll
          for (int nt = 0; nt < 4; ++nt)
            *(LAS v2u*)(L + L_QP + (wave * 8 + l16) * 144 + nt * 32 + g4 * 8) = (v2u){pk2(acc[nt][0], acc[nt][1]), pk2(acc[nt][2], acc[nt][3])};
        }
        LGKM0();
        LBYTE(16 + wave, 1);
        STAMP(2);
        lds_wait(L + L_SY, 0x02000000);
        v4u wf[8];
#pragma unroll
        for (int n = 0; n < 8; ++n) wf[n] = *(const LAS v4u*)(L + 98304 + (wave * 64 + (n >> 1) * 16 + l16) * 128 + (((4 * (n & 1) + g4) ^ (l16 & 7)) * 16));
        lds_wait(L + L_SY + 16, 0x01010101);
        LBYTE(12 + wave, 7);
        v4u bq[4];
#pragma unroll
        for (int k = 0; k < 4; ++k) bq[k] = *(const LAS v4u*)(L + L_QP + ((k >> 1) * 16 + l16) * 144 + (k & 1) * 64 + g4 * 16);
        const float scale = 0.08838834764831845f * 1.4426950408889634f;
        unsigned yp[2][2];
#pragma unroll
        for (int cp = 0; cp < 2; ++cp) {
          f32x4 a0 = {0.f, 0.f, 0.f, 0.f}, a1 = {0.f, 0.f, 0.f, 0.f};
#pragma unroll
          for (int k = 0; k < 4; ++k) { a0 = mfma16(wf[cp * 4 + (k & 1)], bq[k], a0); a1 = mfma16(wf[cp * 4 + 2 + (k & 1)], bq[k], a1); }
          float y[4];
#pragma unroll
          for (int j = 0; j < 4; ++j) {
            const float keep = l16 < 8 ? a0[j] : a1[j], give = l16 < 8 ? a1[j] : a0[j];
            y[j] = (keep + dppm<0x128>(give)) * scale;
          }
          yp[cp][0] = pk2(y[0], y[1]); yp[cp][1] = pk2(y[2], y[3]);
        }
        {
          auto s0 = __builtin_amdgcn_permlane16_swap(yp[0][0], yp[1][0], false, false);
          auto s1 = __builtin_amdgcn_permlane16_swap(yp[0][1], yp[1][1], false, false);
          __builtin_amdgcn_raw_buffer_store_b128((v4u){s0[0], s1[0], s0[1], s1[1]}, wsr,
                                                 WS_QA + (((dh * 8 + (l16 & 7)) * 4 + ch * 2 + (wave >> 1)) * 32 + hF) * 256 + (wave & 1) * 128 +
                                                     (2 * (g4 & 1) + (l16 >> 3)) * 32 + (g4 >> 1) * 16, 0, AUX_SC1);
        }
        STAMP(12);
        VMWAIT(0);
        TAGSET();
        if (lds_ticket(L + L_SY + 24, lane) == 3) {
          if (lane < 8) __builtin_amdgcn_raw_buffer_store_b32(tag, wsr, WS_QF + lane * 4096 + (hF >> 1) * 256 + ((hF & 1) * 4 + dh * 2 + ch) * 4, 0, AUX_SC1);
          LBYTE(22, 1);
        }
        STAMP(3);
      }
    } else {
      TAGLOAD();
      BAR();
    }
    v4u hole;
    {
      const unsigned qo = WS_QA + (bA * 4 + wave) * 8192 + lane * 16;
      const unsigned qd = L_S + wave * 8192 + g4 * 256;
      unsigned k1 = 0x3f80u;
      asm volatile("" : "+s"(k1));
      lds_wait(L + L_SY + 20, 0x0100);
      v4u qa[16];
#pragma unroll
      for (int i = 0; i < 16; ++i) qa[i] = GLOAD(qo + (i >> 1) * 1024 + (i & 1) * 262144);
      const v4u ha = GLOAD(WS_QA + (bA * 4 + 3) * 8192 + 224 + hf * 16), hb = GLOAD(WS_QA + 262144 + (bA * 4 + 3) * 8192 + 224 + hf * 16);
#pragma unroll
      for (int i = 0; i < 8; ++i) {
        const v4u t = addbf4(qa[2 * i], qa[2 * i + 1], k1);
        const unsigned ad = (i == 0 && wave == 3 && lane >= 14 && lane < 16) ? 1024 + (l16 ^ 1) * 16 : i * 1024 + ((l16 ^ ((g4 << 2) | (i & 3))) * 16);
        *(volatile LAS v4u*)(L + qd + ad) = t;
      }
      LGKM0();
      LBYTE(4 + wave, 1);
      hole = addbf4(ha, hb, k1);
      STAMP(13);
    }
    f32x16 sacc;
    {
#pragma unroll
      for (int i = 0; i < 16; ++i) sacc[i] = 0.f;
      const unsigned tz = (hf ^ zl) * 16;
      const unsigned qb = L_S + l32 * 256 + tz;
      const unsigned kb = L_CKV + (wave * 32 + l32) * 256 + tz;
      v4u qf[2][8], kf[2][8];
      unsigned long long have = lds_wait64(L + L_SY, 0x0000000100000101ull);
      STAMP(4);
#pragma unroll
      for (int s = 0; s < 8; ++s) { kf[0][s] = *(const LAS v4u*)(L + (kb ^ (s * 32))); qf[0][s] = *(const LAS v4u*)(L + (qb ^ (s * 32))); }
#pragma unroll
      for (int blk = 0; blk < 4; ++blk) {
        if (blk < 3) {
          const unsigned long long need = (0x0101ull << (blk + 1)) | (1ull << (40 + 8 * blk));
          if ((have & need) != need) {
#pragma unroll
            for (int s = 0; s < 8; ++s) sacc = mfma32(kf[blk & 1][s], qf[blk & 1][s], sacc);
            if (blk == 1) LBYTE(8 + wave, 1);
            have = lds_wait64(L + L_SY, need);
#pragma unroll
            for (int s = 0; s < 8; ++s) {
              kf[(blk + 1) & 1][s] = *(const LAS v4u*)(L + (kb ^ (s * 32)) + (blk + 1) * 32768);
              qf[(blk + 1) & 1][s] = *(const LAS v4u*)(L + (qb ^ (s * 32)) + (blk + 1) * 8192);
            }
          } else {
#pragma unroll
            for (int s = 0; s < 8; ++s) {
              kf[(blk + 1) & 1][s] = *(const LAS v4u*)(L + (kb ^ (s * 32)) + (blk + 1) * 32768);
              qf[(blk + 1) & 1][s] = *(const LAS v4u*)(L + (qb ^ (s * 32)) + (blk + 1) * 8192);
              sacc = mfma32(kf[blk & 1][s], qf[blk & 1][s], sacc);
            }
            if (blk == 1) LBYTE(8 + wave, 1);
            if (blk < 2) have = lds_peek64(L + L_SY);
          }
        } else {
          if (l32 == 0) qf[1][7] = hole;
#pragma unroll
          for (int s = 0; s < 8; ++s) sacc = mfma32(kf[1][s], qf[1][s], sacc);
        }
      }
    }
    STAMP(5);
    GSTART(4, tw, ((i & 2) ? h1 : h0) + (i & 1) * 16);
    lds_wait(L + L_SY + 8, 0x01010101);
    float mx = fmaxf(fmaxf(fmaxf(sacc[0], sacc[1]), fmaxf(sacc[2], sacc[3])), fmaxf(fmaxf(sacc[4], sacc[5]), fmaxf(sacc[6], sacc[7])));
    mx = fmaxf(mx, fmaxf(fmaxf(fmaxf(sacc[8], sacc[9]), fmaxf(sacc[10], sacc[11])), fmaxf(fmaxf(sacc[12], sacc[13]), fmaxf(sacc[14], sacc[15]))));
    mx = __builtin_ceilf(xmax32(mx));
    float ls = 0.f;
#pragma unroll
    for (int q = 0; q < 4; ++q) {
      const unsigned p0 = pk2(__builtin_amdgcn_exp2f(sacc[4 * q] - mx), __builtin_amdgcn_exp2f(sacc[4 * q + 1] - mx));
      const unsigned p1 = pk2(__builtin_amdgcn_exp2f(sacc[4 * q + 2] - mx), __builtin_amdgcn_exp2f(sacc[4 * q + 3] - mx));
      *(LAS v2u*)(L + L_P + l32 * 256 + (((wave * 4 + q) ^ zl) * 16) + hf * 8) = (v2u){p0, p1};
      ls = dot_ones(p1, dot_ones(p0, ls));
    }
    ls = xsum32(ls);
    *(LAS float*)(L + L_ST + hf * 512 + l32 * 16 + wave * 4) = hf ? ls : mx;
  }
  BAR();
  const unsigned wb = L_CKV + (wave >> 1) * 32768;
  const unsigned wq8 = (wave & 1) * 8;
  f32x16 o0, o1;
  {
    v4u pf[8];
    const f32x4 mv = *(const LAS f32x4*)(L + L_ST + l32 * 16), lv = *(const LAS f32x4*)(L + L_ST + 512 + l32 * 16);
    {
      const unsigned pbs = L_P + l32 * 256 + (hf ^ zl) * 16;
#pragma unroll
      for (int ks = 0; ks < 8; ++ks) pf[ks] = *(const LAS v4u*)(L + (pbs ^ (ks * 32)));
    }
    const float M = fmaxf(fmaxf(mv[0], mv[1]), fmaxf(mv[2], mv[3]));
#pragma unroll
    for (int w = 0; w < 4; ++w) {
      const unsigned sh = (unsigned)fminf(M - mv[w], 255.f) * 0x00800080u;
#pragma unroll
      for (int e = 0; e < 2; ++e) {
        v4u t = pf[2 * w + e];
        pf[2 * w + e] = (v4u){pksubsat(t.x, sh), pksubsat(t.y, sh), pksubsat(t.z, sh), pksubsat(t.w, sh)};
      }
    }
    if (wave == 5) {
      const float lt = (lv[0] * __builtin_amdgcn_exp2f(mv[0] - M) + lv[1] * __builtin_amdgcn_exp2f(mv[1] - M)) +
                       (lv[2] * __builtin_amdgcn_exp2f(mv[2] - M) + lv[3] * __builtin_amdgcn_exp2f(mv[3] - M));
      if (lane < 32)
        __builtin_amdgcn_raw_buffer_store_b64((v2u){f2u(M), f2u(lt)}, wsr, WS_FL + ((((lane >> 2) * 8 + bA) * 32 + chunk) * 4 + (lane & 3)) * 8, 0, AUX_SC1);
    }
#pragma unroll
    for (int i = 0; i < 16; ++i) { o0[i] = 0.f; o1[i] = 0.f; }
    const unsigned blk = (lane >> 4) & 1, q = (lane & 15) >> 2, pp = lane & 3;
    unsigned ta[2];
#pragma unroll
    for (int tt = 0; tt < 2; ++tt) {
      const unsigned row = 8 * hf + 4 * tt + q;
      ta[tt] = wb + row * 256 + (((wq8 + 2 * blk + (pp >> 1)) ^ swz(row)) * 16) + 8 * (pp & 1);
    }
#pragma unroll
    for (int ks = 0; ks < 8; ++ks) {
      o0 = mfma32(pf[ks], tr_read2(L + ta[0] + ks * 4096, L + ta[1] + ks * 4096), o0);
      o1 = mfma32(pf[ks], tr_read2(L + (ta[0] ^ 64) + ks * 4096, L + (ta[1] ^ 64) + ks * 4096), o1);
    }
  }
  STAMP(6);
  {
    const unsigned pbase = WS_PART + ((((bA * 32 + 4 * (g4 >> 1) + (g4 & 1)) * 8 + wave) * 32 + chunk) * 16 + l16) * 8;
#pragma unroll
    for (int i = 0; i < 16; i += 2) {
      auto s = __builtin_amdgcn_permlane16_swap(pk2(o0[i], o1[i]), pk2(o0[i + 1], o1[i + 1]), false, false);
      __builtin_amdgcn_raw_buffer_store_b64((v2u){s[0], s[1]}, wsr, pbase + (8 * (i >> 2) + (i & 3)) * (8 * 32 * 128), 0, AUX_SC1);
    }
  }
  if (wave >= 4) {
    const unsigned sc_e = wb + r8 * 256 + (((wq8 + q8) ^ swz(r8)) * 16);
    const unsigned sc_o = wb + (r8 + 8) * 256 + (((wq8 + q8) ^ swz(r8 + 8)) * 16);
#pragma unroll
    for (int e = 0; e < 2; ++e) {
      *(LAS v4u*)(L + sc_e + e * 12288) = tw[5 * e + 0];
      *(LAS v4u*)(L + sc_o + e * 12288) = tw[5 * e + 1];
      *(LAS v4u*)(L + sc_e + e * 12288 + 4096) = tw[5 * e + 2];
      *(LAS v4u*)(L + sc_o + e * 12288 + 4096) = tw[5 * e + 3];
      *(LAS v4u*)(L + sc_e + e * 12288 + 8192) = tw[5 * e + 4];
    }
  }
  BAR();
  STAMP(7);
  const unsigned pc = (l16 & 3) >> 1, ph = (l16 & 1) * 8, rq = l16 >> 2;
  v4u av[2], ao[4], ad0, ad1;
  f32x4 accE = {0.f, 0.f, 0.f, 0.f};
  {
    const unsigned pw = 2 * (wave & 3) + pc;
    const unsigned vr = 8 * (wave >> 2) + 2 * g4;
    const unsigned vb = (2 + (rq >> 1)) * 32768 + ((rq & 1) * 48 + 16 + vr) * 256 + ph;
#pragma unroll
    for (int s = 0; s < 2; ++s)
      av[s] = tr_read2(L + vb + (((8 * s + pw) ^ swz(vr)) * 16), L + vb + 256 + (((8 * s + pw) ^ swz(vr + 1)) * 16));
    if (wave < 4) {
      const unsigned rin = 8 * (g4 & 1) + rq, pco = (g4 >> 1) * 8 + pw;
      const unsigned o0a = 65536 + rin * 256 + ((pco ^ swz(rin)) * 16) + ph, o1a = 65536 + (rin + 4) * 256 + ((pco ^ swz(rin + 4)) * 16) + ph;
#pragma unroll
      for (int ks = 0; ks < 4; ++ks) ao[ks] = tr_read2(L + o0a + (ks & 1) * 32768 + (ks >> 1) * 12288, L + o1a + (ks & 1) * 32768 + (ks >> 1) * 12288);
      const unsigned pcd = (g4 & 1) * 8 + pw, db = (2 + (g4 >> 1)) * 32768 + ph;
      const unsigned d0 = db + (32 + rq) * 256 + ((pcd ^ swz(rq)) * 16), d1 = db + (36 + rq) * 256 + ((pcd ^ swz(rq + 4)) * 16);
      ad0 = tr_read2(L + d0, L + d1); ad1 = tr_read2(L + d0 + 12288, L + d1 + 12288);
    }
    VMWAIT(0);
    TAGSET();
    if (lds_ticket(L + L_SY + 28, lane) == 7) {
      if (lane < 8) __builtin_amdgcn_raw_buffer_store_b32(tag, wsr, WS_PF + lane * 8192 + (bA * 4 + (chunk >> 3)) * 256 + (chunk & 7) * 4, 0, AUX_SC1);
    }
    STAMP(8);
    if (wave < 4) {
      GFINISH(4, tw, ((i & 2) ? h1 : h0) + (i & 1) * 16, 1);
      accE = mfma16(ad0, (v4u){tw[0].x, tw[0].z, tw[1].x, tw[1].z}, accE);
      accE = mfma16(ad1, hon ? (v4u){tw[2].x, tw[2].z, tw[3].x, tw[3].z} : (v4u){0u, 0u, 0u, 0u}, accE);
    }
  }
  {
#pragma nounroll
    for (unsigned spin = 0; spin < SPIN_CAP; ++spin) {
      if (flg_ok(GLOAD(WS_PF + xg * 8192 + (wave * 4 + ((lane >> 1) & 3)) * 256 + (lane & 1) * 16), tag)) break;
      __builtin_amdgcn_s_sleep(1);
    }
    asm volatile("" ::: "memory");
    STAMP(14);
    v2u pr[9];
    const unsigned po = WS_PART + ((((wave * 32 + hA) * 8 + sl) * 32) * 16 + lane) * 8;
    const unsigned fo = WS_FL + (((xg * 8 + wave) * 32 + l32) * 4 + (hA & 3)) * 8;
    pr[8] = __builtin_amdgcn_raw_buffer_load_b64(wsr, fo, 0, AUX_SC1);
#pragma unroll
    for (int i = 0; i < 8; ++i) pr[i] = __builtin_amdgcn_raw_buffer_load_b64(wsr, po + i * 512, 0, AUX_SC1);
    const float mj = u2f(pr[8].x);
    float M = mj;
    M = fmaxf(M, dppm<0x121>(M)); M = fmaxf(M, dppm<0x122>(M)); M = fmaxf(M, dppm<0x124>(M)); M = fmaxf(M, dppm<0x128>(M));
    M = xmax16(M);
    const float wj = bfhi(f2u(__builtin_amdgcn_exp2f(mj - M)));
    float Ls = wj * u2f(pr[8].y);
    Ls += dppm<0x121>(Ls); Ls += dppm<0x122>(Ls); Ls += dppm<0x124>(Ls); Ls += dppm<0x128>(Ls);
    Ls = xsum16(Ls);
    const float inv = __builtin_amdgcn_rcpf(Ls);
    const unsigned e4 = l16 & 3;
    const unsigned sh = (e4 & 1) ? 0u : 16u, k0 = e4 < 2 ? ~0u : 0u;
    unsigned wa[16];
#pragma unroll
    for (int u = 0; u < 8; ++u) {
      const unsigned w = (unsigned)__builtin_amdgcn_ds_bpermute((g4 + 4 * u) * 4, f2u(wj)) >> sh;
      wa[2 * u] = w & k0; wa[2 * u + 1] = w & ~k0;
    }
    f32x4 acc = {0.f, 0.f, 0.f, 0.f};
#pragma unroll
    for (int u = 0; u < 4; ++u)
      acc = mfma16((v4u){wa[4 * u], wa[4 * u + 1], wa[4 * u + 2], wa[4 * u + 3]}, (v4u){pr[2 * u].x, pr[2 * u].y, pr[2 * u + 1].x, pr[2 * u + 1].y}, acc);
    if (lane < 16) *(LAS v2u*)(L + L_CL + wave * 144 + lane * 8) = (v2u){pk2(acc[0] * inv, acc[1] * inv), pk2(acc[2] * inv, acc[3] * inv)};
    BAR();
  }
  const unsigned one = (l16 & 1) ? 0x3f800000u : 0x00003f80u;
  const unsigned s0 = (l16 & 2) ? 0u : one, s1 = (l16 & 2) ? one : 0u;
  {
    const v4u b0 = *(const LAS v4u*)(L + L_CL + (l16 & 7) * 144 + g4 * 16), b1 = *(const LAS v4u*)(L + L_CL + (l16 & 7) * 144 + 64 + g4 * 16);
    f32x4 acc = {0.f, 0.f, 0.f, 0.f};
    acc = mfma16(av[0], b0, acc);
    acc = mfma16(av[1], b1, acc);
    if (l16 < 8)
      __builtin_amdgcn_raw_buffer_store_b128((v4u){pk2(acc[0], acc[1]), tag, pk2(acc[2], acc[3]), tag}, wsr,
                                             WS_CX + ((((hA * 8 + wave) * 8 + sl) * 8 + l16) * 4 + g4) * 16, 0, AUX_SC1);
    STAMP(9);
    v4u cr[4];
    const unsigned co = WS_CX + ((hB * 8 + wave) * 256 + lane) * 16;
    if (CDELAY) __builtin_amdgcn_s_sleep(CDELAY);
#pragma nounroll
    for (unsigned spin = 0; spin < SPIN_CAP; ++spin) {
#pragma unroll
      for (int i = 0; i < 4; ++i) cr[i] = GLOAD(co + i * 1024);
      if (__all((cr[0].y == tag) & (cr[0].w == tag) & (cr[1].y == tag) & (cr[1].w == tag) & (cr[2].y == tag) & (cr[2].w == tag) & (cr[3].y == tag) & (cr[3].w == tag))) break;
      __builtin_amdgcn_s_sleep(1);
    }
    STAMP(10);
    {
      const bool on = ((l16 >> 2) & 1) == (g4 & 1);
      const unsigned t0 = on ? s0 : 0u, t1 = on ? s1 : 0u;
      f32x4 a = {0.f, 0.f, 0.f, 0.f};
      a = mfma16((v4u){t0, t1, t0, t1}, (v4u){cr[0].x, cr[0].z, cr[1].x, cr[1].z}, a);
      a = mfma16((v4u){t0, t1, t0, t1}, (v4u){cr[2].x, cr[2].z, cr[3].x, cr[3].z}, a);
      if (lane < 32) *(LAS v2u*)(L + L_CX + (lane >> 2) * 272 + wave * 32 + (lane & 3) * 8) = (v2u){pk2(a[0], a[1]), pk2(a[2], a[3])};
    }
    BAR();
  }
  if (wave < 4) {
#pragma unroll
    for (int ks = 0; ks < 4; ++ks) accE = mfma16(ao[ks], *(const LAS v4u*)(L + L_CX + (l16 & 7) * 272 + ks * 64 + g4 * 16), accE);
    if (l16 < 8)
      __builtin_amdgcn_raw_buffer_store_b128((v4u){pk2(accE[0], accE[1]), tag, pk2(accE[2], accE[3]), tag}, wsr,
                                             WS_OG + ((((sl * 4 + wave) * 32 + hB) * 8 + l16) * 4 + g4) * 16, 0, AUX_SC1);
    STAMP(11);
  } else if (wave == 4) {
    const unsigned oo = WS_OG + ((((wg & 31) * 32 + (lane >> 2)) * 8 + (wg >> 5)) * 4 + (lane & 3)) * 16;
    const v2u xw = *(const GAS v2u*)(p.x + wg * 16 + (lane & 3) * 4);
    v4u r0, r1;
    if (ODELAY) __builtin_amdgcn_s_sleep(ODELAY);
#pragma nounroll
    for (unsigned spin = 0; spin < SPIN_CAP; ++spin) {
      r0 = GLOAD(oo);
      r1 = GLOAD(oo + 16 * 512);
      if (__all((r0.y == tag) & (r0.w == tag) & (r1.y == tag) & (r1.w == tag))) break;
      __builtin_amdgcn_s_sleep(1);
    }
    f32x4 acc = {0.f, 0.f, 0.f, 0.f};
    acc = mfma16((v4u){s0, s1, s0, s1}, (v4u){r0.x, r0.z, r1.x, r1.z}, acc);
#pragma unroll
    for (int j = 0; j < 4; ++j) { acc[j] += dppm<0x124>(acc[j]); acc[j] += dppm<0x128>(acc[j]); }
    if (lane < 4)
      *(GAS v2u*)(p.out + wg * 16 + lane * 4) = (v2u){pk2(bflo(xw.x) + acc[0], bfhi(xw.x) + acc[1]), pk2(bflo(xw.y) + acc[2], bfhi(xw.y) + acc[3])};
    if (wg == 0 && lane == 0) __builtin_amdgcn_raw_buffer_store_b32(tag, wsr, WS_EPOCH, 0, AUX_SC1);
    STAMP(11);
  }
}

extern "C" void kernel_launch(void* const* d_in, const int* in_sizes, int n_in, void* d_out, int out_size, void* d_ws, size_t ws_size,
                              hipStream_t stream) {
  Params p;
  p.x = (const u16*)d_in[0]; p.ckv = (const u16*)d_in[1]; p.wq = (const u16*)d_in[2]; p.wuk = (const u16*)d_in[3];
  p.wuv = (const u16*)d_in[4]; p.wo = (const u16*)d_in[5]; p.wg = (const u16*)d_in[7]; p.wu = (const u16*)d_in[8]; p.wd = (const u16*)d_in[9];
  p.out = (u16*)d_out; p.ws = (unsigned char*)d_ws;
  (void)hipFuncSetAttribute((const void*)fused_kernel, hipFuncAttributeMaxDynamicSharedMemorySize, LDS_BYTES);
  fused_kernel<<<NWG, NTHR, LDS_BYTES, stream>>>(p);
}
```

```cpp
#include <hip/hip_runtime.h>
#include <hip/hip_bf16.h>
#include <stdint.h>

#define STAMPS 0
#define CODE_LINES 140
#define WINS 12
#define WINF 14
#define ADELAY 100
#define PMASK 0x0202
#define CDELAY 8
#define ODELAY 8
#define WMAX (WINS > WINF ? WINS : WINF)
static_assert(WMAX <= 17, "progress masks");

typedef unsigned v4u __attribute__((ext_vector_type(4)));
typedef unsigned v2u __attribute__((ext_vector_type(2)));
typedef float f32x2 __attribute__((ext_vector_type(2)));
typedef float f32x4 __attribute__((ext_vector_type(4)));
typedef float f32x16 __attribute__((ext_vector_type(16)));
typedef short bf16x8 __attribute__((ext_vector_type(8)));
typedef short s16x4 __attribute__((ext_vector_type(4)));
typedef unsigned short u16x2 __attribute__((ext_vector_type(2)));
typedef __bf16 bf16x2 __attribute__((ext_vector_type(2)));
typedef unsigned short u16;
#define LAS __attribute__((address_space(3)))
#define GAS __attribute__((address_space(1)))

constexpr int NB = 8, NH = 32, SK = 4096, HD = 128, DM = 512, DC = 512, FF = 1376;
constexpr int NWG = 256, NTHR = 512;
constexpr unsigned WS_EPOCH = 0;
constexpr unsigned WS_QF = 4096;
constexpr unsigned WS_PF = WS_QF + 65536;
constexpr unsigned WS_QA = WS_PF + 65536;
constexpr unsigned WS_GU = WS_QA + 8 * 32 * 1024;
constexpr unsigned WS_HM = WS_GU + 8 * 176 * 32 * 16;
constexpr unsigned WS_FL = WS_HM + 176 * 8 * 2 * 16;
constexpr unsigned WS_CX = WS_FL + 8 * 8 * 32 * 4 * 16;
constexpr unsigned WS_OG = WS_CX + 32 * 8 * 8 * 8 * 4 * 16;
constexpr unsigned WS_PART = 4u << 20;
constexpr unsigned WS_END = WS_PART + (8u << 20);
constexpr unsigned WS_STAMP = 32u << 20;
static_assert(WS_OG + 32 * 32 * 8 * 4 * 16 <= WS_PART, "ws map");
constexpr unsigned L_CKV = 0;
constexpr unsigned L_S = 128 * 1024;
constexpr unsigned L_SY = L_S + 24576 + 224;
constexpr unsigned L_QP = L_S;
constexpr unsigned L_LN = L_S + 9216;
constexpr unsigned L_DUMP = L_S + 9472;
constexpr unsigned L_OUT = L_S + 12288;
constexpr unsigned L_X = L_S + 16384;
constexpr unsigned T_O = 0, T_V = 16384, T_D = 32768;
constexpr unsigned L_P = L_S;
constexpr unsigned L_ST = L_S + 8192;
constexpr unsigned L_CL = L_S + 16384;
constexpr unsigned L_CX = L_S + 16384 + 2048;
constexpr unsigned LDS_BYTES = 160 * 1024;
constexpr int AUX_NT = 2, AUX_SC1 = 16;
constexpr unsigned SPIN_CAP = 1u << 18;

struct Params {
  const u16 *x, *ckv, *wq, *wuk, *wuv, *wo, *wg, *wu, *wd;
  u16* out;
  unsigned char* ws;
};

__device__ __forceinline__ float u2f(unsigned u) { return __uint_as_float(u); }
__device__ __forceinline__ unsigned f2u(float f) { return __float_as_uint(f); }
__device__ __forceinline__ float bflo(unsigned w) { return u2f(w << 16); }
__device__ __forceinline__ float bfhi(unsigned w) { return u2f(w & 0xffff0000u); }
__device__ __forceinline__ unsigned pk2(float lo, float hi) {
  f32x2 f = {lo, hi};
  return __builtin_bit_cast(unsigned, __builtin_convertvector(f, bf16x2));
}
__device__ __forceinline__ float dot_ones(unsigned a, float c) {
  return __builtin_amdgcn_fdot2_f32_bf16(__builtin_bit_cast(bf16x2, a), __builtin_bit_cast(bf16x2, 0x3f803f80u), c, false);
}
__device__ __forceinline__ unsigned pksubsat(unsigned a, unsigned b) {
  return __builtin_bit_cast(unsigned, __builtin_elementwise_sub_sat(__builtin_bit_cast(u16x2, a), __builtin_bit_cast(u16x2, b)));
}
__device__ __forceinline__ f32x4 mfma16(v4u a, v4u b, f32x4 c) {
  return __builtin_amdgcn_mfma_f32_16x16x32_bf16(__builtin_bit_cast(bf16x8, a), __builtin_bit_cast(bf16x8, b), c, 0, 0, 0);
}
__device__ __forceinline__ f32x16 mfma32(v4u a, v4u b, f32x16 c) {
  return __builtin_amdgcn_mfma_f32_32x32x16_bf16(__builtin_bit_cast(bf16x8, a), __builtin_bit_cast(bf16x8, b), c, 0, 0, 0);
}
__device__ __forceinline__ unsigned swz(unsigned row) { return ((row & 3) << 2) | ((row >> 2) & 3); }
#define BAR() do { asm volatile("s_waitcnt lgkmcnt(0)" ::: "memory"); __builtin_amdgcn_s_barrier(); asm volatile("" ::: "memory"); } while (0)
#define VMWAIT(n) do { asm volatile("" ::: "memory"); __builtin_amdgcn_s_waitcnt(0x0f70 | ((n) & 15) | (((n) >> 4) << 14)); asm volatile("" ::: "memory"); } while (0)
#define LGKM0() asm volatile("s_waitcnt lgkmcnt(0)" ::: "memory")
#define GLDS_(OP, sbase, voff, dst) asm volatile("s_mov_b32 m0, %2\n\ts_nop 0\n\t" OP :: "v"((unsigned)(voff)), "s"((const void*)(sbase)), "s"((unsigned)(dst)) : "memory", "m0")
#define GLDS(sbase, voff, dst) GLDS_("global_load_lds_dwordx4 %0, %1 nt", sbase, voff, dst)
#define GLDSX(sbase, voff, dst) GLDS_("global_load_lds_dwordx4 %0, %1", sbase, voff, dst)
#define GLDSC(sbase, voff, dst) GLDS_("global_load_lds_dwordx4 %0, %1 sc1", sbase, voff, dst)
#define GLDS4(sbase, voff, dst) GLDS_("global_load_lds_dword %0, %1 nt", sbase, voff, dst)
#define WLOAD(p) __builtin_nontemporal_load(p)
#define LBYTE(off, v) (*(volatile LAS unsigned char*)(L + L_SY + (off)) = (v))
__device__ __forceinline__ v2u tr_read(LAS unsigned char* a) {
  return __builtin_bit_cast(v2u, __builtin_amdgcn_ds_read_tr16_b64_v4i16((LAS s16x4*)a));
}
__device__ __forceinline__ v4u tr_read2(LAS unsigned char* a, LAS unsigned char* b) {
  const v2u x = tr_read(a), y = tr_read(b);
  return (v4u){x.x, x.y, y.x, y.y};
}
template <int CTRL>
__device__ __forceinline__ unsigned dppu(unsigned v) { return __builtin_amdgcn_update_dpp(v, v, CTRL, 0xf, 0xf, false); }
template <int CTRL>
__device__ __forceinline__ float dppm(float v) { return u2f(dppu<CTRL>(f2u(v))); }
__device__ __forceinline__ float xsum16(float v) {
  auto s = __builtin_amdgcn_permlane16_swap(f2u(v), f2u(v), false, false);
  return u2f(s[0]) + u2f(s[1]);
}
__device__ __forceinline__ float xmax16(float v) {
  auto s = __builtin_amdgcn_permlane16_swap(f2u(v), f2u(v), false, false);
  return fmaxf(u2f(s[0]), u2f(s[1]));
}
__device__ __forceinline__ float xsum32(float v) {
  auto s = __builtin_amdgcn_permlane32_swap(f2u(v), f2u(v), false, false);
  return u2f(s[0]) + u2f(s[1]);
}
__device__ __forceinline__ float xmax32(float v) {
  auto s = __builtin_amdgcn_permlane32_swap(f2u(v), f2u(v), false, false);
  return fmaxf(u2f(s[0]), u2f(s[1]));
}
__device__ __forceinline__ bool rec_ok(v4u r, unsigned tag) { return __all((r.y == tag) & (r.w == tag)); }
__device__ __forceinline__ bool flg_ok(v4u r, unsigned tag) { return __all((r.x == tag) & (r.y == tag) & (r.z == tag) & (r.w == tag)); }
__device__ __forceinline__ unsigned lds_wait(LAS unsigned char* a, unsigned mask) {
  unsigned w = 0;
#pragma nounroll
  for (unsigned spin = 0; spin < (SPIN_CAP << 4); ++spin) {
    w = __builtin_amdgcn_readfirstlane(*(volatile LAS unsigned*)a);
    if ((w & mask) == mask) break;
    __builtin_amdgcn_s_sleep(1);
  }
  asm volatile("" ::: "memory");
  return w;
}

__device__ __forceinline__ unsigned lds_ticket(LAS unsigned char* a, int lane) {
  unsigned t = 0;
  if (lane == 0) t = __hip_atomic_fetch_add((LAS unsigned*)a, 1u, __ATOMIC_RELAXED, __HIP_MEMORY_SCOPE_WORKGROUP);
  return __builtin_amdgcn_readfirstlane(t);
}
__device__ __forceinline__ void lds_count(LAS unsigned char* a, int lane) {
  if (lane == 0) __hip_atomic_fetch_add((LAS unsigned*)a, 1u, __ATOMIC_RELAXED, __HIP_MEMORY_SCOPE_WORKGROUP);
}

#define STAMP(k) do { } while (0)

#define GLOAD(ADDR) __builtin_amdgcn_raw_buffer_load_b128(wsr, (ADDR), 0, AUX_SC1)
#define GFINISH(N, qr, ADDR, SLP)                                                                            \
  do {                                                                                                       \
    unsigned done = 0;                                                                                       \
    _Pragma("nounroll") for (unsigned spin = 0; spin < SPIN_CAP; ++spin) {                                   \
      _Pragma("unroll") for (int i = 0; i < N; ++i)                                                          \
        if (!((done >> i) & 1)) { if (rec_ok(qr[i], tag)) done |= 1u << i; }                                 \
      if (done == (1u << N) - 1u) break;                                                                     \
      if (SLP) __builtin_amdgcn_s_sleep(SLP);                                                                \
      _Pragma("unroll") for (int i = 0; i < N; ++i)                                                          \
        if (!((done >> i) & 1)) qr[i] = GLOAD(ADDR);                                                         \
    }                                                                                                        \
  } while (0)
#define GSTART(N, qr, ADDR) do { _Pragma("unroll") for (int i = 0; i < N; ++i) qr[i] = GLOAD(ADDR); } while (0)

__global__ void __launch_bounds__(NTHR, 2) fused_kernel(Params p) {
  extern __shared__ __attribute__((aligned(16))) unsigned char lds_raw[];
  LAS unsigned char* L = (LAS unsigned char*)lds_raw;
  const int tid = threadIdx.x, lane = tid & 63;
  const int wave = __builtin_amdgcn_readfirstlane(tid >> 6);
  const int wg = blockIdx.x, xg = wg & 7, r = wg >> 3;
  const int bA = xg, chunk = r;
  const int hA = xg * 4 + (r >> 3), sl = r & 7;
  const int hB = ((xg + 1) & 7) * 4 + (r >> 3);
  const bool clsF = r & 1;
  const int sidx = (r >> 1) * 8 + xg, cq = (r >> 1) & 3;
  const int ntask = hB < 12 ? 6 : 5, task0 = hB < 12 ? 6 * hB : 12 + 5 * hB;
  const __amdgpu_buffer_rsrc_t wsr = __builtin_amdgcn_make_buffer_rsrc((void*)p.ws, 0, WS_END, 0x00020000);
  const int l16 = lane & 15, g4 = lane >> 4;
  const int l32 = lane & 31, hf = lane >> 5;
  const unsigned r8 = lane >> 3, q8 = lane & 7;
  STAMP(0);
  unsigned long long pc0;
  asm volatile("s_getpc_b64 %0" : "=s"(pc0));
  unsigned tag;
#define TAG() do { const unsigned char* e_ = p.ws; asm volatile("" : "+s"(e_)); tag = *(const __attribute__((address_space(4))) unsigned*)e_ + 1u; asm volatile("" :: "s"(tag)); } while (0)
  if (wave == 7) {
#pragma unroll
    for (int i = 0; i < 8; ++i) GLDSX(p.x + i * DM, (lane ^ (2 * i)) * 16, L_X + i * 1024);
  } else if (wave == 4) {
    if (lane < 8) *(LAS unsigned*)(L + L_SY + lane * 4) = (lane == 4 && !clsF) ? 3 : 0;
  } else if (wave == 6) {
    GLDS4((const void*)(pc0 & ~127ull), min((unsigned)((r & 3) * 64 + lane), (unsigned)CODE_LINES - 1u) * 128, L_DUMP + 2048);
  } else if (wave == 5) {
    GLDS4(p.ws, lane < 4 ? WS_PART + lane * (2u << 20) : (lane == 4 ? 2048 : WS_OG), L_DUMP + 2304);
    GLDS4(p.wuv + ((size_t)(sl * 64) * NH + hA) * HD, 0, L_DUMP + 2304);
    GLDS4(p.wo + (size_t)hB * HD * DM, 0, L_DUMP + 2304);
    GLDS4(p.wd, 0, L_DUMP + 2304);
    GLDS4(p.out, 0, L_DUMP + 2304);
  } else if (!clsF) {
    const size_t po = (size_t)((r >> 1) * 32 + wave * 8) * (NH * HD) + xg * 512;
    GLDS4(p.wq + po, r8 * 8192 + q8 * 128, L_DUMP + wave * 512);
    GLDS4(p.wuk + po, r8 * 8192 + q8 * 128, L_DUMP + wave * 512 + 256);
  }
  const unsigned pz = q8 ^ (((r8 & 3) << 1) | (r8 >> 2));
  const unsigned fa0 = (8 * g4 + (l16 >> 2)) * 128 + ((((l16 & 3) >> 1) ^ ((l16 >> 2) << 1)) * 16) + (l16 & 1) * 8;
  const unsigned fa1 = (fa0 ^ 16) + 512;
#define FRAG(base, nt, s) tr_read2(L + (base) + (fa0 ^ ((nt) * 32)) + (s) * 4096, L + (base) + (fa1 ^ ((nt) * 32)) + (s) * 4096)

  if (wave == 7) {
    BAR();
    VMWAIT(0);
    LBYTE(4, 1);
    STAMP(15);
    const unsigned xb = L_X + (l16 & 7) * 1024;
    const v4u ones = {0x3f803f80u, 0x3f803f80u, 0x3f803f80u, 0x3f803f80u};
    v4u xs[8];
#pragma unroll
    for (int t = 0; t < 8; ++t) xs[t] = *(const LAS v4u*)(L + xb + (((8 * t + 4 * (l16 >> 3) + g4) ^ (2 * (l16 & 7))) * 16));
    f32x4 G = {0.f, 0.f, 0.f, 0.f}, S = {0.f, 0.f, 0.f, 0.f};
#pragma unroll
    for (int t = 0; t < 8; ++t) { G = mfma16(xs[t], xs[t], G); S = mfma16(ones, xs[t], S); }
    float dg = (l16 & 2) ? ((l16 & 1) ? G[3] : G[2]) : ((l16 & 1) ? G[1] : G[0]);
    dg = g4 == (l16 >> 2) ? dg : 0.f;
    dg = xsum32(xsum16(dg));
    dg += dppm<0x128>(dg);
    const float s0 = S[0];
    const float mean = (s0 + dppm<0x128>(s0)) * (1.f / DM);
    const float rstd = __builtin_amdgcn_rsqf(fmaxf(dg * (1.f / DM) - mean * mean, 0.f) + 1e-5f);
    if (lane < 8) *(LAS v2u*)(L + L_LN + lane * 8) = (v2u){f2u(mean), f2u(rstd)};
    LGKM0();
    LBYTE(4, 3);
  }
  if (clsF || wave == 0 || (wave == 1 && sidx < 48)) {
    const unsigned st = (clsF ? 0 : 98304) + wave * 16384;
    const int t1 = wave * 128 + sidx, cg = (t1 % 88) >> 2, mat = t1 >= 88;
    const int kq1 = (clsF ? wave : t1) & 3;
    {
      const u16* src; int stride; unsigned vo;
      if (clsF) { stride = NH * HD; src = p.wq + (size_t)(kq1 * 128) * stride + hA * HD + (wave >> 2) * 64; vo = r8 * 8192 + pz * 16; }
      else { stride = FF; src = (mat ? p.wu : p.wg) + (size_t)(kq1 * 128) * stride; vo = (r8 * FF + min(cg * 64 + (int)pz * 8, FF - 8)) * 2; }
#pragma unroll
      for (int i = 0; i < 16; ++i) {
        if (i == 4 && wave != 7) BAR();
        GLDS(src + i * 8 * stride, vo, st + i * 1024);
      }
    }
    TAG();
    lds_wait(L + L_SY + 4, 0x01);
    if (wave != 7) STAMP(15);
    v4u xr[4];
#pragma unroll
    for (int s = 0; s < 4; ++s) {
      xr[s] = *(const LAS v4u*)(L + L_X + (l16 & 7) * 1024 + (((16 * kq1 + 4 * s + g4) ^ (2 * (l16 & 7))) * 16));
      if (l16 >= 8) xr[s] = (v4u){0x3f803f80u, 0x3f803f80u, 0x3f803f80u, 0x3f803f80u};
    }
    f32x4 acc[4];
#pragma unroll
    for (int nt = 0; nt < 4; ++nt) acc[nt] = (f32x4){0.f, 0.f, 0.f, 0.f};
#pragma unroll
    for (int s = 0; s < 4; ++s) {
      if (s == 0) VMWAIT(12);
      else if (s == 1) { if (clsF) VMWAIT(12); else VMWAIT(8); }
      else if (s == 2) { if (clsF) VMWAIT(8); else VMWAIT(4); }
      else { if (clsF) VMWAIT(4); else VMWAIT(0); STAMP(1); }
      v4u af[4];
#pragma unroll
      for (int nt = 0; nt < 4; ++nt) af[nt] = FRAG(st, nt, s);
      if (s == 0 && clsF) {
        LGKM0();
        const u16* su = p.wuk + ((size_t)(cq * 128 + wave * 16) * NH + hA) * HD;
#pragma unroll
        for (int j = 0; j < 4; ++j) GLDS(su + (size_t)j * 4 * NH * HD, g4 * 8192 + ((l16 ^ (4 * j + g4)) * 16), st + j * 1024);
      }
#pragma unroll
      for (int nt = 0; nt < 4; ++nt) acc[nt] = mfma16(af[nt], xr[s], acc[nt]);
    }
    lds_wait(L + L_SY + 4, 0x02);
    const v2u ln = *(const LAS v2u*)(L + L_LN + (l16 & 7) * 8);
    const float mean = u2f(ln.x), rstd = u2f(ln.y);
#pragma unroll
    for (int nt = 0; nt < 4; ++nt)
#pragma unroll
      for (int j = 0; j < 4; ++j) acc[nt][j] = (acc[nt][j] - mean * dppm<0x158>(acc[nt][j])) * rstd;
    if (!clsF) {
      lds_count(L + L_SY + 16, lane);
#pragma unroll
      for (int nt = 0; nt < 4; ++nt) {
        const unsigned c = dppu<0x128>(f2u(acc[nt][2])), d = dppu<0x128>(f2u(acc[nt][3]));
        __builtin_amdgcn_raw_buffer_store_b128((v4u){l16 < 8 ? f2u(acc[nt][0]) : c, tag, l16 < 8 ? f2u(acc[nt][1]) : d, tag}, wsr,
                                               WS_GU + ((((mat * 4 + kq1) * 176 + cg * 8 + 2 * nt + (g4 >> 1)) * 8 + (l16 & 7)) * 4 + 2 * (g4 & 1) + (l16 >> 3)) * 16, 0, AUX_SC1);
      }
      STAMP(2);
    } else {
      if (l16 < 8) {
#pragma unroll
        for (int nt = 0; nt < 4; ++nt)
          *(LAS v2u*)(L + L_QP + (kq1 * 8 + l16) * 272 + (wave >> 2) * 128 + nt * 32 + g4 * 8) = (v2u){pk2(acc[nt][0], acc[nt][1]), pk2(acc[nt][2], acc[nt][3])};
      }
      LGKM0();
      lds_count(L + L_SY + 12, lane);
      STAMP(2);
      VMWAIT(0);
      v4u wf[4], bq[8];
#pragma unroll
      for (int s = 0; s < 4; ++s) wf[s] = *(const LAS v4u*)(L + st + l16 * 256 + (((4 * s + g4) ^ l16) * 16));
      lds_wait(L + L_SY + 12, 8);
#pragma unroll
      for (int k = 0; k < 8; ++k) bq[k] = *(const LAS v4u*)(L + L_QP + ((k >> 2) * 16 + l16) * 272 + (k & 3) * 64 + g4 * 16);
      f32x4 a0 = {0.f, 0.f, 0.f, 0.f}, a1 = {0.f, 0.f, 0.f, 0.f};
#pragma unroll
      for (int k = 0; k < 4; ++k) { a0 = mfma16(wf[k], bq[k], a0); a1 = mfma16(wf[k], bq[4 + k], a1); }
      const float scale = 0.08838834764831845f * 1.4426950408889634f;
      float y[4];
#pragma unroll
      for (int j = 0; j < 4; ++j) { const float t = a0[j] + a1[j]; y[j] = (t + dppm<0x128>(t)) * scale; }
      if (l16 < 8) *(LAS v2u*)(L + L_OUT + l16 * 256 + wave * 32 + g4 * 8) = (v2u){pk2(y[0], y[1]), pk2(y[2], y[3])};
      LGKM0();
      lds_count(L + L_SY + 16, lane);
      STAMP(12);
    }
  } else {
    if (wave != 7) BAR();
    TAG();
    if (wave < 6 && wave != 4) { VMWAIT(0); lds_count(L + L_SY + 16, lane); }
  }
  const bool hon = 4 + g4 < ntask;
  const unsigned h0 = WS_HM + ((task0 + g4) * 8 + (l16 & 7)) * 32, h1 = WS_HM + ((task0 + (hon ? 4 + g4 : 0)) * 8 + (l16 & 7)) * 32;
  const unsigned zl = swz(l32);
  v4u tw[4];

  if (wave >= 6) {
    const int v = wave - 6;
    const u16* srcb = p.ckv + ((size_t)bA * SK + chunk * 128 + 4 * v) * DC;
    const unsigned vo0 = g4 * 1024 + (l16 ^ ((g4 << 2) | v)) * 16, vo1 = g4 * 1024 + (l16 ^ ((g4 << 2) | (2 + v))) * 16;
#pragma nounroll
    for (int blk = 0; blk < 4; ++blk) {
      if (blk == (clsF ? 0 : 3)) lds_wait(L + L_SY + 16, 8);
      if (blk == 0) STAMP(3);
#pragma unroll
      for (int n = 0; n < 16; ++n) {
        GLDS(srcb + n * 8 * DC + blk * 128, (n & 1) ? vo1 : vo0, L_CKV + blk * 32768 + (n * 2 + v) * 1024);
        if (WINS == WINF || clsF) VMWAIT(WINF - 1); else VMWAIT(WINS - 1);
        if (n == ((WMAX - 2) & 15) && blk > (WMAX - 2) / 16) LBYTE(v, (1 << (blk - (WMAX - 2) / 16)) - 1);
      }
    }
    STAMP(4);
    if (!(__builtin_amdgcn_readfirstlane(*(volatile LAS unsigned*)(L + L_SY + 4)) & 0x100)) {
      VMWAIT(0); LBYTE(v, 15);
      lds_wait(L + L_SY + 4, 0x100);
    }
    {
      const unsigned char* qs = p.ws + WS_QA + bA * 32768 + v * 1024;
      const unsigned q0 = g4 * 256 + (l16 ^ ((g4 << 2) | v)) * 16, q1 = g4 * 256 + (l16 ^ ((g4 << 2) | (2 + v))) * 16;
#pragma unroll
      for (int n = 0; n < 16; ++n) {
        if (n != 12 || v != 0 || lane < 14 || lane >= 16) GLDSC(qs + n * 2048, (n & 1) ? q1 : q0, L_S + n * 2048 + v * 1024);
      }
    }
    VMWAIT(16); LBYTE(v, 15);
    STAMP(5);
    VMWAIT(12); LBYTE(2 + v, 1);
    VMWAIT(8); LBYTE(2 + v, 3);
    VMWAIT(4); LBYTE(2 + v, 7);
    VMWAIT(0); LBYTE(2 + v, 15);
    STAMP(13);
  } else if (wave == 4) {
    lds_wait(L + L_SY, PMASK);
    STAMP(3);
#pragma nounroll
    for (unsigned spin = 0; spin < SPIN_CAP; ++spin) {
      if (flg_ok(GLOAD(WS_QF + xg * 4096 + (l32 >> 1) * 256 + (lane & 1) * 16), tag)) break;
      __builtin_amdgcn_s_sleep(1);
    }
    asm volatile("" ::: "memory");
    const unsigned pfq = __builtin_amdgcn_raw_buffer_load_b32(wsr, WS_QA + bA * 32768 + r * 1024 + q8 * 128, 0, AUX_SC1);
    LBYTE(5, 1);
    STAMP(4);
    asm volatile("" :: "v"(pfq));
  } else if (wave == 5) {
    if (clsF) {
      lds_wait(L + L_SY + 16, 8);
      const v4u t0 = *(const LAS v4u*)(L + L_OUT + lane * 16), t1 = *(const LAS v4u*)(L + L_OUT + 1024 + lane * 16);
      const unsigned qo = WS_QA + ((g4 * 4 + cq) * 32 + hA) * 256 + l16 * 16;
      __builtin_amdgcn_raw_buffer_store_b128(t0, wsr, qo, 0, AUX_SC1);
      __builtin_amdgcn_raw_buffer_store_b128(t1, wsr, qo + 4 * 32768, 0, AUX_SC1);
      STAMP(3);
      VMWAIT(0);
      if (lane < 8) __builtin_amdgcn_raw_buffer_store_b32(tag, wsr, WS_QF + lane * 4096 + (hA >> 1) * 256 + ((hA & 1) * 4 + cq) * 4, 0, AUX_SC1);
      STAMP(4);
    } else if (sidx < 86) {
      __builtin_amdgcn_s_sleep(ADELAY);
#pragma nounroll
      for (int ft = 2 * sidx; ft < 2 * sidx + 2; ++ft) {
        v4u gr[4];
        GSTART(4, gr, WS_GU + (((2 * i + hf) * 176 + ft) * 32 + l32) * 16);
        GFINISH(4, gr, WS_GU + (((2 * i + hf) * 176 + ft) * 32 + l32) * 16, 8);
        float hm[2];
#pragma unroll
        for (int e = 0; e < 2; ++e) {
          const float gv = xsum32(u2f(e ? gr[0].z : gr[0].x) + u2f(e ? gr[1].z : gr[1].x));
          const float uv = xsum32(u2f(e ? gr[2].z : gr[2].x) + u2f(e ? gr[3].z : gr[3].x));
          const float yy = 0.7978845608028654f * (gv + 0.044715f * gv * gv * gv);
          const float th = 1.f - 2.f / (__expf(2.f * yy) + 1.f);
          hm[e] = 0.5f * gv * (1.f + th) * uv;
        }
        const unsigned a2 = pk2(hm[0], hm[1]), b2 = dppu<0xb1>(a2);
        if (lane < 32 && !(lane & 1))
          __builtin_amdgcn_raw_buffer_store_b128((v4u){a2, tag, b2, tag}, wsr, WS_HM + (ft * 16 + (lane >> 1)) * 16, 0, AUX_SC1);
      }
      STAMP(4);
    }
  } else {
    f32x16 sacc;
    {
#pragma unroll
      for (int i = 0; i < 16; ++i) sacc[i] = 0.f;
      const unsigned tz = (hf ^ zl) * 16;
      const unsigned qb = L_S + l32 * 256 + tz;
      const unsigned kb = L_CKV + (wave * 32 + l32) * 256 + tz;
      v4u kf[32];
#pragma unroll
      for (int blk = 0; blk < 3; ++blk) {
        lds_wait(L + L_SY, 0x0101u << blk);
#pragma unroll
        for (int s = 0; s < 8; ++s) kf[blk * 8 + s] = *(const LAS v4u*)(L + (kb ^ (s * 32)) + blk * 32768);
      }
      lds_wait(L + L_SY + 4, 0x100);
      const v4u hole = GLOAD(WS_QA + bA * 32768 + 3 * 8192 + 224 + hf * 16);
      lds_wait(L + L_SY, 0x0808);
#pragma unroll
      for (int s = 0; s < 8; ++s) kf[24 + s] = *(const LAS v4u*)(L + (kb ^ (s * 32)) + 3 * 32768);
      STAMP(3);
#pragma unroll
      for (int blk = 0; blk < 4; ++blk) {
        lds_wait(L + L_SY, 0x01010000u << blk);
        if (blk == 0) STAMP(4);
        v4u qf[8];
#pragma unroll
        for (int s = 0; s < 8; ++s) qf[s] = *(const LAS v4u*)(L + (qb ^ (s * 32)) + blk * 8192);
        if (blk == 3 && l32 == 0) qf[7] = hole;
#pragma unroll
        for (int s = 0; s < 8; ++s) sacc = mfma32(kf[blk * 8 + s], qf[s], sacc);
        if (blk == 1) LBYTE(8 + wave, 1);
      }
    }
    STAMP(5);
    GSTART(4, tw, ((i & 2) ? h1 : h0) + (i & 1) * 16);
    lds_wait(L + L_SY + 8, 0x01010101);
    float mx = fmaxf(fmaxf(fmaxf(sacc[0], sacc[1]), fmaxf(sacc[2], sacc[3])), fmaxf(fmaxf(sacc[4], sacc[5]), fmaxf(sacc[6], sacc[7])));
    mx = fmaxf(mx, fmaxf(fmaxf(fmaxf(sacc[8], sacc[9]), fmaxf(sacc[10], sacc[11])), fmaxf(fmaxf(sacc[12], sacc[13]), fmaxf(sacc[14], sacc[15]))));
    mx = __builtin_ceilf(xmax32(mx));
    float ls = 0.f;
#pragma unroll
    for (int q = 0; q < 4; ++q) {
      const unsigned p0 = pk2(__builtin_amdgcn_exp2f(sacc[4 * q] - mx), __builtin_amdgcn_exp2f(sacc[4 * q + 1] - mx));
      const unsigned p1 = pk2(__builtin_amdgcn_exp2f(sacc[4 * q + 2] - mx), __builtin_amdgcn_exp2f(sacc[4 * q + 3] - mx));
      *(LAS v2u*)(L + L_P + l32 * 256 + (((wave * 4 + q) ^ zl) * 16) + hf * 8) = (v2u){p0, p1};
      ls = dot_ones(p1, dot_ones(p0, ls));
    }
    ls = xsum32(ls);
    *(LAS float*)(L + L_ST + hf * 512 + l32 * 16 + wave * 4) = hf ? ls : mx;
  }
  BAR();
  const unsigned wb = L_CKV + (wave >> 1) * 32768;
  const unsigned wq8 = (wave & 1) * 8;
  f32x16 o0, o1;
  {
    v4u pf[8];
    const f32x4 mv = *(const LAS f32x4*)(L + L_ST + l32 * 16), lv = *(const LAS f32x4*)(L + L_ST + 512 + l32 * 16);
    {
      const unsigned pbs = L_P + l32 * 256 + (hf ^ zl) * 16;
#pragma unroll
      for (int ks = 0; ks < 8; ++ks) pf[ks] = *(const LAS v4u*)(L + (pbs ^ (ks * 32)));
    }
    const float M = fmaxf(fmaxf(mv[0], mv[1]), fmaxf(mv[2], mv[3]));
#pragma unroll
    for (int w = 0; w < 4; ++w) {
      const unsigned sh = (unsigned)fminf(M - mv[w], 255.f) * 0x00800080u;
#pragma unroll
      for (int e = 0; e < 2; ++e) {
        v4u t = pf[2 * w + e];
        pf[2 * w + e] = (v4u){pksubsat(t.x, sh), pksubsat(t.y, sh), pksubsat(t.z, sh), pksubsat(t.w, sh)};
      }
    }
    if (wave == 5) {
      const float lt = (lv[0] * __builtin_amdgcn_exp2f(mv[0] - M) + lv[1] * __builtin_amdgcn_exp2f(mv[1] - M)) +
                       (lv[2] * __builtin_amdgcn_exp2f(mv[2] - M) + lv[3] * __builtin_amdgcn_exp2f(mv[3] - M));
      if (lane < 32)
        __builtin_amdgcn_raw_buffer_store_b64((v2u){f2u(M), f2u(lt)}, wsr, WS_FL + ((((lane >> 2) * 8 + bA) * 32 + chunk) * 4 + (lane & 3)) * 8, 0, AUX_SC1);
    }
#pragma unroll
    for (int i = 0; i < 16; ++i) { o0[i] = 0.f; o1[i] = 0.f; }
    const unsigned blk = (lane >> 4) & 1, q = (lane & 15) >> 2, pp = lane & 3;
    unsigned ta[2];
#pragma unroll
    for (int tt = 0; tt < 2; ++tt) {
      const unsigned row = 8 * hf + 4 * tt + q;
      ta[tt] = wb + row * 256 + (((wq8 + 2 * blk + (pp >> 1)) ^ swz(row)) * 16) + 8 * (pp & 1);
    }
#pragma unroll
    for (int ks = 0; ks < 8; ++ks) {
      o0 = mfma32(pf[ks], tr_read2(L + ta[0] + ks * 4096, L + ta[1] + ks * 4096), o0);
      o1 = mfma32(pf[ks], tr_read2(L + (ta[0] ^ 64) + ks * 4096, L + (ta[1] ^ 64) + ks * 4096), o1);
    }
  }
  STAMP(6);
  {
    const unsigned pbase = WS_PART + ((((bA * 32 + 4 * (g4 >> 1) + (g4 & 1)) * 8 + wave) * 32 + chunk) * 16 + l16) * 8;
#pragma unroll
    for (int i = 0; i < 16; i += 2) {
      auto s = __builtin_amdgcn_permlane16_swap(pk2(o0[i], o1[i]), pk2(o0[i + 1], o1[i + 1]), false, false);
      __builtin_amdgcn_raw_buffer_store_b64((v2u){s[0], s[1]}, wsr, pbase + (8 * (i >> 2) + (i & 3)) * (8 * 32 * 128), 0, AUX_SC1);
    }
  }
  BAR();
  STAMP(7);
  {
    const unsigned cr = 2 * wave + (r8 >> 2) + (r8 & 1) * 32 + (r8 & 2) * 8;
    const u16* sv = p.wuv + ((size_t)(sl * 64) * NH + hA) * HD;
    GLDS(sv, cr * 8192 + pz * 16, T_V + wave * 1024);
    GLDS(sv + 64, cr * 8192 + pz * 16, T_V + 8192 + wave * 1024);
    const u16* so = p.wo + ((size_t)hB * HD + wave * 8) * DM + sl * 64;
    GLDS(so, r8 * 1024 + pz * 16, T_O + wave * 1024);
    GLDS(so + 64 * DM, r8 * 1024 + pz * 16, T_O + 8192 + wave * 1024);
    GLDS(p.wd + (size_t)task0 * 8 * DM + sl * 64, min(wave * 8 + (int)r8, ntask * 8 - 1) * 1024 + pz * 16, T_D + wave * 1024);
  }
  VMWAIT(5);
  if (lds_ticket(L + L_SY + 20, lane) == 7) {
    if (lane < 8) __builtin_amdgcn_raw_buffer_store_b32(tag, wsr, WS_PF + lane * 8192 + (bA * 4 + (chunk >> 3)) * 256 + (chunk & 7) * 4, 0, AUX_SC1);
  }
  STAMP(8);
  if (wave < 4) GFINISH(4, tw, ((i & 2) ? h1 : h0) + (i & 1) * 16, 1);
  VMWAIT(0);
  BAR();
  STAMP(15);
  v4u av[2], ao[4];
  f32x4 accE = {0.f, 0.f, 0.f, 0.f};
  av[0] = FRAG(T_V + (wave >> 2) * 8192, wave & 3, 0);
  av[1] = FRAG(T_V + (wave >> 2) * 8192, wave & 3, 1);
  if (wave < 4) {
#pragma unroll
    for (int ks = 0; ks < 4; ++ks) ao[ks] = FRAG(T_O, wave, ks);
    accE = mfma16(FRAG(T_D, wave, 0), (v4u){tw[0].x, tw[0].z, tw[1].x, tw[1].z}, accE);
    accE = mfma16(FRAG(T_D, wave, 1), hon ? (v4u){tw[2].x, tw[2].z, tw[3].x, tw[3].z} : (v4u){0u, 0u, 0u, 0u}, accE);
  }
  {
#pragma nounroll
    for (unsigned spin = 0; spin < SPIN_CAP; ++spin) {
      if (flg_ok(GLOAD(WS_PF + xg * 8192 + (wave * 4 + ((lane >> 1) & 3)) * 256 + (lane & 1) * 16), tag)) break;
      __builtin_amdgcn_s_sleep(1);
    }
    asm volatile("" ::: "memory");
    STAMP(14);
    v2u pr[9];
    const unsigned po = WS_PART + ((((wave * 32 + hA) * 8 + sl) * 32) * 16 + lane) * 8;
    const unsigned fo = WS_FL + (((xg * 8 + wave) * 32 + l32) * 4 + (hA & 3)) * 8;
    pr[8] = __builtin_amdgcn_raw_buffer_load_b64(wsr, fo, 0, AUX_SC1);
#pragma unroll
    for (int i = 0; i < 8; ++i) pr[i] = __builtin_amdgcn_raw_buffer_load_b64(wsr, po + i * 512, 0, AUX_SC1);
    const float mj = u2f(pr[8].x);
    float M = mj;
    M = fmaxf(M, dppm<0x121>(M)); M = fmaxf(M, dppm<0x122>(M)); M = fmaxf(M, dppm<0x124>(M)); M = fmaxf(M, dppm<0x128>(M));
    M = xmax16(M);
    const float wj = bfhi(f2u(__builtin_amdgcn_exp2f(mj - M)));
    float Ls = wj * u2f(pr[8].y);
    Ls += dppm<0x121>(Ls); Ls += dppm<0x122>(Ls); Ls += dppm<0x124>(Ls); Ls += dppm<0x128>(Ls);
    Ls = xsum16(Ls);
    const float inv = __builtin_amdgcn_rcpf(Ls);
    const unsigned e4 = l16 & 3;
    const unsigned sh = (e4 & 1) ? 0u : 16u, k0 = e4 < 2 ? ~0u : 0u;
    unsigned wa[16];
#pragma unroll
    for (int u = 0; u < 8; ++u) {
      const unsigned w = (unsigned)__builtin_amdgcn_ds_bpermute((g4 + 4 * u) * 4, f2u(wj)) >> sh;
      wa[2 * u] = w & k0; wa[2 * u + 1] = w & ~k0;
    }
    f32x4 acc = {0.f, 0.f, 0.f, 0.f};
#pragma unroll
    for (int u = 0; u < 4; ++u)
      acc = mfma16((v4u){wa[4 * u], wa[4 * u + 1], wa[4 * u + 2], wa[4 * u + 3]}, (v4u){pr[2 * u].x, pr[2 * u].y, pr[2 * u + 1].x, pr[2 * u + 1].y}, acc);
    if (lane < 16) *(LAS v2u*)(L + L_CL + wave * 144 + lane * 8) = (v2u){pk2(acc[0] * inv, acc[1] * inv), pk2(acc[2] * inv, acc[3] * inv)};
    BAR();
  }
  const unsigned one = (l16 & 1) ? 0x3f800000u : 0x00003f80u;
  const unsigned s0 = (l16 & 2) ? 0u : one, s1 = (l16 & 2) ? one : 0u;
  {
    const v4u b0 = *(const LAS v4u*)(L + L_CL + (l16 & 7) * 144 + g4 * 16), b1 = *(const LAS v4u*)(L + L_CL + (l16 & 7) * 144 + 64 + g4 * 16);
    f32x4 acc = {0.f, 0.f, 0.f, 0.f};
    acc = mfma16(av[0], b0, acc);
    acc = mfma16(av[1], b1, acc);
    if (l16 < 8)
      __builtin_amdgcn_raw_buffer_store_b128((v4u){pk2(acc[0], acc[1]), tag, pk2(acc[2], acc[3]), tag}, wsr,
                                             WS_CX + ((((hA * 8 + wave) * 8 + sl) * 8 + l16) * 4 + g4) * 16, 0, AUX_SC1);
    STAMP(9);
    v4u cr[4];
    const unsigned co = WS_CX + ((hB * 8 + wave) * 256 + lane) * 16;
    if (CDELAY) __builtin_amdgcn_s_sleep(CDELAY);
#pragma nounroll
    for (unsigned spin = 0; spin < SPIN_CAP; ++spin) {
#pragma unroll
      for (int i = 0; i < 4; ++i) cr[i] = GLOAD(co + i * 1024);
      if (__all((cr[0].y == tag) & (cr[0].w == tag) & (cr[1].y == tag) & (cr[1].w == tag) & (cr[2].y == tag) & (cr[2].w == tag) & (cr[3].y == tag) & (cr[3].w == tag))) break;
      __builtin_amdgcn_s_sleep(1);
    }
    STAMP(10);
    {
      const bool on = ((l16 >> 2) & 1) == (g4 & 1);
      const unsigned t0 = on ? s0 : 0u, t1 = on ? s1 : 0u;
      f32x4 a = {0.f, 0.f, 0.f, 0.f};
      a = mfma16((v4u){t0, t1, t0, t1}, (v4u){cr[0].x, cr[0].z, cr[1].x, cr[1].z}, a);
      a = mfma16((v4u){t0, t1, t0, t1}, (v4u){cr[2].x, cr[2].z, cr[3].x, cr[3].z}, a);
      if (lane < 32) *(LAS v2u*)(L + L_CX + (lane >> 2) * 272 + wave * 32 + (lane & 3) * 8) = (v2u){pk2(a[0], a[1]), pk2(a[2], a[3])};
    }
    BAR();
  }
  if (wave < 4) {
#pragma unroll
    for (int ks = 0; ks < 4; ++ks) accE = mfma16(ao[ks], *(const LAS v4u*)(L + L_CX + (l16 & 7) * 272 + ks * 64 + g4 * 16), accE);
    if (l16 < 8)
      __builtin_amdgcn_raw_buffer_store_b128((v4u){pk2(accE[0], accE[1]), tag, pk2(accE[2], accE[3]), tag}, wsr,
                                             WS_OG + ((((sl * 4 + wave) * 32 + hB) * 8 + l16) * 4 + g4) * 16, 0, AUX_SC1);
    STAMP(11);
  } else if (wave == 4) {
    const unsigned oo = WS_OG + ((((wg & 31) * 32 + (lane >> 2)) * 8 + (wg >> 5)) * 4 + (lane & 3)) * 16;
    const v2u xw = *(const GAS v2u*)(p.x + wg * 16 + (lane & 3) * 4);
    v4u r0, r1;
    if (ODELAY) __builtin_amdgcn_s_sleep(ODELAY);
#pragma nounroll
    for (unsigned spin = 0; spin < SPIN_CAP; ++spin) {
      r0 = GLOAD(oo);
      r1 = GLOAD(oo + 16 * 512);
      if (__all((r0.y == tag) & (r0.w == tag) & (r1.y == tag) & (r1.w == tag))) break;
      __builtin_amdgcn_s_sleep(1);
    }
    f32x4 acc = {0.f, 0.f, 0.f, 0.f};
    acc = mfma16((v4u){s0, s1, s0, s1}, (v4u){r0.x, r0.z, r1.x, r1.z}, acc);
#pragma unroll
    for (int j = 0; j < 4; ++j) { acc[j] += dppm<0x124>(acc[j]); acc[j] += dppm<0x128>(acc[j]); }
    if (lane < 4)
      *(GAS v2u*)(p.out + wg * 16 + lane * 4) = (v2u){pk2(bflo(xw.x) + acc[0], bfhi(xw.x) + acc[1]), pk2(bflo(xw.y) + acc[2], bfhi(xw.y) + acc[3])};
    if (wg == 0 && lane == 0) __builtin_amdgcn_raw_buffer_store_b32(tag, wsr, WS_EPOCH, 0, AUX_SC1);
    STAMP(11);
  }
}

extern "C" void kernel_launch(void* const* d_in, const int* in_sizes, int n_in, void* d_out, int out_size, void* d_ws, size_t ws_size,
                              hipStream_t stream) {
  Params p;
  p.x = (const u16*)d_in[0]; p.ckv = (const u16*)d_in[1]; p.wq = (const u16*)d_in[2]; p.wuk = (const u16*)d_in[3];
  p.wuv = (const u16*)d_in[4]; p.wo = (const u16*)d_in[5]; p.wg = (const u16*)d_in[7]; p.wu = (const u16*)d_in[8]; p.wd = (const u16*)d_in[9];
  p.out = (u16*)d_out; p.ws = (unsigned char*)d_ws;
  (void)hipFuncSetAttribute((const void*)fused_kernel, hipFuncAttributeMaxDynamicSharedMemorySize, LDS_BYTES);
  fused_kernel<<<NWG, NTHR, LDS_BYTES, stream>>>(p);
}
```

```cpp
#define DK 0
#define DW 4
#define DCL 107
#include <hip/hip_runtime.h>
#include <hip/hip_bf16.h>
#include <stdint.h>

#define STAMPS 0
#define CODE_LINES 160
#define WINS 16
#define WINF 20
#define WIN0 6
#define ADELAY 120
#define PDELAY 60
#define CDELAY 8
#define ODELAY 8
#define SDELAY 0
#define WMAX (WINS > WINF ? WINS : WINF)

typedef unsigned v4u __attribute__((ext_vector_type(4)));
typedef unsigned v2u __attribute__((ext_vector_type(2)));
typedef float f32x2 __attribute__((ext_vector_type(2)));
typedef float f32x4 __attribute__((ext_vector_type(4)));
typedef float f32x16 __attribute__((ext_vector_type(16)));
typedef short bf16x8 __attribute__((ext_vector_type(8)));
typedef short s16x4 __attribute__((ext_vector_type(4)));
typedef unsigned short u16x2 __attribute__((ext_vector_type(2)));
typedef __bf16 bf16x2 __attribute__((ext_vector_type(2)));
typedef unsigned short u16;
#define LAS __attribute__((address_space(3)))
#define GAS __attribute__((address_space(1)))

constexpr int NB = 8, NH = 32, SK = 4096, HD = 128, DM = 512, DC = 512, FF = 1376;
constexpr int NWG = 256, NTHR = 512;
constexpr unsigned WS_EPOCH = 0;
constexpr unsigned WS_QF = 4096;
constexpr unsigned WS_PF = WS_QF + 65536;
constexpr unsigned WS_QA = WS_PF + 65536;
constexpr unsigned WS_GU = WS_QA + 2 * 8 * 32 * 1024;
constexpr unsigned WS_HM = WS_GU + 8 * 176 * 32 * 16;
constexpr unsigned WS_FL = WS_HM + 176 * 8 * 2 * 16;
constexpr unsigned WS_CX = WS_FL + 8 * 8 * 32 * 4 * 16;
constexpr unsigned WS_OG = WS_CX + 32 * 8 * 8 * 8 * 4 * 16;
constexpr unsigned WS_PART = 4u << 20;
constexpr unsigned WS_END = WS_PART + (8u << 20);
constexpr unsigned WS_STAMP = 32u << 20;
static_assert(WS_OG + 32 * 32 * 8 * 4 * 16 <= WS_PART, "ws map");
constexpr unsigned L_CKV = 0;
constexpr unsigned L_S = 128 * 1024;
constexpr unsigned L_SY = L_S + 24576 + 224;
constexpr unsigned L_QP = L_S;
constexpr unsigned L_LN = L_S + 8192;
constexpr unsigned L_DUMP = L_S + 9216;
constexpr unsigned L_X = L_S + 16384;
constexpr unsigned L_P = L_S;
constexpr unsigned L_ST = L_S + 8192;
constexpr unsigned L_CL = L_S + 16384;
constexpr unsigned L_CX = L_S + 16384 + 2048;
constexpr unsigned LDS_BYTES = 160 * 1024;
constexpr int AUX_NT = 2, AUX_SC1 = 16;
constexpr unsigned SPIN_CAP = 1u << 18;

struct Params {
  const u16 *x, *ckv, *wq, *wuk, *wuv, *wo, *wg, *wu, *wd;
  u16* out;
  unsigned char* ws;
};

__device__ __forceinline__ float u2f(unsigned u) { return __uint_as_float(u); }
__device__ __forceinline__ unsigned f2u(float f) { return __float_as_uint(f); }
__device__ __forceinline__ float bflo(unsigned w) { return u2f(w << 16); }
__device__ __forceinline__ float bfhi(unsigned w) { return u2f(w & 0xffff0000u); }
__device__ __forceinline__ unsigned pk2(float lo, float hi) {
  f32x2 f = {lo, hi};
  return __builtin_bit_cast(unsigned, __builtin_convertvector(f, bf16x2));
}
__device__ __forceinline__ float dot_ones(unsigned a, float c) {
  return __builtin_amdgcn_fdot2_f32_bf16(__builtin_bit_cast(bf16x2, a), __builtin_bit_cast(bf16x2, 0x3f803f80u), c, false);
}
__device__ __forceinline__ unsigned pksubsat(unsigned a, unsigned b) {
  return __builtin_bit_cast(unsigned, __builtin_elementwise_sub_sat(__builtin_bit_cast(u16x2, a), __builtin_bit_cast(u16x2, b)));
}
__device__ __forceinline__ f32x4 mfma16(v4u a, v4u b, f32x4 c) {
  return __builtin_amdgcn_mfma_f32_16x16x32_bf16(__builtin_bit_cast(bf16x8, a), __builtin_bit_cast(bf16x8, b), c, 0, 0, 0);
}
__device__ __forceinline__ f32x16 mfma32(v4u a, v4u b, f32x16 c) {
  return __builtin_amdgcn_mfma_f32_32x32x16_bf16(__builtin_bit_cast(bf16x8, a), __builtin_bit_cast(bf16x8, b), c, 0, 0, 0);
}
__device__ __forceinline__ unsigned swz(unsigned row) { return ((row & 3) << 2) | ((row >> 2) & 3); }
#define BAR() do { asm volatile("s_waitcnt lgkmcnt(0)" ::: "memory"); __builtin_amdgcn_s_barrier(); asm volatile("" ::: "memory"); } while (0)
#define VMWAIT(n) do { asm volatile("" ::: "memory"); __builtin_amdgcn_s_waitcnt(0x0f70 | ((n) & 15) | (((n) >> 4) << 14)); asm volatile("" ::: "memory"); } while (0)
#define LGKM0() asm volatile("s_waitcnt lgkmcnt(0)" ::: "memory")
#define GLDS_(OP, sbase, voff, dst) asm volatile("s_mov_b32 m0, %2\n\ts_nop 0\n\t" OP :: "v"((unsigned)(voff)), "s"((const void*)(sbase)), "s"((unsigned)(dst)) : "memory", "m0")
#define GLDS(sbase, voff, dst) GLDS_("global_load_lds_dwordx4 %0, %1 nt", sbase, voff, dst)
#define GLDSX(sbase, voff, dst) GLDS_("global_load_lds_dwordx4 %0, %1", sbase, voff, dst)
#define GLDS4(sbase, voff, dst) GLDS_("global_load_lds_dword %0, %1 nt", sbase, voff, dst)
#define WLOAD(p) __builtin_nontemporal_load(p)
#define LBYTE(off, v) (*(volatile LAS unsigned char*)(L + L_SY + (off)) = (v))
__device__ __forceinline__ v2u tr_read(LAS unsigned char* a) {
  return __builtin_bit_cast(v2u, __builtin_amdgcn_ds_read_tr16_b64_v4i16((LAS s16x4*)a));
}
__device__ __forceinline__ v4u tr_read2(LAS unsigned char* a, LAS unsigned char* b) {
  const v2u x = tr_read(a), y = tr_read(b);
  return (v4u){x.x, x.y, y.x, y.y};
}
template <int CTRL>
__device__ __forceinline__ unsigned dppu(unsigned v) { return __builtin_amdgcn_update_dpp(v, v, CTRL, 0xf, 0xf, false); }
template <int CTRL>
__device__ __forceinline__ float dppm(float v) { return u2f(dppu<CTRL>(f2u(v))); }
__device__ __forceinline__ float xsum16(float v) {
  auto s = __builtin_amdgcn_permlane16_swap(f2u(v), f2u(v), false, false);
  return u2f(s[0]) + u2f(s[1]);
}
__device__ __forceinline__ float xmax16(float v) {
  auto s = __builtin_amdgcn_permlane16_swap(f2u(v), f2u(v), false, false);
  return fmaxf(u2f(s[0]), u2f(s[1]));
}
__device__ __forceinline__ float xsum32(float v) {
  auto s = __builtin_amdgcn_permlane32_swap(f2u(v), f2u(v), false, false);
  return u2f(s[0]) + u2f(s[1]);
}
__device__ __forceinline__ float xmax32(float v) {
  auto s = __builtin_amdgcn_permlane32_swap(f2u(v), f2u(v), false, false);
  return fmaxf(u2f(s[0]), u2f(s[1]));
}
__device__ __forceinline__ bool rec_ok(v4u r, unsigned tag) { return __all((r.y == tag) & (r.w == tag)); }
__device__ __forceinline__ bool flg_ok(v4u r, unsigned tag) { return __all((r.x == tag) & (r.y == tag) & (r.z == tag) & (r.w == tag)); }
__device__ __forceinline__ unsigned lds_wait(LAS unsigned char* a, unsigned mask) {
  unsigned w = 0;
#pragma nounroll
  for (unsigned spin = 0; spin < (SPIN_CAP << 4); ++spin) {
    w = __builtin_amdgcn_readfirstlane(*(volatile LAS unsigned*)a);
    if ((w & mask) == mask) break;
    __builtin_amdgcn_s_sleep(1);
  }
  asm volatile("" ::: "memory");
  return w;
}

__device__ __forceinline__ unsigned long long lds_peek64(LAS unsigned char* a) {
  const v2u w = *(volatile LAS v2u*)a;
  return (unsigned long long)__builtin_amdgcn_readfirstlane(w.x) | ((unsigned long long)__builtin_amdgcn_readfirstlane(w.y) << 32);
}
__device__ __forceinline__ unsigned long long lds_wait64(LAS unsigned char* a, unsigned long long mask) {
  unsigned long long w = 0;
#pragma nounroll
  for (unsigned spin = 0; spin < (SPIN_CAP << 4); ++spin) {
    w = lds_peek64(a);
    if ((w & mask) == mask) break;
    __builtin_amdgcn_s_sleep(1);
  }
  asm volatile("" ::: "memory");
  return w;
}
__device__ __forceinline__ unsigned lds_ticket(LAS unsigned char* a, int lane) {
  unsigned t = 0;
  if (lane == 0) t = __hip_atomic_fetch_add((LAS unsigned*)a, 1u, __ATOMIC_RELAXED, __HIP_MEMORY_SCOPE_WORKGROUP);
  return __builtin_amdgcn_readfirstlane(t);
}
__device__ __forceinline__ unsigned addbf(unsigned a, unsigned b, unsigned k) {
  const float lo = __builtin_amdgcn_fdot2_f32_bf16(__builtin_bit_cast(bf16x2, b), __builtin_bit_cast(bf16x2, k), bflo(a), false);
  const float hi = __builtin_amdgcn_fdot2_f32_bf16(__builtin_bit_cast(bf16x2, b), __builtin_bit_cast(bf16x2, k << 16), bfhi(a), false);
  return pk2(lo, hi);
}
__device__ __forceinline__ v4u addbf4(v4u a, v4u b, unsigned k) { return (v4u){addbf(a.x, b.x, k), addbf(a.y, b.y, k), addbf(a.z, b.z, k), addbf(a.w, b.w, k)}; }

#define STAMP(k) do { if ((k) == 0) dt0 = dts = (unsigned)__builtin_amdgcn_s_memrealtime(); else if ((k) == DK) dts = (unsigned)__builtin_amdgcn_s_memrealtime(); } while (0)

#define GLOAD(ADDR) __builtin_amdgcn_raw_buffer_load_b128(wsr, (ADDR), 0, AUX_SC1)
#define GFINISH(N, qr, ADDR, SLP)                                                                            \
  do {                                                                                                       \
    unsigned done = 0;                                                                                       \
    _Pragma("nounroll") for (unsigned spin = 0; spin < SPIN_CAP; ++spin) {                                   \
      _Pragma("unroll") for (int i = 0; i < N; ++i)                                                          \
        if (!((done >> i) & 1)) { if (rec_ok(qr[i], tag)) done |= 1u << i; }                                 \
      if (done == (1u << N) - 1u) break;                                                                     \
      if (SLP) __builtin_amdgcn_s_sleep(SLP);                                                                \
      _Pragma("unroll") for (int i = 0; i < N; ++i)                                                          \
        if (!((done >> i) & 1)) qr[i] = GLOAD(ADDR);                                                         \
    }                                                                                                        \
  } while (0)
#define GSTART(N, qr, ADDR) do { _Pragma("unroll") for (int i = 0; i < N; ++i) qr[i] = GLOAD(ADDR); } while (0)

__global__ void __launch_bounds__(NTHR, 2) fused_kernel(Params p) {
  extern __shared__ __attribute__((aligned(16))) unsigned char lds_raw[];
  LAS unsigned char* L = (LAS unsigned char*)lds_raw;
  const int tid = threadIdx.x, lane = tid & 63;
  const int wave = __builtin_amdgcn_readfirstlane(tid >> 6);
  const int wg = blockIdx.x, xg = wg & 7, r = wg >> 3;
  const int bA = xg, chunk = r;
  const int hA = xg * 4 + (r >> 3), sl = r & 7;
  const int hB = ((xg + 1) & 7) * 4 + (r >> 3);
  const bool clsF = r & 1;
  const int sidx = (r >> 1) * 8 + xg;
#define RP 0
  const int rf = (r + RP) & 31;
  const int hF = xg * 4 + (rf >> 3), dh = (rf >> 2) & 1, ch = (rf >> 1) & 1;
  const int ntask = hB < 12 ? 6 : 5, task0 = hB < 12 ? 6 * hB : 12 + 5 * hB;
  const __amdgpu_buffer_rsrc_t wsr = __builtin_amdgcn_make_buffer_rsrc((void*)p.ws, 0, WS_END, 0x00020000);
  const int l16 = lane & 15, g4 = lane >> 4;
  const int l32 = lane & 31, hf = lane >> 5;
  const unsigned r8 = lane >> 3, q8 = lane & 7;
  unsigned dt0, dts;
  STAMP(0);
  unsigned long long pc0;
  asm volatile("s_getpc_b64 %0" : "=s"(pc0));
  unsigned tag, tagv;
#define TAGLOAD() tagv = __builtin_amdgcn_raw_buffer_load_b32(wsr, WS_EPOCH, 0, 0)
#define TAGSET() tag = __builtin_amdgcn_readfirstlane(tagv) + 1u
  if (wave == 5) {
#pragma unroll
    for (int i = 0; i < 8; ++i) GLDSX(p.x + i * DM, (lane ^ (2 * i)) * 16, L_X + i * 1024);
    STAMP(4);
  } else if (wave == 4) {
    if (lane < 8) *(LAS unsigned*)(L + L_SY + lane * 4) = (lane == 3 && !clsF) ? (sidx >= 48 ? 0x07070700 : 0x07070000) : ((lane == 0 && !clsF) ? 0x01000000 : 0);
  } else if (wave == 6) {
    GLDS4((const void*)(pc0 & ~127ull), min((unsigned)((r & 3) * 64 + lane), (unsigned)CODE_LINES - 1u) * 128, L_DUMP + 2048);
  } else if (wave == 7) {
    GLDS4(p.ws, lane < 4 ? WS_PART + lane * (2u << 20) : (lane == 4 ? 2048 : WS_OG), L_DUMP + 2304);
    GLDS4(p.wuv + ((size_t)(sl * 64) * NH + hA) * HD, 0, L_DUMP + 2304);
    GLDS4(p.wo + (size_t)hB * HD * DM, 0, L_DUMP + 2304);
    GLDS4(p.wd, 0, L_DUMP + 2304);
    GLDS4(p.out, 0, L_DUMP + 2304);
  } else if (!clsF) {
    const size_t po = (size_t)((r >> 1) * 32 + wave * 8) * (NH * HD) + xg * 512;
    GLDS4(p.wq + po, r8 * 8192 + q8 * 128, L_DUMP + wave * 512);
    GLDS4(p.wuk + po, r8 * 8192 + q8 * 128, L_DUMP + wave * 512 + 256);
  }
  v4u tw[10];
  auto tail_loads = [&]() {
#pragma unroll
    for (int e = 0; e < 2; ++e) {
      const int u = (wave & 3) + 4 * e;
      const u16* so = p.wo + ((size_t)hB * HD + u * 16 + r8) * DM + sl * 64 + q8 * 8;
      tw[5 * e + 0] = WLOAD((const GAS v4u*)so);
      tw[5 * e + 1] = WLOAD((const GAS v4u*)(so + 8 * DM));
      const u16* sv = p.wuv + ((size_t)(sl * 64 + u * 8 + r8) * NH + hA) * HD + q8 * 8;
      tw[5 * e + 2] = WLOAD((const GAS v4u*)sv);
      tw[5 * e + 3] = WLOAD((const GAS v4u*)(sv + 64));
      const int fr = min(u * 8 + (int)r8, ntask * 8 - 1);
      tw[5 * e + 4] = WLOAD((const GAS v4u*)(p.wd + (size_t)(task0 * 8 + fr) * DM + sl * 64 + q8 * 8));
    }
  };
  const bool hon = 4 + g4 < ntask;
  const unsigned h0 = WS_HM + ((task0 + g4) * 8 + (l16 & 7)) * 32, h1 = WS_HM + ((task0 + (hon ? 4 + g4 : 0)) * 8 + (l16 & 7)) * 32;
  const unsigned zl = swz(l32);

  if (wave >= 4) {
    bool early = false;
    if (wave >= 6) {
      const int v = wave - 6;
      const u16* srcb = p.ckv + ((size_t)bA * SK + chunk * 128 + 4 * v) * DC;
      const unsigned vo0 = g4 * 1024 + (l16 ^ ((g4 << 2) | v)) * 16, vo1 = g4 * 1024 + (l16 ^ ((g4 << 2) | (2 + v))) * 16;
      BAR();
      int mode = clsF ? 0 : 1;
      lds_wait(L + L_SY + 12, 0x01010101);
      lds_wait(L + L_SY, 0x01000000);
      if (SDELAY && !clsF) __builtin_amdgcn_s_sleep(SDELAY);
      STAMP(3);
#pragma nounroll
      for (int blk = 0; blk < 4; ++blk) {
        if (blk == 1 && clsF) lds_wait(L + L_SY + 12, 0x02020202);
        if (blk == 3) lds_wait(L + L_SY + 12, 0x04040404);
#pragma unroll
        for (int n = 0; n < 16; ++n) {
          GLDS(srcb + n * 8 * DC + blk * 128, (n & 1) ? vo1 : vo0, L_CKV + blk * 32768 + (n * 2 + v) * 1024);
          if (mode == 0) {
            VMWAIT(WIN0 - 1);
            if (__builtin_amdgcn_readfirstlane(*(volatile LAS unsigned*)(L + L_SY + 20)) & 0x10000) mode = 2;
          } else if (mode == 1) VMWAIT(WINS - 1);
          else VMWAIT(WINF - 1);
          if (n == ((WMAX - 2) & 15) && blk > (WMAX - 2) / 16) LBYTE(v, (1 << (blk - (WMAX - 2) / 16)) - 1);
        }
      }
      STAMP(1);
      early = __builtin_amdgcn_readfirstlane(*(volatile LAS unsigned*)(L + L_SY + 4)) == 0x01010101u;
      if (!early) {
        VMWAIT(16); LBYTE(v, 7);
        VMWAIT(0); LBYTE(v, 15);
        STAMP(2);
      }
    } else if (wave == 4) {
      BAR();
      TAGLOAD();
      if (clsF) {
        const u16* src = p.wuk + ((size_t)(ch * 256) * NH + hF) * HD + dh * 64;
        lds_wait(L + L_SY + 12, 0x01010101);
#pragma unroll
        for (int n = 0; n < 32; ++n) GLDS(src + (size_t)n * 8 * NH * HD, r8 * 8192 + (q8 ^ r8) * 16, 98304 + n * 1024);
        LBYTE(3, 1);
      }
      VMWAIT(0);
      if (clsF) LBYTE(3, 3);
      STAMP(3);
      TAGSET();
      lds_wait(L + L_SY + 12, 0x04040404);
      if (clsF) lds_wait(L + L_SY + 20, 0x10000);
      else __builtin_amdgcn_s_sleep(PDELAY);
      STAMP(1);
#pragma nounroll
      for (unsigned spin = 0; spin < SPIN_CAP; ++spin) {
        if (flg_ok(GLOAD(WS_QF + xg * 4096 + (l32 >> 1) * 256 + (lane & 1) * 16), tag)) break;
        __builtin_amdgcn_s_sleep(1);
      }
      asm volatile("" ::: "memory");
      LBYTE(21, 1);
      STAMP(2);
    } else {
      BAR();
      VMWAIT(0);
      LBYTE(2, 1);
      STAMP(1);
      {
        const unsigned xb = L_X + (l16 & 7) * 1024;
        const v4u ones = {0x3f803f80u, 0x3f803f80u, 0x3f803f80u, 0x3f803f80u};
        v4u xs[8];
#pragma unroll
        for (int t = 0; t < 8; ++t) xs[t] = *(const LAS v4u*)(L + xb + (((8 * t + 4 * (l16 >> 3) + g4) ^ (2 * (l16 & 7))) * 16));
        f32x4 G = {0.f, 0.f, 0.f, 0.f}, S = {0.f, 0.f, 0.f, 0.f};
#pragma unroll
        for (int t = 0; t < 8; ++t) { G = mfma16(xs[t], xs[t], G); S = mfma16(ones, xs[t], S); }
        float dg = (l16 & 2) ? ((l16 & 1) ? G[3] : G[2]) : ((l16 & 1) ? G[1] : G[0]);
        dg = g4 == (l16 >> 2) ? dg : 0.f;
        dg = xsum32(xsum16(dg));
        dg += dppm<0x128>(dg);
        const float s0 = S[0];
        const float mean = (s0 + dppm<0x128>(s0)) * (1.f / DM);
        const float rstd = __builtin_amdgcn_rsqf(fmaxf(dg * (1.f / DM) - mean * mean, 0.f) + 1e-5f);
        if (lane < 8) *(LAS v2u*)(L + L_LN + lane * 8) = (v2u){f2u(mean), f2u(rstd)};
        LGKM0();
        LBYTE(2, 3);
      }
      STAMP(3);
      TAGLOAD();
      TAGSET();
      if (!clsF && sidx < 86) {
        __builtin_amdgcn_s_sleep(ADELAY);
#pragma nounroll
        for (int ft = 2 * sidx; ft < 2 * sidx + 2; ++ft) {
          v4u gr[4];
          GSTART(4, gr, WS_GU + (((2 * i + hf) * 176 + ft) * 32 + l32) * 16);
          GFINISH(4, gr, WS_GU + (((2 * i + hf) * 176 + ft) * 32 + l32) * 16, 8);
          float hm[2];
#pragma unroll
          for (int e = 0; e < 2; ++e) {
            const float gv = xsum32(u2f(e ? gr[0].z : gr[0].x) + u2f(e ? gr[1].z : gr[1].x));
            const float uv = xsum32(u2f(e ? gr[2].z : gr[2].x) + u2f(e ? gr[3].z : gr[3].x));
            const float yy = 0.7978845608028654f * (gv + 0.044715f * gv * gv * gv);
            const float th = 1.f - 2.f / (__expf(2.f * yy) + 1.f);
            hm[e] = 0.5f * gv * (1.f + th) * uv;
          }
          const unsigned a2 = pk2(hm[0], hm[1]), b2 = dppu<0xb1>(a2);
          if (lane < 32 && !(lane & 1))
            __builtin_amdgcn_raw_buffer_store_b128((v4u){a2, tag, b2, tag}, wsr, WS_HM + (ft * 16 + (lane >> 1)) * 16, 0, AUX_SC1);
        }
        STAMP(2);
      }
    }
    if (!early) lds_wait64(L + L_SY, 0x0101010100000404ull);
    tail_loads();
    if (early) { VMWAIT(26); LBYTE(wave - 6, 7); }
    VMWAIT(10);
    if (early) { LBYTE(wave - 6, 15); STAMP(2); }
    if (wave >= 6) TAGLOAD();
  } else {
    if (clsF || wave == 0 || (wave == 1 && sidx < 48)) {
      const unsigned st = (clsF ? 32768 : 98304) + wave * 16384;
      const int t1 = wave * 128 + sidx, cg = (t1 % 88) >> 2, mat = t1 >= 88;
      const int kq1 = clsF ? wave : (t1 & 3);
      TAGLOAD();
      {
        const unsigned pz = q8 ^ (((r8 & 3) << 1) | (r8 >> 2));
        const u16* src; int stride; unsigned vo;
        if (clsF) { stride = NH * HD; src = p.wq + (size_t)(kq1 * 128) * stride + hF * HD + dh * 64; vo = r8 * 8192 + pz * 16; }
        else { stride = FF; src = (mat ? p.wu : p.wg) + (size_t)(kq1 * 128) * stride; vo = (r8 * FF + min(cg * 64 + (int)pz * 8, FF - 8)) * 2; }
#pragma unroll
        for (int i = 0; i < 16; ++i) {
          if (i == 8) BAR();
          GLDS(src + i * 8 * stride, vo, st + i * 1024);
        }
      }
      LBYTE(12 + wave, 1);
      lds_wait(L + L_SY, 0x010000);
      STAMP(15);
      v4u xr[4];
#pragma unroll
      for (int s = 0; s < 4; ++s) {
        xr[s] = *(const LAS v4u*)(L + L_X + (l16 & 7) * 1024 + (((16 * kq1 + 4 * s + g4) ^ (2 * (l16 & 7))) * 16));
        if (l16 >= 8) xr[s] = (v4u){0x3f803f80u, 0x3f803f80u, 0x3f803f80u, 0x3f803f80u};
      }
      f32x4 acc[4];
      {
        const unsigned rq = l16 >> 2, pc = (l16 & 3) >> 1, ph = (l16 & 1) * 8;
        const unsigned a0 = st + (8 * g4 + rq) * 128 + ((pc ^ (rq << 1)) * 16) + ph;
        const unsigned a1 = st + (8 * g4 + rq + 4) * 128 + ((pc ^ (rq << 1) ^ 1) * 16) + ph;
#pragma unroll
        for (int nt = 0; nt < 4; ++nt) acc[nt] = (f32x4){0.f, 0.f, 0.f, 0.f};
#pragma unroll
        for (int s = 0; s < 4; ++s) {
          if (s == 0) VMWAIT(12); else if (s == 1) VMWAIT(8); else if (s == 2) VMWAIT(4); else VMWAIT(0);
          if (s == 3) STAMP(1);
          v4u af[4];
#pragma unroll
          for (int nt = 0; nt < 4; ++nt) af[nt] = tr_read2(L + (a0 ^ (nt * 32)) + s * 4096, L + (a1 ^ (nt * 32)) + s * 4096);
#pragma unroll
          for (int nt = 0; nt < 4; ++nt) acc[nt] = mfma16(af[nt], xr[s], acc[nt]);
        }
      }
      lds_wait(L + L_SY, 0x020000);
      const v2u ln = *(const LAS v2u*)(L + L_LN + (l16 & 7) * 8);
      const float mean = u2f(ln.x), rstd = u2f(ln.y);
      LBYTE(12 + wave, clsF ? 3 : 7);
#pragma unroll
      for (int nt = 0; nt < 4; ++nt)
#pragma unroll
        for (int j = 0; j < 4; ++j) acc[nt][j] = (acc[nt][j] - mean * dppm<0x158>(acc[nt][j])) * rstd;
      if (!clsF) {
        TAGSET();
#pragma unroll
        for (int nt = 0; nt < 4; ++nt) {
          const unsigned c = dppu<0x128>(f2u(acc[nt][2])), d = dppu<0x128>(f2u(acc[nt][3]));
          __builtin_amdgcn_raw_buffer_store_b128((v4u){l16 < 8 ? f2u(acc[nt][0]) : c, tag, l16 < 8 ? f2u(acc[nt][1]) : d, tag}, wsr,
                                                 WS_GU + ((((mat * 4 + kq1) * 176 + cg * 8 + 2 * nt + (g4 >> 1)) * 8 + (l16 & 7)) * 4 + 2 * (g4 & 1) + (l16 >> 3)) * 16, 0, AUX_SC1);
        }
        STAMP(2);
      } else {
        if (l16 < 8) {
#pragma unroll
          for (int nt = 0; nt < 4; ++nt)
            *(LAS v2u*)(L + L_QP + (wave * 8 + l16) * 144 + nt * 32 + g4 * 8) = (v2u){pk2(acc[nt][0], acc[nt][1]), pk2(acc[nt][2], acc[nt][3])};
        }
        LGKM0();
        LBYTE(16 + wave, 1);
        STAMP(2);
        lds_wait(L + L_SY, 0x02000000);
        v4u wf[8];
#pragma unroll
        for (int n = 0; n < 8; ++n) wf[n] = *(const LAS v4u*)(L + 98304 + (wave * 64 + (n >> 1) * 16 + l16) * 128 + (((4 * (n & 1) + g4) ^ (l16 & 7)) * 16));
        lds_wait(L + L_SY + 16, 0x01010101);
        LBYTE(12 + wave, 7);
        v4u bq[4];
#pragma unroll
        for (int k = 0; k < 4; ++k) bq[k] = *(const LAS v4u*)(L + L_QP + ((k >> 1) * 16 + l16) * 144 + (k & 1) * 64 + g4 * 16);
        const float scale = 0.08838834764831845f * 1.4426950408889634f;
        unsigned yp[2][2];
#pragma unroll
        for (int cp = 0; cp < 2; ++cp) {
          f32x4 a0 = {0.f, 0.f, 0.f, 0.f}, a1 = {0.f, 0.f, 0.f, 0.f};
#pragma unroll
          for (int k = 0; k < 4; ++k) { a0 = mfma16(wf[cp * 4 + (k & 1)], bq[k], a0); a1 = mfma16(wf[cp * 4 + 2 + (k & 1)], bq[k], a1); }
          float y[4];
#pragma unroll
          for (int j = 0; j < 4; ++j) {
            const float keep = l16 < 8 ? a0[j] : a1[j], give = l16 < 8 ? a1[j] : a0[j];
            y[j] = (keep + dppm<0x128>(give)) * scale;
          }
          yp[cp][0] = pk2(y[0], y[1]); yp[cp][1] = pk2(y[2], y[3]);
        }
        {
          auto s0 = __builtin_amdgcn_permlane16_swap(yp[0][0], yp[1][0], false, false);
          auto s1 = __builtin_amdgcn_permlane16_swap(yp[0][1], yp[1][1], false, false);
          __builtin_amdgcn_raw_buffer_store_b128((v4u){s0[0], s1[0], s0[1], s1[1]}, wsr,
                                                 WS_QA + (((dh * 8 + (l16 & 7)) * 4 + ch * 2 + (wave >> 1)) * 32 + hF) * 256 + (wave & 1) * 128 +
                                                     (2 * (g4 & 1) + (l16 >> 3)) * 32 + (g4 >> 1) * 16, 0, AUX_SC1);
        }
        STAMP(12);
        VMWAIT(0);
        TAGSET();
        if (lds_ticket(L + L_SY + 24, lane) == 3) {
          if (lane < 8) __builtin_amdgcn_raw_buffer_store_b32(tag, wsr, WS_QF + lane * 4096 + (hF >> 1) * 256 + ((hF & 1) * 4 + dh * 2 + ch) * 4, 0, AUX_SC1);
          LBYTE(22, 1);
        }
        STAMP(3);
      }
    } else {
      TAGLOAD();
      BAR();
    }
    v4u hole;
    {
      const unsigned qo = WS_QA + (bA * 4 + wave) * 8192 + lane * 16;
      const unsigned qd = L_S + wave * 8192 + g4 * 256;
      unsigned k1 = 0x3f80u;
      asm volatile("" : "+s"(k1));
      lds_wait(L + L_SY + 20, 0x0100);
      v4u qa[16];
#pragma unroll
      for (int i = 0; i < 16; ++i) qa[i] = GLOAD(qo + (i >> 1) * 1024 + (i & 1) * 262144);
      const v4u ha = GLOAD(WS_QA + (bA * 4 + 3) * 8192 + 224 + hf * 16), hb = GLOAD(WS_QA + 262144 + (bA * 4 + 3) * 8192 + 224 + hf * 16);
#pragma unroll
      for (int i = 0; i < 8; ++i) {
        const v4u t = addbf4(qa[2 * i], qa[2 * i + 1], k1);
        const unsigned ad = (i == 0 && wave == 3 && lane >= 14 && lane < 16) ? 1024 + (l16 ^ 1) * 16 : i * 1024 + ((l16 ^ ((g4 << 2) | (i & 3))) * 16);
        *(volatile LAS v4u*)(L + qd + ad) = t;
      }
      LGKM0();
      LBYTE(4 + wave, 1);
      hole = addbf4(ha, hb, k1);
      STAMP(13);
    }
    f32x16 sacc;
    {
#pragma unroll
      for (int i = 0; i < 16; ++i) sacc[i] = 0.f;
      const unsigned tz = (hf ^ zl) * 16;
      const unsigned qb = L_S + l32 * 256 + tz;
      const unsigned kb = L_CKV + (wave * 32 + l32) * 256 + tz;
      v4u qf[2][8], kf[2][8];
      unsigned long long have = lds_wait64(L + L_SY, 0x0000000100000101ull);
      STAMP(4);
#pragma unroll
      for (int s = 0; s < 8; ++s) { kf[0][s] = *(const LAS v4u*)(L + (kb ^ (s * 32))); qf[0][s] = *(const LAS v4u*)(L + (qb ^ (s * 32))); }
#pragma unroll
      for (int blk = 0; blk < 4; ++blk) {
        if (blk < 3) {
          const unsigned long long need = (0x0101ull << (blk + 1)) | (1ull << (40 + 8 * blk));
          if ((have & need) != need) {
#pragma unroll
            for (int s = 0; s < 8; ++s) sacc = mfma32(kf[blk & 1][s], qf[blk & 1][s], sacc);
            if (blk == 1) LBYTE(8 + wave, 1);
            have = lds_wait64(L + L_SY, need);
#pragma unroll
            for (int s = 0; s < 8; ++s) {
              kf[(blk + 1) & 1][s] = *(const LAS v4u*)(L + (kb ^ (s * 32)) + (blk + 1) * 32768);
              qf[(blk + 1) & 1][s] = *(const LAS v4u*)(L + (qb ^ (s * 32)) + (blk + 1) * 8192);
            }
          } else {
#pragma unroll
            for (int s = 0; s < 8; ++s) {
              kf[(blk + 1) & 1][s] = *(const LAS v4u*)(L + (kb ^ (s * 32)) + (blk + 1) * 32768);
              qf[(blk + 1) & 1][s] = *(const LAS v4u*)(L + (qb ^ (s * 32)) + (blk + 1) * 8192);
              sacc = mfma32(kf[blk & 1][s], qf[blk & 1][s], sacc);
            }
            if (blk == 1) LBYTE(8 + wave, 1);
            if (blk < 2) have = lds_peek64(L + L_SY);
          }
        } else {
          if (l32 == 0) qf[1][7] = hole;
#pragma unroll
          for (int s = 0; s < 8; ++s) sacc = mfma32(kf[1][s], qf[1][s], sacc);
        }
      }
    }
    STAMP(5);
    GSTART(4, tw, ((i & 2) ? h1 : h0) + (i & 1) * 16);
    lds_wait(L + L_SY + 8, 0x01010101);
    float mx = fmaxf(fmaxf(fmaxf(sacc[0], sacc[1]), fmaxf(sacc[2], sacc[3])), fmaxf(fmaxf(sacc[4], sacc[5]), fmaxf(sacc[6], sacc[7])));
    mx = fmaxf(mx, fmaxf(fmaxf(fmaxf(sacc[8], sacc[9]), fmaxf(sacc[10], sacc[11])), fmaxf(fmaxf(sacc[12], sacc[13]), fmaxf(sacc[14], sacc[15]))));
    mx = __builtin_ceilf(xmax32(mx));
    float ls = 0.f;
#pragma unroll
    for (int q = 0; q < 4; ++q) {
      const unsigned p0 = pk2(__builtin_amdgcn_exp2f(sacc[4 * q] - mx), __builtin_amdgcn_exp2f(sacc[4 * q + 1] - mx));
      const unsigned p1 = pk2(__builtin_amdgcn_exp2f(sacc[4 * q + 2] - mx), __builtin_amdgcn_exp2f(sacc[4 * q + 3] - mx));
      *(LAS v2u*)(L + L_P + l32 * 256 + (((wave * 4 + q) ^ zl) * 16) + hf * 8) = (v2u){p0, p1};
      ls = dot_ones(p1, dot_ones(p0, ls));
    }
    ls = xsum32(ls);
    *(LAS float*)(L + L_ST + hf * 512 + l32 * 16 + wave * 4) = hf ? ls : mx;
  }
  BAR();
  const unsigned wb = L_CKV + (wave >> 1) * 32768;
  const unsigned wq8 = (wave & 1) * 8;
  f32x16 o0, o1;
  {
    v4u pf[8];
    const f32x4 mv = *(const LAS f32x4*)(L + L_ST + l32 * 16), lv = *(const LAS f32x4*)(L + L_ST + 512 + l32 * 16);
    {
      const unsigned pbs = L_P + l32 * 256 + (hf ^ zl) * 16;
#pragma unroll
      for (int ks = 0; ks < 8; ++ks) pf[ks] = *(const LAS v4u*)(L + (pbs ^ (ks * 32)));
    }
    const float M = fmaxf(fmaxf(mv[0], mv[1]), fmaxf(mv[2], mv[3]));
#pragma unroll
    for (int w = 0; w < 4; ++w) {
      const unsigned sh = (unsigned)fminf(M - mv[w], 255.f) * 0x00800080u;
#pragma unroll
      for (int e = 0; e < 2; ++e) {
        v4u t = pf[2 * w + e];
        pf[2 * w + e] = (v4u){pksubsat(t.x, sh), pksubsat(t.y, sh), pksubsat(t.z, sh), pksubsat(t.w, sh)};
      }
    }
    if (wave == 5) {
      const float lt = (lv[0] * __builtin_amdgcn_exp2f(mv[0] - M) + lv[1] * __builtin_amdgcn_exp2f(mv[1] - M)) +
                       (lv[2] * __builtin_amdgcn_exp2f(mv[2] - M) + lv[3] * __builtin_amdgcn_exp2f(mv[3] - M));
      if (lane < 32)
        __builtin_amdgcn_raw_buffer_store_b64((v2u){f2u(M), f2u(lt)}, wsr, WS_FL + ((((lane >> 2) * 8 + bA) * 32 + chunk) * 4 + (lane & 3)) * 8, 0, AUX_SC1);
    }
#pragma unroll
    for (int i = 0; i < 16; ++i) { o0[i] = 0.f; o1[i] = 0.f; }
    const unsigned blk = (lane >> 4) & 1, q = (lane & 15) >> 2, pp = lane & 3;
    unsigned ta[2];
#pragma unroll
    for (int tt = 0; tt < 2; ++tt) {
      const unsigned row = 8 * hf + 4 * tt + q;
      ta[tt] = wb + row * 256 + (((wq8 + 2 * blk + (pp >> 1)) ^ swz(row)) * 16) + 8 * (pp & 1);
    }
#pragma unroll
    for (int ks = 0; ks < 8; ++ks) {
      o0 = mfma32(pf[ks], tr_read2(L + ta[0] + ks * 4096, L + ta[1] + ks * 4096), o0);
      o1 = mfma32(pf[ks], tr_read2(L + (ta[0] ^ 64) + ks * 4096, L + (ta[1] ^ 64) + ks * 4096), o1);
    }
  }
  STAMP(6);
  {
    const unsigned pbase = WS_PART + ((((bA * 32 + 4 * (g4 >> 1) + (g4 & 1)) * 8 + wave) * 32 + chunk) * 16 + l16) * 8;
#pragma unroll
    for (int i = 0; i < 16; i += 2) {
      auto s = __builtin_amdgcn_permlane16_swap(pk2(o0[i], o1[i]), pk2(o0[i + 1], o1[i + 1]), false, false);
      __builtin_amdgcn_raw_buffer_store_b64((v2u){s[0], s[1]}, wsr, pbase + (8 * (i >> 2) + (i & 3)) * (8 * 32 * 128), 0, AUX_SC1);
    }
  }
  if (wave >= 4) {
    const unsigned sc_e = wb + r8 * 256 + (((wq8 + q8) ^ swz(r8)) * 16);
    const unsigned sc_o = wb + (r8 + 8) * 256 + (((wq8 + q8) ^ swz(r8 + 8)) * 16);
#pragma unroll
    for (int e = 0; e < 2; ++e) {
      *(LAS v4u*)(L + sc_e + e * 12288) = tw[5 * e + 0];
      *(LAS v4u*)(L + sc_o + e * 12288) = tw[5 * e + 1];
      *(LAS v4u*)(L + sc_e + e * 12288 + 4096) = tw[5 * e + 2];
      *(LAS v4u*)(L + sc_o + e * 12288 + 4096) = tw[5 * e + 3];
      *(LAS v4u*)(L + sc_e + e * 12288 + 8192) = tw[5 * e + 4];
    }
  }
  BAR();
  STAMP(7);
  const unsigned pc = (l16 & 3) >> 1, ph = (l16 & 1) * 8, rq = l16 >> 2;
  v4u av[2], ao[4], ad0, ad1;
  f32x4 accE = {0.f, 0.f, 0.f, 0.f};
  {
    const unsigned pw = 2 * (wave & 3) + pc;
    const unsigned vr = 8 * (wave >> 2) + 2 * g4;
    const unsigned vb = (2 + (rq >> 1)) * 32768 + ((rq & 1) * 48 + 16 + vr) * 256 + ph;
#pragma unroll
    for (int s = 0; s < 2; ++s)
      av[s] = tr_read2(L + vb + (((8 * s + pw) ^ swz(vr)) * 16), L + vb + 256 + (((8 * s + pw) ^ swz(vr + 1)) * 16));
    if (wave < 4) {
      const unsigned rin = 8 * (g4 & 1) + rq, pco = (g4 >> 1) * 8 + pw;
      const unsigned o0a = 65536 + rin * 256 + ((pco ^ swz(rin)) * 16) + ph, o1a = 65536 + (rin + 4) * 256 + ((pco ^ swz(rin + 4)) * 16) + ph;
#pragma unroll
      for (int ks = 0; ks < 4; ++ks) ao[ks] = tr_read2(L + o0a + (ks & 1) * 32768 + (ks >> 1) * 12288, L + o1a + (ks & 1) * 32768 + (ks >> 1) * 12288);
      const unsigned pcd = (g4 & 1) * 8 + pw, db = (2 + (g4 >> 1)) * 32768 + ph;
      const unsigned d0 = db + (32 + rq) * 256 + ((pcd ^ swz(rq)) * 16), d1 = db + (36 + rq) * 256 + ((pcd ^ swz(rq + 4)) * 16);
      ad0 = tr_read2(L + d0, L + d1); ad1 = tr_read2(L + d0 + 12288, L + d1 + 12288);
    }
    VMWAIT(0);
    TAGSET();
    if (lds_ticket(L + L_SY + 28, lane) == 7) {
      if (lane < 8) __builtin_amdgcn_raw_buffer_store_b32(tag, wsr, WS_PF + lane * 8192 + (bA * 4 + (chunk >> 3)) * 256 + (chunk & 7) * 4, 0, AUX_SC1);
    }
    STAMP(8);
    if (wave < 4) {
      GFINISH(4, tw, ((i & 2) ? h1 : h0) + (i & 1) * 16, 1);
      accE = mfma16(ad0, (v4u){tw[0].x, tw[0].z, tw[1].x, tw[1].z}, accE);
      accE = mfma16(ad1, hon ? (v4u){tw[2].x, tw[2].z, tw[3].x, tw[3].z} : (v4u){0u, 0u, 0u, 0u}, accE);
    }
  }
  {
#pragma nounroll
    for (unsigned spin = 0; spin < SPIN_CAP; ++spin) {
      if (flg_ok(GLOAD(WS_PF + xg * 8192 + (wave * 4 + ((lane >> 1) & 3)) * 256 + (lane & 1) * 16), tag)) break;
      __builtin_amdgcn_s_sleep(1);
    }
    asm volatile("" ::: "memory");
    STAMP(14);
    v2u pr[9];
    const unsigned po = WS_PART + ((((wave * 32 + hA) * 8 + sl) * 32) * 16 + lane) * 8;
    const unsigned fo = WS_FL + (((xg * 8 + wave) * 32 + l32) * 4 + (hA & 3)) * 8;
    pr[8] = __builtin_amdgcn_raw_buffer_load_b64(wsr, fo, 0, AUX_SC1);
#pragma unroll
    for (int i = 0; i < 8; ++i) pr[i] = __builtin_amdgcn_raw_buffer_load_b64(wsr, po + i * 512, 0, AUX_SC1);
    const float mj = u2f(pr[8].x);
    float M = mj;
    M = fmaxf(M, dppm<0x121>(M)); M = fmaxf(M, dppm<0x122>(M)); M = fmaxf(M, dppm<0x124>(M)); M = fmaxf(M, dppm<0x128>(M));
    M = xmax16(M);
    const float wj = bfhi(f2u(__builtin_amdgcn_exp2f(mj - M)));
    float Ls = wj * u2f(pr[8].y);
    Ls += dppm<0x121>(Ls); Ls += dppm<0x122>(Ls); Ls += dppm<0x124>(Ls); Ls += dppm<0x128>(Ls);
    Ls = xsum16(Ls);
    const float inv = __builtin_amdgcn_rcpf(Ls);
    const unsigned e4 = l16 & 3;
    const unsigned sh = (e4 & 1) ? 0u : 16u, k0 = e4 < 2 ? ~0u : 0u;
    unsigned wa[16];
#pragma unroll
    for (int u = 0; u < 8; ++u) {
      const unsigned w = (unsigned)__builtin_amdgcn_ds_bpermute((g4 + 4 * u) * 4, f2u(wj)) >> sh;
      wa[2 * u] = w & k0; wa[2 * u + 1] = w & ~k0;
    }
    f32x4 acc = {0.f, 0.f, 0.f, 0.f};
#pragma unroll
    for (int u = 0; u < 4; ++u)
      acc = mfma16((v4u){wa[4 * u], wa[4 * u + 1], wa[4 * u + 2], wa[4 * u + 3]}, (v4u){pr[2 * u].x, pr[2 * u].y, pr[2 * u + 1].x, pr[2 * u + 1].y}, acc);
    if (lane < 16) *(LAS v2u*)(L + L_CL + wave * 144 + lane * 8) = (v2u){pk2(acc[0] * inv, acc[1] * inv), pk2(acc[2] * inv, acc[3] * inv)};
    BAR();
  }
  const unsigned one = (l16 & 1) ? 0x3f800000u : 0x00003f80u;
  const unsigned s0 = (l16 & 2) ? 0u : one, s1 = (l16 & 2) ? one : 0u;
  {
    const v4u b0 = *(const LAS v4u*)(L + L_CL + (l16 & 7) * 144 + g4 * 16), b1 = *(const LAS v4u*)(L + L_CL + (l16 & 7) * 144 + 64 + g4 * 16);
    f32x4 acc = {0.f, 0.f, 0.f, 0.f};
    acc = mfma16(av[0], b0, acc);
    acc = mfma16(av[1], b1, acc);
    if (l16 < 8)
      __builtin_amdgcn_raw_buffer_store_b128((v4u){pk2(acc[0], acc[1]), tag, pk2(acc[2], acc[3]), tag}, wsr,
                                             WS_CX + ((((hA * 8 + wave) * 8 + sl) * 8 + l16) * 4 + g4) * 16, 0, AUX_SC1);
    STAMP(9);
    v4u cr[4];
    const unsigned co = WS_CX + ((hB * 8 + wave) * 256 + lane) * 16;
    if (CDELAY) __builtin_amdgcn_s_sleep(CDELAY);
#pragma nounroll
    for (unsigned spin = 0; spin < SPIN_CAP; ++spin) {
#pragma unroll
      for (int i = 0; i < 4; ++i) cr[i] = GLOAD(co + i * 1024);
      if (__all((cr[0].y == tag) & (cr[0].w == tag) & (cr[1].y == tag) & (cr[1].w == tag) & (cr[2].y == tag) & (cr[2].w == tag) & (cr[3].y == tag) & (cr[3].w == tag))) break;
      __builtin_amdgcn_s_sleep(1);
    }
    STAMP(10);
    {
      const bool on = ((l16 >> 2) & 1) == (g4 & 1);
      const unsigned t0 = on ? s0 : 0u, t1 = on ? s1 : 0u;
      f32x4 a = {0.f, 0.f, 0.f, 0.f};
      a = mfma16((v4u){t0, t1, t0, t1}, (v4u){cr[0].x, cr[0].z, cr[1].x, cr[1].z}, a);
      a = mfma16((v4u){t0, t1, t0, t1}, (v4u){cr[2].x, cr[2].z, cr[3].x, cr[3].z}, a);
      if (lane < 32) *(LAS v2u*)(L + L_CX + (lane >> 2) * 272 + wave * 32 + (lane & 3) * 8) = (v2u){pk2(a[0], a[1]), pk2(a[2], a[3])};
    }
    BAR();
  }
  if (wave < 4) {
#pragma unroll
    for (int ks = 0; ks < 4; ++ks) accE = mfma16(ao[ks], *(const LAS v4u*)(L + L_CX + (l16 & 7) * 272 + ks * 64 + g4 * 16), accE);
    if (l16 < 8)
      __builtin_amdgcn_raw_buffer_store_b128((v4u){pk2(accE[0], accE[1]), tag, pk2(accE[2], accE[3]), tag}, wsr,
                                             WS_OG + ((((sl * 4 + wave) * 32 + hB) * 8 + l16) * 4 + g4) * 16, 0, AUX_SC1);
    STAMP(11);
  } else if (wave == 4) {
    const unsigned oo = WS_OG + ((((wg & 31) * 32 + (lane >> 2)) * 8 + (wg >> 5)) * 4 + (lane & 3)) * 16;
    const v2u xw = *(const GAS v2u*)(p.x + wg * 16 + (lane & 3) * 4);
    v4u r0, r1;
    if (ODELAY) __builtin_amdgcn_s_sleep(ODELAY);
#pragma nounroll
    for (unsigned spin = 0; spin < SPIN_CAP; ++spin) {
      r0 = GLOAD(oo);
      r1 = GLOAD(oo + 16 * 512);
      if (__all((r0.y == tag) & (r0.w == tag) & (r1.y == tag) & (r1.w == tag))) break;
      __builtin_amdgcn_s_sleep(1);
    }
    f32x4 acc = {0.f, 0.f, 0.f, 0.f};
    acc = mfma16((v4u){s0, s1, s0, s1}, (v4u){r0.x, r0.z, r1.x, r1.z}, acc);
#pragma unroll
    for (int j = 0; j < 4; ++j) { acc[j] += dppm<0x124>(acc[j]); acc[j] += dppm<0x128>(acc[j]); }
    if (lane < 4)
      *(GAS v2u*)(p.out + wg * 16 + lane * 4) = (v2u){pk2(bflo(xw.x) + acc[0], bfhi(xw.x) + acc[1]), pk2(bflo(xw.y) + acc[2], bfhi(xw.y) + acc[3])};
    if (wg == 0 && lane == 0) __builtin_amdgcn_raw_buffer_store_b32(tag, wsr, WS_EPOCH, 0, AUX_SC1);
    STAMP(11);
  }
  if (wave == DW && ((DCL) == 2 || ((DCL) < 2 && (r & 1) == (DCL)) || ((DCL) >= 1000 && wg == (DCL) - 1000) || ((DCL) >= 100 && (DCL) < 108 && xg == (DCL) - 100))) {
    unsigned dur = dts - dt0;
    if (dur > 5000) dur = 0;
    dur += 3000;
    for (unsigned spin = 0; spin < 1000000; ++spin) { if ((unsigned)__builtin_amdgcn_s_memrealtime() - dt0 >= dur) break; __builtin_amdgcn_s_sleep(1); }
  }
}

extern "C" void kernel_launch(void* const* d_in, const int* in_sizes, int n_in, void* d_out, int out_size, void* d_ws, size_t ws_size,
                              hipStream_t stream) {
  Params p;
  p.x = (const u16*)d_in[0]; p.ckv = (const u16*)d_in[1]; p.wq = (const u16*)d_in[2]; p.wuk = (const u16*)d_in[3];
  p.wuv = (const u16*)d_in[4]; p.wo = (const u16*)d_in[5]; p.wg = (const u16*)d_in[7]; p.wu = (const u16*)d_in[8]; p.wd = (const u16*)d_in[9];
  p.out = (u16*)d_out; p.ws = (unsigned char*)d_ws;
  (void)hipFuncSetAttribute((const void*)fused_kernel, hipFuncAttributeMaxDynamicSharedMemorySize, LDS_BYTES);
  fused_kernel<<<NWG, NTHR, LDS_BYTES, stream>>>(p);
}
```

```cpp
#include <hip/hip_runtime.h>
#include <hip/hip_bf16.h>
#include <stdint.h>

#define STAMPS 0
#define CODE_LINES 156
#define WIN 10
#define ADELAY 120
#define CDELAY 8
#define ODELAY 8
#define NFFN 176
#define ILV 1
#define WIN0 4
#define EMASK 0x0100
#define TPFD 0
#define PDELAY 0
#define PSLP 1
#define TF 0
#define TP 340
#define TQ 340
#define TG 580

typedef unsigned v4u __attribute__((ext_vector_type(4)));
typedef unsigned v2u __attribute__((ext_vector_type(2)));
typedef float f32x2 __attribute__((ext_vector_type(2)));
typedef float f32x4 __attribute__((ext_vector_type(4)));
typedef float f32x16 __attribute__((ext_vector_type(16)));
typedef short bf16x8 __attribute__((ext_vector_type(8)));
typedef short s16x4 __attribute__((ext_vector_type(4)));
typedef unsigned short u16x2 __attribute__((ext_vector_type(2)));
typedef __bf16 bf16x2 __attribute__((ext_vector_type(2)));
typedef unsigned short u16;
#define LAS __attribute__((address_space(3)))
#define GAS __attribute__((address_space(1)))

constexpr int NB = 8, NH = 32, SK = 4096, HD = 128, DM = 512, DC = 512, FF = 1376;
constexpr int NWG = 256, NTHR = 512;
constexpr unsigned WS_EPOCH = 0;
#define RS 262144
constexpr unsigned WS_QF = 4096;
constexpr unsigned WS_PF = WS_QF + 8192;
constexpr unsigned WS_QA = WS_PF + 8192;
constexpr unsigned WS_QP = WS_QA + 32768;
static_assert(WS_QP + 65536 <= RS, "ws map");
constexpr unsigned WS_GU = 8 * RS;
constexpr unsigned WS_HM = WS_GU + 8 * 176 * 32 * 16;
constexpr unsigned WS_FL = WS_HM + 176 * 8 * 2 * 16;
constexpr unsigned WS_CX = WS_FL + 8 * 8 * 32 * 4 * 16;
constexpr unsigned WS_OG = WS_CX + 32 * 8 * 8 * 8 * 4 * 16;
constexpr unsigned WS_PART = 6u << 20;
constexpr unsigned WS_END = WS_PART + (8u << 20);
constexpr unsigned WS_STAMP = 32u << 20;
static_assert(WS_OG + 32 * 32 * 8 * 4 * 16 <= WS_PART, "ws map");
constexpr unsigned L_CKV = 0;
constexpr unsigned L_S = 128 * 1024;
constexpr unsigned L_SY = L_S + 24576 + 224;
constexpr unsigned L_WQ = L_S, L_WU = 98304, L_WF = 114688;
constexpr unsigned L_X = L_S + 16384;
constexpr unsigned L_LN = L_S + 25600;
constexpr unsigned L_DUMP = L_S + 26624;
constexpr unsigned T_O = 0, T_V = 16384, T_D = 32768;
constexpr unsigned L_P = L_S;
constexpr unsigned L_ST = L_S + 8192;
constexpr unsigned L_CL = L_S + 16384;
constexpr unsigned L_CX = L_S + 16384 + 2048;
constexpr unsigned LDS_BYTES = 160 * 1024;
constexpr int AUX_NT = 2, AUX_SC1 = 16;
constexpr unsigned SPIN_CAP = 1u << 18;
constexpr unsigned OOB = 0x80000000u;

struct Params {
  const u16 *x, *ckv, *wq, *wuk, *wuv, *wo, *wg, *wu, *wd;
  u16* out;
  unsigned char* ws;
};

__device__ __forceinline__ float u2f(unsigned u) { return __uint_as_float(u); }
__device__ __forceinline__ unsigned f2u(float f) { return __float_as_uint(f); }
__device__ __forceinline__ float bflo(unsigned w) { return u2f(w << 16); }
__device__ __forceinline__ float bfhi(unsigned w) { return u2f(w & 0xffff0000u); }
__device__ __forceinline__ unsigned pk2(float lo, float hi) {
  f32x2 f = {lo, hi};
  return __builtin_bit_cast(unsigned, __builtin_convertvector(f, bf16x2));
}
__device__ __forceinline__ float dot_ones(unsigned a, float c) {
  return __builtin_amdgcn_fdot2_f32_bf16(__builtin_bit_cast(bf16x2, a), __builtin_bit_cast(bf16x2, 0x3f803f80u), c, false);
}
__device__ __forceinline__ unsigned pksubsat(unsigned a, unsigned b) {
  return __builtin_bit_cast(unsigned, __builtin_elementwise_sub_sat(__builtin_bit_cast(u16x2, a), __builtin_bit_cast(u16x2, b)));
}
__device__ __forceinline__ f32x4 mfma16(v4u a, v4u b, f32x4 c) {
  return __builtin_amdgcn_mfma_f32_16x16x32_bf16(__builtin_bit_cast(bf16x8, a), __builtin_bit_cast(bf16x8, b), c, 0, 0, 0);
}
__device__ __forceinline__ f32x16 mfma32(v4u a, v4u b, f32x16 c) {
  return __builtin_amdgcn_mfma_f32_32x32x16_bf16(__builtin_bit_cast(bf16x8, a), __builtin_bit_cast(bf16x8, b), c, 0, 0, 0);
}
__device__ __forceinline__ unsigned swz(unsigned row) { return ((row & 3) << 2) | ((row >> 2) & 3); }
#define BAR() do { asm volatile("s_waitcnt lgkmcnt(0)" ::: "memory"); __builtin_amdgcn_s_barrier(); asm volatile("" ::: "memory"); } while (0)
#define VMWAIT(n) do { asm volatile("" ::: "memory"); __builtin_amdgcn_s_waitcnt(0x0f70 | ((n) & 15) | (((n) >> 4) << 14)); asm volatile("" ::: "memory"); } while (0)
#define LGKM0() asm volatile("s_waitcnt lgkmcnt(0)" ::: "memory")
#define GLDS_(OP, sbase, voff, dst) asm volatile("s_mov_b32 m0, %2\n\ts_nop 0\n\t" OP :: "v"((unsigned)(voff)), "s"((const void*)(sbase)), "s"((unsigned)(dst)) : "memory", "m0")
#define GLDS(sbase, voff, dst) GLDS_("global_load_lds_dwordx4 %0, %1 nt", sbase, voff, dst)
#define GLDSX(sbase, voff, dst) GLDS_("global_load_lds_dwordx4 %0, %1", sbase, voff, dst)
#define GLDS4(sbase, voff, dst) GLDS_("global_load_lds_dword %0, %1 nt", sbase, voff, dst)
#define PFR(sbase, voff) asm volatile("global_load_dword %0, %1, %2" : "+v"(tpf) : "v"((unsigned)(voff)), "s"((const void*)(sbase)) : "memory")
#define WLOAD(p) __builtin_nontemporal_load(p)
#define LBYTE(off, v) (*(volatile LAS unsigned char*)(L + L_SY + (off)) = (v))
__device__ __forceinline__ v2u tr_read(LAS unsigned char* a) {
  return __builtin_bit_cast(v2u, __builtin_amdgcn_ds_read_tr16_b64_v4i16((LAS s16x4*)a));
}
__device__ __forceinline__ v4u tr_read2(LAS unsigned char* a, LAS unsigned char* b) {
  const v2u x = tr_read(a), y = tr_read(b);
  return (v4u){x.x, x.y, y.x, y.y};
}
template <int CTRL>
__device__ __forceinline__ unsigned dppu(unsigned v) { return __builtin_amdgcn_update_dpp(v, v, CTRL, 0xf, 0xf, false); }
template <int CTRL>
__device__ __forceinline__ float dppm(float v) { return u2f(dppu<CTRL>(f2u(v))); }
__device__ __forceinline__ float xsum16(float v) {
  auto s = __builtin_amdgcn_permlane16_swap(f2u(v), f2u(v), false, false);
  return u2f(s[0]) + u2f(s[1]);
}
__device__ __forceinline__ float xmax16(float v) {
  auto s = __builtin_amdgcn_permlane16_swap(f2u(v), f2u(v), false, false);
  return fmaxf(u2f(s[0]), u2f(s[1]));
}
__device__ __forceinline__ float xsum32(float v) {
  auto s = __builtin_amdgcn_permlane32_swap(f2u(v), f2u(v), false, false);
  return u2f(s[0]) + u2f(s[1]);
}
__device__ __forceinline__ float xmax32(float v) {
  auto s = __builtin_amdgcn_permlane32_swap(f2u(v), f2u(v), false, false);
  return fmaxf(u2f(s[0]), u2f(s[1]));
}
__device__ __forceinline__ bool rec_ok(v4u r, unsigned tag) { return __all((r.y == tag) & (r.w == tag)); }
__device__ __forceinline__ bool flg_ok(v4u r, unsigned tag) { return __all((r.x == tag) & (r.y == tag) & (r.z == tag) & (r.w == tag)); }
__device__ __forceinline__ unsigned lds_wait(LAS unsigned char* a, unsigned mask) {
  unsigned w = 0;
#pragma nounroll
  for (unsigned spin = 0; spin < (SPIN_CAP << 4); ++spin) {
    w = __builtin_amdgcn_readfirstlane(*(volatile LAS unsigned*)a);
    if ((w & mask) == mask) break;
    __builtin_amdgcn_s_sleep(1);
  }
  asm volatile("" ::: "memory");
  return w;
}

__device__ __forceinline__ unsigned lds_nz(LAS unsigned char* a) {
  unsigned w = 0;
#pragma nounroll
  for (unsigned spin = 0; spin < (SPIN_CAP << 4); ++spin) {
    w = __builtin_amdgcn_readfirstlane(*(volatile LAS unsigned*)a);
    if (w) break;
    __builtin_amdgcn_s_sleep(1);
  }
  asm volatile("" ::: "memory");
  return w;
}
__device__ __forceinline__ unsigned long long lds_peek64(LAS unsigned char* a) {
  const v2u w = *(volatile LAS v2u*)a;
  return (unsigned long long)__builtin_amdgcn_readfirstlane(w.x) | ((unsigned long long)__builtin_amdgcn_readfirstlane(w.y) << 32);
}
__device__ __forceinline__ unsigned long long lds_wait64(LAS unsigned char* a, unsigned long long mask) {
  unsigned long long w = 0;
#pragma nounroll
  for (unsigned spin = 0; spin < (SPIN_CAP << 4); ++spin) {
    w = lds_peek64(a);
    if ((w & mask) == mask) break;
    __builtin_amdgcn_s_sleep(1);
  }
  asm volatile("" ::: "memory");
  return w;
}
__device__ __forceinline__ unsigned lds_ticket(LAS unsigned char* a, int lane) {
  unsigned t = 0;
  if (lane == 0) t = __hip_atomic_fetch_add((LAS unsigned*)a, 1u, __ATOMIC_RELAXED, __HIP_MEMORY_SCOPE_WORKGROUP);
  return __builtin_amdgcn_readfirstlane(t);
}
#define STAMP(k) do { } while (0)

#define GLOAD(ADDR) __builtin_amdgcn_raw_buffer_load_b128(wsr, (ADDR), 0, AUX_SC1)
#define GFINISH(N, qr, ADDR, SLP)                                                                            \
  do {                                                                                                       \
    unsigned done = 0;                                                                                       \
    _Pragma("nounroll") for (unsigned spin = 0; spin < SPIN_CAP; ++spin) {                                   \
      _Pragma("unroll") for (int i = 0; i < N; ++i)                                                          \
        if (!((done >> i) & 1)) { if (rec_ok(qr[i], tag)) done |= 1u << i; }                                 \
      if (done == (1u << N) - 1u) break;                                                                     \
      if (SLP) __builtin_amdgcn_s_sleep(SLP);                                                                \
      _Pragma("unroll") for (int i = 0; i < N; ++i)                                                          \
        if (!((done >> i) & 1)) qr[i] = GLOAD(ADDR);                                                         \
    }                                                                                                        \
  } while (0)
#define OK_FLG(r) (((r).x == tag) & ((r).y == tag) & ((r).z == tag) & ((r).w == tag))
#define KEEPV(r) asm volatile("" :: "v"(r))
#define WAIT_UNTIL(t) do { _Pragma("nounroll") for (unsigned spin = 0; spin < SPIN_CAP; ++spin) { if ((unsigned)__builtin_amdgcn_s_memrealtime() - t00 >= (t)) break; __builtin_amdgcn_s_sleep(1); } } while (0)
#define GSTART(N, qr, ADDR) do { _Pragma("unroll") for (int i = 0; i < N; ++i) qr[i] = GLOAD(ADDR); } while (0)

__global__ void __launch_bounds__(NTHR, 2) fused_kernel(Params p) {
  extern __shared__ __attribute__((aligned(16))) unsigned char lds_raw[];
  STAMP(0);
  const unsigned tstart = (unsigned)__builtin_amdgcn_s_memrealtime();
  static_assert(L_DUMP == 0x26800 && L_X == 0x24000 && L_WQ == 0x20000 && L_WU == 0x18000 && L_SY == 0x260e0 && CODE_LINES <= 192, "volley");
  asm volatile(
      "s_load_dwordx8 s[40:47], %0, 0x0\n\t"
      "v_readfirstlane_b32 s48, %2\n\t"
      "v_and_b32 v13, 63, %2\n\t"
      "v_mov_b32 v10, 0\n\t"
      "v_mov_b32 v11, 0\n\t"
      "v_mov_b32 v12, 0x260e0\n\t"
      "s_getpc_b64 s[50:51]\n\t"
      "s_lshr_b32 s48, s48, 6\n\t"
      "s_cmp_lg_u32 s48, 1\n\t"
      "s_cbranch_scc1 7f\n\t"
      "ds_write2_b64 v12, v[10:11], v[10:11] offset1:1\n\t"
      "ds_write2_b64 v12, v[10:11], v[10:11] offset0:2 offset1:3\n\t"
      "s_waitcnt lgkmcnt(0)\n"
      "7:\n\t"
      "s_mov_b32 s54, 0\n\t"
      "s_and_b32 s49, %1, 7\n\t"
      "s_cmp_lg_u32 s48, 4\n\t"
      "s_cbranch_scc1 1f\n\t"
      "s_bfe_u32 s52, %1, 0x40003\n\t"
      "s_mul_i32 s52, s52, 12\n\t"
      "v_min_u32 v10, 11, v13\n\t"
      "v_add_u32 v10, s52, v10\n\t"
      "v_min_u32 v10, %3, v10\n\t"
      "v_lshlrev_b32 v10, 7, v10\n\t"
      "s_andn2_b32 s50, s50, 0x7f\n\t"
      "s_mov_b32 m0, 0x26800\n\t"
      "s_nop 0\n\t"
      "global_load_lds_dword v10, s[50:51]\n\t"
      "s_branch 6f\n"
      "1:\n\t"
      "s_cmp_lg_u32 s48, 0\n\t"
      "s_cbranch_scc1 2f\n\t"
      "v_lshrrev_b32 v10, 3, v13\n\t"
      "v_and_b32 v11, 7, v13\n\t"
      "v_bfe_u32 v12, v13, 3, 2\n\t"
      "v_lshrrev_b32 v13, 5, v13\n\t"
      "v_lshl_or_b32 v12, v12, 1, v13\n\t"
      "v_xor_b32 v11, v11, v12\n\t"
      "v_lshlrev_b32 v11, 4, v11\n\t"
      "v_lshl_or_b32 v10, v10, 13, v11\n\t"
      "s_lshl_b32 s49, s49, 10\n\t"
      "s_lshr_b32 s52, %1, 5\n\t"
      "s_lshl_b32 s52, s52, 7\n\t"
      "s_or_b32 s49, s49, s52\n\t"
      "s_bfe_u32 s52, %1, 0x20003\n\t"
      "s_lshl_b32 s52, s52, 20\n\t"
      "s_or_b32 s49, s49, s52\n\t"
      "s_mov_b32 m0, 0x20000\n\t"
      "s_mov_b32 s56, 0x24000\n\t"
      "s_mov_b32 s52, 0x10000\n\t"
      "s_waitcnt lgkmcnt(0)\n\t"
      "s_add_u32 s50, s44, s49\n\t"
      "s_addc_u32 s51, s45, 0\n\t"
      "s_branch 5f\n"
      "2:\n\t"
      "s_cmp_lg_u32 s48, 5\n\t"
      "s_cbranch_scc1 6f\n\t"
      "v_lshlrev_b32 v10, 4, v13\n\t"
      "s_mov_b32 m0, 0x24000\n\t"
      "s_mov_b32 s56, 0x26000\n\t"
      "s_movk_i32 s52, 0x420\n\t"
      "s_waitcnt lgkmcnt(0)\n\t"
      "s_mov_b64 s[50:51], s[40:41]\n"
      "5:\n\t"
      "v_xor_b32 v11, s54, v10\n\t"
      "global_load_lds_dwordx4 v11, s[50:51]\n\t"
      "s_add_u32 s54, s54, s52\n\t"
      "s_add_u32 m0, m0, 0x400\n\t"
      "s_cmp_lg_u32 m0, s56\n\t"
      "s_cbranch_scc1 5b\n\t"
      "s_barrier\n"
      "6:\n\t"
      "s_waitcnt lgkmcnt(0)"
      :: "s"(__builtin_amdgcn_kernarg_segment_ptr()), "s"(__builtin_amdgcn_workgroup_id_x()), "v"(__builtin_amdgcn_workitem_id_x()), "n"(CODE_LINES - 1)
      : "memory", "m0", "scc", "s40", "s41", "s42", "s43", "s44", "s45", "s46", "s47", "s48", "s49", "s50", "s51", "s52", "s54", "s56",
        "v10", "v11", "v12", "v13");
  STAMP(15);
  LAS unsigned char* L = (LAS unsigned char*)lds_raw;
  const int tid = threadIdx.x, lane = tid & 63;
  const int wave = __builtin_amdgcn_readfirstlane(tid >> 6);
  const int wg = blockIdx.x, xg = wg & 7, r = wg >> 3;
  const int bA = xg, chunk = r;
  const int hA = xg * 4 + (r >> 3), sl = r & 7;
  const int hB = ((xg + 1) & 7) * 4 + (r >> 3);
  const int ntask = hB < 12 ? 6 : 5, task0 = hB < 12 ? 6 * hB : 12 + 5 * hB;
  const __amdgpu_buffer_rsrc_t wsr = __builtin_amdgcn_make_buffer_rsrc((void*)p.ws, 0, WS_END, 0x00020000);
  const int l16 = lane & 15, g4 = lane >> 4;
  const int l32 = lane & 31, hf = lane >> 5;
  const unsigned r8 = lane >> 3, q8 = lane & 7;
  const unsigned t00 = tstart;
  unsigned tag, tagv;
#define TAGLOAD() tagv = __builtin_amdgcn_raw_buffer_load_b32(wsr, WS_EPOCH, 0, 0)
#define TAGSET() do { tag = __builtin_amdgcn_readfirstlane(tagv) + 1u; tag += tag == 0; } while (0)
#define TAGGET() tag = lds_nz(L + L_SY + 24)
  const unsigned pz = q8 ^ (((r8 & 3) << 1) | (r8 >> 2));
  v4u tw[4];
  unsigned tpf = 0;
  const unsigned fa0 = (8 * g4 + (l16 >> 2)) * 128 + ((((l16 & 3) >> 1) ^ ((l16 >> 2) << 1)) * 16) + (l16 & 1) * 8;
  const unsigned fa1 = (fa0 ^ 16) + 512;
#define FRAG(base, nt, s) tr_read2(L + (base) + (fa0 ^ ((nt) * 32)) + (s) * 4096, L + (base) + (fa1 ^ ((nt) * 32)) + (s) * 4096)
  const bool hon = 4 + g4 < ntask;
  const unsigned h0 = WS_HM + ((task0 + g4) * 8 + (l16 & 7)) * 32, h1 = WS_HM + ((task0 + (hon ? 4 + g4 : 0)) * 8 + (l16 & 7)) * 32;
  const unsigned zl = swz(l32);

  if (wave >= 4) {
    if (wave >= 6) {
      const int v = wave - 6;
      const u16* srcb = p.ckv + ((size_t)bA * SK + chunk * 4) * DC;
      const unsigned vo0 = g4 * 1024 + (l16 ^ ((g4 << 2) | v)) * 16, vo1 = vo0 ^ 32;
      BAR();
      lds_wait(L + L_SY + 16, 1);
      bool early = true;
      STAMP(3);
#pragma nounroll
      for (int blk = 0; blk < 4; ++blk) {
        if (blk == 3) lds_wait(L + L_SY + 12, 0x00010100);
#pragma unroll
        for (int n = 0; n < 16; ++n) {
          GLDS(srcb + ((2 * n + v + chunk * ILV) & 31) * (128 * DC) + blk * 128, (n & 1) ? vo1 : vo0, L_CKV + blk * 32768 + (n * 2 + v) * 1024);
          if (early) {
            VMWAIT(WIN0 - 1);
            if (__builtin_amdgcn_readfirstlane(*(volatile LAS unsigned*)(L + L_SY + 16)) & EMASK) early = false;
          } else VMWAIT(WIN - 1);
          if (n == ((WIN - 2) & 15) && blk > (WIN - 2) / 16) LBYTE(v, (1 << (blk - (WIN - 2) / 16)) - 1);
        }
      }
      STAMP(1);
      if (WIN > 17) { VMWAIT(16); LBYTE(v, 7); }
      VMWAIT(0); LBYTE(v, 15);
      STAMP(2);
    } else if (wave == 4) {
      TAGLOAD();
      BAR();
      VMWAIT(0);
      TAGSET();
      *(volatile LAS unsigned*)(L + L_SY + 24) = tag;
      STAMP(3);
      WAIT_UNTIL(TQ);
      STAMP(1);
      {
        unsigned fo = WS_QF + xg * RS + (lane >> 1) * 256 + (lane & 1) * 16;
#pragma nounroll
        for (unsigned spin = 0; spin < SPIN_CAP; ++spin) {
          const v4u f = GLOAD(fo);
          if (OK_FLG(f)) fo = OOB;
          if (__all(fo == OOB)) break;
          __builtin_amdgcn_s_sleep(8);
          asm volatile("" ::: "memory");
        }
      }
      lds_wait(L + L_SY + 12, 0x00000101);
      LBYTE(21, 1);
      STAMP(2);
      PFR(p.ws, min(lane, 5) * (2u << 20) + (2u << 20));
      PFR(p.wuv + ((size_t)(sl * 64) * NH + hA) * HD, 0);
      PFR(p.wo + (size_t)hB * HD * DM, 0);
      PFR(p.wd, 0);
      PFR(p.out, 0);
      lds_wait64(L + L_SY, 0x0101010100000808ull);
      if (TPFD) __builtin_amdgcn_s_sleep(TPFD);
      {
        const u16* sv = p.wuv + ((size_t)(sl * 64) * NH + hA) * HD;
        const u16* so = p.wo + ((size_t)hB * HD) * DM + sl * 64;
        PFR(sv, l32 * 8192 + hf * 128);
        PFR(sv + 32 * NH * HD, l32 * 8192 + hf * 128);
        PFR(so, lane * 1024);
        PFR(so + 64 * DM, lane * 1024);
        PFR(p.wd + (size_t)task0 * 8 * DM + sl * 64, min(lane, ntask * 8 - 1) * 1024);
      }
    } else {
      VMWAIT(0);
      LBYTE(2, 1);
      STAMP(1);
      {
        const unsigned xb = L_X + (l16 & 7) * 1024;
        const v4u ones = {0x3f803f80u, 0x3f803f80u, 0x3f803f80u, 0x3f803f80u};
        v4u xs[8];
#pragma unroll
        for (int t = 0; t < 8; ++t) xs[t] = *(const LAS v4u*)(L + xb + (((8 * t + 4 * (l16 >> 3) + g4) ^ (2 * (l16 & 7))) * 16));
        f32x4 G = {0.f, 0.f, 0.f, 0.f}, S = {0.f, 0.f, 0.f, 0.f};
#pragma unroll
        for (int t = 0; t < 8; ++t) { G = mfma16(xs[t], xs[t], G); S = mfma16(ones, xs[t], S); }
        float dg = (l16 & 2) ? ((l16 & 1) ? G[3] : G[2]) : ((l16 & 1) ? G[1] : G[0]);
        dg = g4 == (l16 >> 2) ? dg : 0.f;
        dg = xsum32(xsum16(dg));
        dg += dppm<0x128>(dg);
        const float s0 = S[0];
        const float mean = (s0 + dppm<0x128>(s0)) * (1.f / DM);
        const float rstd = __builtin_amdgcn_rsqf(fmaxf(dg * (1.f / DM) - mean * mean, 0.f) + 1e-5f);
        if (lane < 8) *(LAS v2u*)(L + L_LN + lane * 8) = (v2u){f2u(mean), f2u(rstd)};
        LGKM0();
        LBYTE(2, 3);
      }
      STAMP(3);
      if (wg >= 176 && NFFN) {
        TAGGET();
        __builtin_amdgcn_s_sleep(ADELAY);
#pragma nounroll
        for (int ft = wg - 176; ft < 172; ft += 80) {
          v4u gr[4];
          GSTART(4, gr, WS_GU + (((2 * i + hf) * 176 + ft) * 32 + l32) * 16);
          GFINISH(4, gr, WS_GU + (((2 * i + hf) * 176 + ft) * 32 + l32) * 16, 8);
          float hm[2];
#pragma unroll
          for (int e = 0; e < 2; ++e) {
            const float gv = xsum32(u2f(e ? gr[0].z : gr[0].x) + u2f(e ? gr[1].z : gr[1].x));
            const float uv = xsum32(u2f(e ? gr[2].z : gr[2].x) + u2f(e ? gr[3].z : gr[3].x));
            const float yy = 0.7978845608028654f * (gv + 0.044715f * gv * gv * gv);
            const float th = 1.f - 2.f / (__expf(2.f * yy) + 1.f);
            hm[e] = 0.5f * gv * (1.f + th) * uv;
          }
          const unsigned a2 = pk2(hm[0], hm[1]), b2 = dppu<0xb1>(a2);
          if (lane < 32 && !(lane & 1))
            __builtin_amdgcn_raw_buffer_store_b128((v4u){a2, tag, b2, tag}, wsr, WS_HM + (ft * 16 + (lane >> 1)) * 16, 0, AUX_SC1);
        }
        STAMP(2);
      }
    }
  } else {
    if (wave == 0 || (wave == 1 && wg < NFFN)) {
      const unsigned st = wave ? L_WF : L_WQ;
      const int cg = (wg % 88) >> 2, mat = wg >= 88;
      const int kq1 = wave ? (wg & 3) : (sl & 3);
      if (wave) {
        const u16* src = (mat ? p.wu : p.wg) + (size_t)(kq1 * 128) * FF;
        const unsigned vo = (r8 * FF + min(cg * 64 + (int)pz * 8, FF - 8)) * 2;
        BAR();
        lds_wait(L + L_SY + 16, 0x0101);
#pragma unroll
        for (int i = 0; i < 16; ++i) GLDS(src + i * 8 * FF, vo, st + i * 1024);
      }
      lds_wait(L + L_SY, 0x010000);
      v4u xr[4];
#pragma unroll
      for (int s = 0; s < 4; ++s) {
        xr[s] = *(const LAS v4u*)(L + L_X + (l16 & 7) * 1024 + (((16 * kq1 + 4 * s + g4) ^ (2 * (l16 & 7))) * 16));
        if (l16 >= 8) xr[s] = (v4u){0x3f803f80u, 0x3f803f80u, 0x3f803f80u, 0x3f803f80u};
      }
      f32x4 acc[4];
      {
#pragma unroll
        for (int nt = 0; nt < 4; ++nt) acc[nt] = (f32x4){0.f, 0.f, 0.f, 0.f};
#pragma unroll
        for (int s = 0; s < 4; ++s) {
          if (s == 0) VMWAIT(12); else if (s == 1) VMWAIT(8); else if (s == 2) VMWAIT(4); else VMWAIT(0);
          if (s == 3) STAMP(1);
          v4u af[4];
#pragma unroll
          for (int nt = 0; nt < 4; ++nt) af[nt] = FRAG(st, nt, s);
#pragma unroll
          for (int nt = 0; nt < 4; ++nt) acc[nt] = mfma16(af[nt], xr[s], acc[nt]);
        }
      }
      TAGGET();
      lds_wait(L + L_SY, 0x020000);
      const v2u ln = *(const LAS v2u*)(L + L_LN + (l16 & 7) * 8);
      const float mean = u2f(ln.x), rstd = u2f(ln.y);
      LGKM0();
      LBYTE(12 + wave, 1);
#pragma unroll
      for (int nt = 0; nt < 4; ++nt)
#pragma unroll
        for (int j = 0; j < 4; ++j) acc[nt][j] = (acc[nt][j] - mean * dppm<0x158>(acc[nt][j])) * rstd;
      if (wave) {
#pragma unroll
        for (int nt = 0; nt < 4; ++nt) {
          const unsigned c = dppu<0x128>(f2u(acc[nt][2])), d = dppu<0x128>(f2u(acc[nt][3]));
          __builtin_amdgcn_raw_buffer_store_b128((v4u){l16 < 8 ? f2u(acc[nt][0]) : c, tag, l16 < 8 ? f2u(acc[nt][1]) : d, tag}, wsr,
                                                 WS_GU + ((((mat * 4 + kq1) * 176 + cg * 8 + 2 * nt + (g4 >> 1)) * 8 + (l16 & 7)) * 4 + 2 * (g4 & 1) + (l16 >> 3)) * 16, 0, AUX_SC1);
        }
      } else {
#pragma unroll
        for (int i = 0; i < 2; ++i) {
          const unsigned a = pk2(acc[2 * i][0], acc[2 * i][1]), b = pk2(acc[2 * i][2], acc[2 * i][3]);
          const unsigned c = dppu<0x128>(pk2(acc[2 * i + 1][0], acc[2 * i + 1][1])), d = dppu<0x128>(pk2(acc[2 * i + 1][2], acc[2 * i + 1][3]));
          __builtin_amdgcn_raw_buffer_store_b128((v4u){l16 < 8 ? a : c, tag, l16 < 8 ? b : d, tag}, wsr,
                                                 WS_QP + xg * RS + (hA & 3) * 16384 + ((((sl >> 2) * 2 + i) * 2 + (kq1 >> 1)) * 2 + (g4 & 1)) * 1024 +
                                                     (2 * (l16 >> 3) + (g4 >> 1)) * 256 + ((kq1 & 1) * 8 + (l16 & 7)) * 16, 0, AUX_SC1);
        }
        asm volatile("" ::: "memory");
        LBYTE(17, 1);
      }
      STAMP(2);
    } else if (wave == 1) {
      BAR();
      LBYTE(13, 1);
    } else if (wave == 2) {
      {
        const u16* src = p.wuk + ((size_t)(sl * 64) * NH + hA) * HD;
        const unsigned vo = g4 * 8192 + (l16 ^ g4) * 16;
        BAR();
#pragma unroll
        for (int n = 0; n < 16; ++n) GLDS(src + n * 4 * NH * HD, vo ^ ((n & 3) * 64), L_WU + n * 1024);
        LBYTE(16, 1);
      }
      VMWAIT(0);
      STAMP(1);
      v4u wf[16];
#pragma unroll
      for (int n = 0; n < 16; ++n) wf[n] = *(const LAS v4u*)(L + L_WU + ((n >> 2) * 16 + l16) * 256 + (((4 * (n & 3) + g4) ^ l16) * 16));
      LGKM0();
      LBYTE(14, 1);
      TAGGET();
      WAIT_UNTIL(TP);
      v4u qr[16];
      {
        bool todo = true;
#pragma nounroll
        for (unsigned spin = 0; spin < SPIN_CAP; ++spin) {
          if (todo) {
            GSTART(16, qr, WS_QP + xg * RS + (hA & 3) * 16384 + i * 1024 + lane * 16);
            unsigned bad = 0;
#pragma unroll
            for (int i = 0; i < 16; ++i) bad |= (qr[i].y ^ tag) | (qr[i].w ^ tag);
            todo = bad != 0;
          }
          if (!__any(todo)) break;
          __builtin_amdgcn_s_sleep(4);
          asm volatile("" ::: "memory");
        }
      }
      STAMP(2);
      const float scale = 0.08838834764831845f * 1.4426950408889634f;
      unsigned yp[2][2];
#pragma unroll
      for (int cp = 0; cp < 2; ++cp) {
        f32x4 a0 = {0.f, 0.f, 0.f, 0.f}, a1 = {0.f, 0.f, 0.f, 0.f};
#pragma unroll
        for (int k = 0; k < 8; ++k) {
          const v4u bq = {qr[2 * k].x, qr[2 * k].z, qr[2 * k + 1].x, qr[2 * k + 1].z};
          a0 = mfma16(wf[cp * 8 + (k >> 1)], bq, a0);
          a1 = mfma16(wf[cp * 8 + 4 + (k >> 1)], bq, a1);
        }
        float y[4];
#pragma unroll
        for (int j = 0; j < 4; ++j) {
          const float keep = l16 < 8 ? a0[j] : a1[j], give = l16 < 8 ? a1[j] : a0[j];
          y[j] = (keep + dppm<0x128>(give)) * scale;
        }
        yp[cp][0] = pk2(y[0], y[1]); yp[cp][1] = pk2(y[2], y[3]);
      }
      lds_wait(L + L_SY + 20, 0x010000);
      {
        auto s0 = __builtin_amdgcn_permlane16_swap(yp[0][0], yp[1][0], false, false);
        auto s1 = __builtin_amdgcn_permlane16_swap(yp[0][1], yp[1][1], false, false);
        __builtin_amdgcn_raw_buffer_store_b128((v4u){min(s0[0], ~1u), min(s1[0], ~1u), min(s0[1], ~1u), min(s1[1], ~1u)}, wsr,
                                               WS_QA + (l16 & 7) * RS + ((sl >> 1) * 32 + hA) * 256 + (sl & 1) * 128 +
                                                   (2 * (g4 & 1) + (l16 >> 3)) * 32 + (g4 >> 1) * 16, 0, AUX_SC1);
      }
      asm volatile("" ::: "memory");
      LBYTE(18, 1);
      STAMP(12);
    } else {
      __builtin_amdgcn_raw_buffer_store_b128((v4u){~0u, ~0u, ~0u, ~0u}, wsr, WS_QA + r8 * RS + ((sl >> 1) * 32 + hA) * 256 + (sl & 1) * 128 + q8 * 16, 0, AUX_SC1);
      BAR();
      VMWAIT(0);
      LBYTE(22, 1);
      TAGGET();
      if (lane < 8) __builtin_amdgcn_raw_buffer_store_b32(tag, wsr, WS_QF + lane * RS + hA * 256 + sl * 4, 0, AUX_SC1);
      STAMP(12);
    }
    v4u hole;
    {
      const unsigned qo = WS_QA + bA * RS + wave * 8192 + lane * 16;
      const unsigned qd = L_S + wave * 8192 + g4 * 256;
      lds_wait(L + L_SY + 20, 0x0100);
      WAIT_UNTIL(TG);
      STAMP(3);
      unsigned todo = 0x1ff;
#pragma nounroll
      for (unsigned spin = 0; spin < SPIN_CAP; ++spin) {
        v4u qa[9];
#pragma unroll
        for (int i = 0; i < 8; ++i) qa[i] = GLOAD(((todo >> i) & 1) ? qo + i * 1024 : OOB);
        qa[8] = GLOAD((todo >> 8) ? WS_QA + bA * RS + 3 * 8192 + 224 + hf * 16 : OOB);
#pragma unroll
        for (int i = 0; i < 9; ++i) {
          const bool ok = ((todo >> i) & 1) && max(max(qa[i].x, qa[i].y), max(qa[i].z, qa[i].w)) != ~0u;
          if (ok) {
            todo &= ~(1u << i);
            if (i == 8) hole = qa[8];
            else if (!(i == 0 && wave == 3 && lane >= 14 && lane < 16))
              *(volatile LAS v4u*)(L + qd + i * 1024 + ((l16 ^ ((g4 << 2) | (i & 3))) * 16)) = qa[i];
          }
        }
        if (!__any(todo != 0)) break;
        __builtin_amdgcn_s_sleep(4);
        asm volatile("" ::: "memory");
      }
      LGKM0();
      LBYTE(4 + wave, 1);
      STAMP(13);
    }
    f32x16 sacc;
    {
#pragma unroll
      for (int i = 0; i < 16; ++i) sacc[i] = 0.f;
      const unsigned tz = (hf ^ zl) * 16;
      const unsigned qb = L_S + l32 * 256 + tz;
      const unsigned kb = L_CKV + (wave * 32 + l32) * 256 + tz;
      v4u qf[2][8], kf[2][8];
      unsigned long long have = lds_wait64(L + L_SY, 0x0000000100000101ull);
      STAMP(4);
#pragma unroll
      for (int s = 0; s < 8; ++s) { kf[0][s] = *(const LAS v4u*)(L + (kb ^ (s * 32))); qf[0][s] = *(const LAS v4u*)(L + (qb ^ (s * 32))); }
#pragma unroll
      for (int blk = 0; blk < 4; ++blk) {
        if (blk < 3) {
          const unsigned long long need = (0x0101ull << (blk + 1)) | (1ull << (40 + 8 * blk));
          if ((have & need) != need) {
#pragma unroll
            for (int s = 0; s < 8; ++s) sacc = mfma32(kf[blk & 1][s], qf[blk & 1][s], sacc);
            if (blk == 1) LBYTE(8 + wave, 1);
            have = lds_wait64(L + L_SY, need);
#pragma unroll
            for (int s = 0; s < 8; ++s) {
              kf[(blk + 1) & 1][s] = *(const LAS v4u*)(L + (kb ^ (s * 32)) + (blk + 1) * 32768);
              qf[(blk + 1) & 1][s] = *(const LAS v4u*)(L + (qb ^ (s * 32)) + (blk + 1) * 8192);
            }
          } else {
#pragma unroll
            for (int s = 0; s < 8; ++s) {
              kf[(blk + 1) & 1][s] = *(const LAS v4u*)(L + (kb ^ (s * 32)) + (blk + 1) * 32768);
              qf[(blk + 1) & 1][s] = *(const LAS v4u*)(L + (qb ^ (s * 32)) + (blk + 1) * 8192);
              sacc = mfma32(kf[blk & 1][s], qf[blk & 1][s], sacc);
            }
            if (blk == 1) LBYTE(8 + wave, 1);
            if (blk < 2) have = lds_peek64(L + L_SY);
          }
        } else {
          if (l32 == 0) qf[1][7] = hole;
#pragma unroll
          for (int s = 0; s < 8; ++s) sacc = mfma32(kf[1][s], qf[1][s], sacc);
        }
      }
    }
    STAMP(5);
    lds_wait(L + L_SY + 8, 0x01010101);
    float mx = fmaxf(fmaxf(fmaxf(sacc[0], sacc[1]), fmaxf(sacc[2], sacc[3])), fmaxf(fmaxf(sacc[4], sacc[5]), fmaxf(sacc[6], sacc[7])));
    mx = fmaxf(mx, fmaxf(fmaxf(fmaxf(sacc[8], sacc[9]), fmaxf(sacc[10], sacc[11])), fmaxf(fmaxf(sacc[12], sacc[13]), fmaxf(sacc[14], sacc[15]))));
    mx = __builtin_ceilf(xmax32(mx));
    float ls = 0.f;
#pragma unroll
    for (int q = 0; q < 4; ++q) {
      const unsigned p0 = pk2(__builtin_amdgcn_exp2f(sacc[4 * q] - mx), __builtin_amdgcn_exp2f(sacc[4 * q + 1] - mx));
      const unsigned p1 = pk2(__builtin_amdgcn_exp2f(sacc[4 * q + 2] - mx), __builtin_amdgcn_exp2f(sacc[4 * q + 3] - mx));
      *(LAS v2u*)(L + L_P + l32 * 256 + (((wave * 4 + q) ^ zl) * 16) + hf * 8) = (v2u){p0, p1};
      ls = dot_ones(p1, dot_ones(p0, ls));
    }
    ls = xsum32(ls);
    *(LAS float*)(L + L_ST + hf * 512 + l32 * 16 + wave * 4) = hf ? ls : mx;
  }
  BAR();
  const unsigned wb = L_CKV + (wave >> 1) * 32768;
  const unsigned wq8 = (wave & 1) * 8;
  f32x16 o0, o1;
  {
    v4u pf[8];
    const f32x4 mv = *(const LAS f32x4*)(L + L_ST + l32 * 16), lv = *(const LAS f32x4*)(L + L_ST + 512 + l32 * 16);
    tagv = *(const LAS unsigned*)(L + L_SY + 24);
    {
      const unsigned pbs = L_P + l32 * 256 + (hf ^ zl) * 16;
#pragma unroll
      for (int ks = 0; ks < 8; ++ks) pf[ks] = *(const LAS v4u*)(L + (pbs ^ (ks * 32)));
    }
    const float M = fmaxf(fmaxf(mv[0], mv[1]), fmaxf(mv[2], mv[3]));
#pragma unroll
    for (int w = 0; w < 4; ++w) {
      const unsigned sh = (unsigned)fminf(M - mv[w], 255.f) * 0x00800080u;
#pragma unroll
      for (int e = 0; e < 2; ++e) {
        v4u t = pf[2 * w + e];
        pf[2 * w + e] = (v4u){pksubsat(t.x, sh), pksubsat(t.y, sh), pksubsat(t.z, sh), pksubsat(t.w, sh)};
      }
    }
    if (wave == 5) {
      const float lt = (lv[0] * __builtin_amdgcn_exp2f(mv[0] - M) + lv[1] * __builtin_amdgcn_exp2f(mv[1] - M)) +
                       (lv[2] * __builtin_amdgcn_exp2f(mv[2] - M) + lv[3] * __builtin_amdgcn_exp2f(mv[3] - M));
      if (lane < 32)
        __builtin_amdgcn_raw_buffer_store_b64((v2u){f2u(M), f2u(lt)}, wsr, WS_FL + ((((lane >> 2) * 8 + bA) * 32 + chunk) * 4 + (lane & 3)) * 8, 0, AUX_SC1);
    }
#pragma unroll
    for (int i = 0; i < 16; ++i) { o0[i] = 0.f; o1[i] = 0.f; }
    const unsigned blk = (lane >> 4) & 1, q = (lane & 15) >> 2, pp = lane & 3;
    unsigned ta[2];
#pragma unroll
    for (int tt = 0; tt < 2; ++tt) {
      const unsigned row = 8 * hf + 4 * tt + q;
      ta[tt] = wb + row * 256 + (((wq8 + 2 * blk + (pp >> 1)) ^ swz(row)) * 16) + 8 * (pp & 1);
    }
#pragma unroll
    for (int ks = 0; ks < 8; ++ks) {
      o0 = mfma32(pf[ks], tr_read2(L + ta[0] + ks * 4096, L + ta[1] + ks * 4096), o0);
      o1 = mfma32(pf[ks], tr_read2(L + (ta[0] ^ 64) + ks * 4096, L + (ta[1] ^ 64) + ks * 4096), o1);
    }
  }
  STAMP(6);
  {
    const unsigned pbase = WS_PART + ((((bA * 32 + 4 * (g4 >> 1) + (g4 & 1)) * 8 + wave) * 32 + chunk) * 16 + l16) * 8;
#pragma unroll
    for (int i = 0; i < 16; i += 2) {
      auto s = __builtin_amdgcn_permlane16_swap(pk2(o0[i], o1[i]), pk2(o0[i + 1], o1[i + 1]), false, false);
      __builtin_amdgcn_raw_buffer_store_b64((v2u){s[0], s[1]}, wsr, pbase + (8 * (i >> 2) + (i & 3)) * (8 * 32 * 128), 0, AUX_SC1);
    }
  }
  tag = __builtin_amdgcn_readfirstlane(tagv);
  STAMP(7);
  VMWAIT(0);
  if (lds_ticket(L + L_SY + 28, lane) == 7) {
    if (lane < 8) __builtin_amdgcn_raw_buffer_store_b32(tag, wsr, WS_PF + lane * RS + (bA * 4 + (chunk >> 3)) * 256 + (chunk & 7) * 4, 0, AUX_SC1);
  }
  STAMP(8);
  KEEPV(tpf);
  BAR();
  {
    const unsigned cr = 2 * wave + (r8 >> 2) + (r8 & 1) * 32 + (r8 & 2) * 8;
    const u16* sv = p.wuv + ((size_t)(sl * 64) * NH + hA) * HD;
    GLDS(sv, cr * 8192 + pz * 16, T_V + wave * 1024);
    GLDS(sv + 64, cr * 8192 + pz * 16, T_V + 8192 + wave * 1024);
    const u16* so = p.wo + ((size_t)hB * HD + wave * 8) * DM + sl * 64;
    GLDS(so, r8 * 1024 + pz * 16, T_O + wave * 1024);
    GLDS(so + 64 * DM, r8 * 1024 + pz * 16, T_O + 8192 + wave * 1024);
    GLDS(p.wd + (size_t)task0 * 8 * DM + sl * 64, min(wave * 8 + (int)r8, ntask * 8 - 1) * 1024 + pz * 16, T_D + wave * 1024);
  }
  if (wave < 4 && NFFN) GSTART(4, tw, ((i & 2) ? h1 : h0) + (i & 1) * 16);
  {
    {
      unsigned fo = lane < 8 ? WS_PF + xg * RS + (wave * 4 + (lane >> 1)) * 256 + (lane & 1) * 16 : OOB;
      if (PDELAY) __builtin_amdgcn_s_sleep(PDELAY);
      if (TF) WAIT_UNTIL(TF);
#pragma nounroll
      for (unsigned spin = 0; spin < SPIN_CAP; ++spin) {
        const v4u f = GLOAD(fo);
        if (OK_FLG(f)) fo = OOB;
        if (__all(fo == OOB)) break;
        __builtin_amdgcn_s_sleep(PSLP);
        asm volatile("" ::: "memory");
      }
      asm volatile("" ::: "memory");
    }
    STAMP(14);
    v2u pr[9];
    const unsigned po = WS_PART + ((((wave * 32 + hA) * 8 + sl) * 32) * 16 + lane) * 8;
    const unsigned fo = WS_FL + (((xg * 8 + wave) * 32 + l32) * 4 + (hA & 3)) * 8;
    pr[8] = __builtin_amdgcn_raw_buffer_load_b64(wsr, fo, 0, AUX_SC1);
#pragma unroll
    for (int i = 0; i < 8; ++i) pr[i] = __builtin_amdgcn_raw_buffer_load_b64(wsr, po + i * 512, 0, AUX_SC1);
    const float mj = u2f(pr[8].x);
    float M = mj;
    M = fmaxf(M, dppm<0x121>(M)); M = fmaxf(M, dppm<0x122>(M)); M = fmaxf(M, dppm<0x124>(M)); M = fmaxf(M, dppm<0x128>(M));
    M = xmax16(M);
    const float wj = bfhi(f2u(__builtin_amdgcn_exp2f(mj - M)));
    float Ls = wj * u2f(pr[8].y);
    Ls += dppm<0x121>(Ls); Ls += dppm<0x122>(Ls); Ls += dppm<0x124>(Ls); Ls += dppm<0x128>(Ls);
    Ls = xsum16(Ls);
    const float inv = __builtin_amdgcn_rcpf(Ls);
    const unsigned e4 = l16 & 3;
    const unsigned sh = (e4 & 1) ? 0u : 16u, k0 = e4 < 2 ? ~0u : 0u;
    unsigned wa[16];
#pragma unroll
    for (int u = 0; u < 8; ++u) {
      const unsigned w = (unsigned)__builtin_amdgcn_ds_bpermute((g4 + 4 * u) * 4, f2u(wj)) >> sh;
      wa[2 * u] = w & k0; wa[2 * u + 1] = w & ~k0;
    }
    f32x4 acc = {0.f, 0.f, 0.f, 0.f};
#pragma unroll
    for (int u = 0; u < 4; ++u)
      acc = mfma16((v4u){wa[4 * u], wa[4 * u + 1], wa[4 * u + 2], wa[4 * u + 3]}, (v4u){pr[2 * u].x, pr[2 * u].y, pr[2 * u + 1].x, pr[2 * u + 1].y}, acc);
    if (lane < 16) *(LAS v2u*)(L + L_CL + wave * 144 + lane * 8) = (v2u){pk2(acc[0] * inv, acc[1] * inv), pk2(acc[2] * inv, acc[3] * inv)};
    BAR();
  }
  v4u ao[4];
  f32x4 accE = {0.f, 0.f, 0.f, 0.f};
  const unsigned one = (l16 & 1) ? 0x3f800000u : 0x00003f80u;
  const unsigned s0 = (l16 & 2) ? 0u : one, s1 = (l16 & 2) ? one : 0u;
  {
    const v4u b0 = *(const LAS v4u*)(L + L_CL + (l16 & 7) * 144 + g4 * 16), b1 = *(const LAS v4u*)(L + L_CL + (l16 & 7) * 144 + 64 + g4 * 16);
    f32x4 acc = {0.f, 0.f, 0.f, 0.f};
    acc = mfma16(FRAG(T_V + (wave >> 2) * 8192, wave & 3, 0), b0, acc);
    acc = mfma16(FRAG(T_V + (wave >> 2) * 8192, wave & 3, 1), b1, acc);
    if (l16 < 8)
      __builtin_amdgcn_raw_buffer_store_b128((v4u){pk2(acc[0], acc[1]), tag, pk2(acc[2], acc[3]), tag}, wsr,
                                             WS_CX + ((((hA * 8 + wave) * 8 + sl) * 8 + l16) * 4 + g4) * 16, 0, AUX_SC1);
    STAMP(9);
    if (wave < 4) {
#pragma unroll
      for (int ks = 0; ks < 4; ++ks) ao[ks] = FRAG(T_O, wave, ks);
      const v4u ad0 = FRAG(T_D, wave, 0), ad1 = FRAG(T_D, wave, 1);
      if (NFFN) GFINISH(4, tw, ((i & 2) ? h1 : h0) + (i & 1) * 16, 1);
      accE = mfma16(ad0, (v4u){tw[0].x, tw[0].z, tw[1].x, tw[1].z}, accE);
      accE = mfma16(ad1, hon ? (v4u){tw[2].x, tw[2].z, tw[3].x, tw[3].z} : (v4u){0u, 0u, 0u, 0u}, accE);
    }
    v4u cr[4];
    const unsigned co = WS_CX + ((hB * 8 + wave) * 256 + lane) * 16;
    if (CDELAY) __builtin_amdgcn_s_sleep(CDELAY);
#pragma nounroll
    for (unsigned spin = 0; spin < SPIN_CAP; ++spin) {
#pragma unroll
      for (int i = 0; i < 4; ++i) cr[i] = GLOAD(co + i * 1024);
      if (__all((cr[0].y == tag) & (cr[0].w == tag) & (cr[1].y == tag) & (cr[1].w == tag) & (cr[2].y == tag) & (cr[2].w == tag) & (cr[3].y == tag) & (cr[3].w == tag))) break;
      __builtin_amdgcn_s_sleep(1);
    }
    STAMP(10);
    {
      const bool on = ((l16 >> 2) & 1) == (g4 & 1);
      const unsigned t0 = on ? s0 : 0u, t1 = on ? s1 : 0u;
      f32x4 a = {0.f, 0.f, 0.f, 0.f};
      a = mfma16((v4u){t0, t1, t0, t1}, (v4u){cr[0].x, cr[0].z, cr[1].x, cr[1].z}, a);
      a = mfma16((v4u){t0, t1, t0, t1}, (v4u){cr[2].x, cr[2].z, cr[3].x, cr[3].z}, a);
      if (lane < 32) *(LAS v2u*)(L + L_CX + (lane >> 2) * 272 + wave * 32 + (lane & 3) * 8) = (v2u){pk2(a[0], a[1]), pk2(a[2], a[3])};
    }
    BAR();
  }
  if (wave < 4) {
#pragma unroll
    for (int ks = 0; ks < 4; ++ks) accE = mfma16(ao[ks], *(const LAS v4u*)(L + L_CX + (l16 & 7) * 272 + ks * 64 + g4 * 16), accE);
    if (l16 < 8)
      __builtin_amdgcn_raw_buffer_store_b128((v4u){pk2(accE[0], accE[1]), tag, pk2(accE[2], accE[3]), tag}, wsr,
                                             WS_OG + ((((sl * 4 + wave) * 32 + hB) * 8 + l16) * 4 + g4) * 16, 0, AUX_SC1);
    STAMP(11);
  } else if (wave == 4) {
    const unsigned oo = WS_OG + ((((wg & 31) * 32 + (lane >> 2)) * 8 + (wg >> 5)) * 4 + (lane & 3)) * 16;
    const v2u xw = *(const GAS v2u*)(p.x + wg * 16 + (lane & 3) * 4);
    v4u r0, r1;
    if (ODELAY) __builtin_amdgcn_s_sleep(ODELAY);
#pragma nounroll
    for (unsigned spin = 0; spin < SPIN_CAP; ++spin) {
      r0 = GLOAD(oo);
      r1 = GLOAD(oo + 16 * 512);
      if (__all((r0.y == tag) & (r0.w == tag) & (r1.y == tag) & (r1.w == tag))) break;
      __builtin_amdgcn_s_sleep(1);
    }
    f32x4 acc = {0.f, 0.f, 0.f, 0.f};
    acc = mfma16((v4u){s0, s1, s0, s1}, (v4u){r0.x, r0.z, r1.x, r1.z}, acc);
#pragma unroll
    for (int j = 0; j < 4; ++j) { acc[j] += dppm<0x124>(acc[j]); acc[j] += dppm<0x128>(acc[j]); }
    if (lane < 4)
      *(GAS v2u*)(p.out + wg * 16 + lane * 4) = (v2u){pk2(bflo(xw.x) + acc[0], bfhi(xw.x) + acc[1]), pk2(bflo(xw.y) + acc[2], bfhi(xw.y) + acc[3])};
    if (wg == 0 && lane == 0) __builtin_amdgcn_raw_buffer_store_b32(tag, wsr, WS_EPOCH, 0, AUX_SC1);
    STAMP(11);
  }
}

extern "C" void kernel_launch(void* const* d_in, const int* in_sizes, int n_in, void* d_out, int out_size, void* d_ws, size_t ws_size,
                              hipStream_t stream) {
  Params p;
  p.x = (const u16*)d_in[0]; p.ckv = (const u16*)d_in[1]; p.wq = (const u16*)d_in[2]; p.wuk = (const u16*)d_in[3];
  p.wuv = (const u16*)d_in[4]; p.wo = (const u16*)d_in[5]; p.wg = (const u16*)d_in[7]; p.wu = (const u16*)d_in[8]; p.wd = (const u16*)d_in[9];
  p.out = (u16*)d_out; p.ws = (unsigned char*)d_ws;
  (void)hipFuncSetAttribute((const void*)fused_kernel, hipFuncAttributeMaxDynamicSharedMemorySize, LDS_BYTES);
  fused_kernel<<<NWG, NTHR, LDS_BYTES, stream>>>(p);
}
```

```cpp
#include <hip/hip_runtime.h>
#include <hip/hip_bf16.h>
#include <stdint.h>

#define STAMPS 0
#define CODE_LINES 164
#define WIN 14
#define CDELAY 8
#define ODELAY 8
#define NFFN 176
#define WIN0 6
#define EMASK 0x010000
#define PSLP 1
#define TF 0
#define FPL 32
#define TP 310
#define TQ 310
#define OV 5
#define PM 0
#define QS 0
#define CXN 8
#define NOO 0
#define OD 0
#define QM 0
#define TG 500

typedef unsigned v4u __attribute__((ext_vector_type(4)));
typedef unsigned v2u __attribute__((ext_vector_type(2)));
typedef float f32x2 __attribute__((ext_vector_type(2)));
typedef float f32x4 __attribute__((ext_vector_type(4)));
typedef float f32x16 __attribute__((ext_vector_type(16)));
typedef short bf16x8 __attribute__((ext_vector_type(8)));
typedef short s16x4 __attribute__((ext_vector_type(4)));
typedef unsigned short u16x2 __attribute__((ext_vector_type(2)));
typedef __bf16 bf16x2 __attribute__((ext_vector_type(2)));
typedef unsigned short u16;
#define LAS __attribute__((address_space(3)))
#define GAS __attribute__((address_space(1)))

constexpr int NB = 8, NH = 32, SK = 4096, HD = 128, DM = 512, DC = 512, FF = 1376;
constexpr int NWG = 256, NTHR = 512;
constexpr unsigned WS_EPOCH = 0;
#define RS 262144
constexpr unsigned WS_QF = 4096;
constexpr unsigned WS_PF = WS_QF + 8192;
constexpr unsigned WS_QA = WS_PF + 8192;
constexpr unsigned WS_QP = WS_QA + 32768;
static_assert(WS_QP + 65536 <= RS, "ws map");
constexpr unsigned WS_GU = 8 * RS;
constexpr unsigned WS_FL = WS_GU + 176 * 2048;
constexpr unsigned WS_CX = WS_FL + 8 * 8 * 32 * 4 * 16;
constexpr unsigned WS_OG = WS_CX + 32 * 8 * 8 * 8 * 4 * 16;
constexpr unsigned WS_PART = 6u << 20;
constexpr unsigned WS_END = WS_PART + (8u << 20);
constexpr unsigned WS_STAMP = 32u << 20;
static_assert(WS_OG + 32 * 32 * 8 * 4 * 16 <= WS_PART, "ws map");
constexpr unsigned L_CKV = 0;
constexpr unsigned L_S = 128 * 1024;
constexpr unsigned L_SY = L_S + 24576 + 224;
constexpr unsigned L_WQ = L_S, L_WU = 98304, L_WF = 114688;
constexpr unsigned L_X = L_S + 16384;
constexpr unsigned L_LN = L_S + 25600;
constexpr unsigned L_DUMP = L_S + 26624;
constexpr unsigned T_O = 0, T_V = 16384, T_D = 32768, T_W = 49152;
constexpr unsigned L_P = L_S;
constexpr unsigned L_ST = L_S + 8192;
constexpr unsigned L_CL = L_S + 16384;
constexpr unsigned L_CX = L_S + 16384 + 2048;
constexpr unsigned L_HM = L_S + 16384 + 4608;
constexpr unsigned LDS_BYTES = 160 * 1024;
constexpr int AUX_NT = 2, AUX_SC1 = 16;
constexpr unsigned SPIN_CAP = 1u << 18;
constexpr unsigned OOB = 0x80000000u;

struct Params {
  const u16 *x, *ckv, *wq, *wuk, *wuv, *wo, *wg, *wu, *wd;
  u16* out;
  unsigned char* ws;
};

__device__ __forceinline__ float u2f(unsigned u) { return __uint_as_float(u); }
__device__ __forceinline__ unsigned f2u(float f) { return __float_as_uint(f); }
__device__ __forceinline__ float bflo(unsigned w) { return u2f(w << 16); }
__device__ __forceinline__ float bfhi(unsigned w) { return u2f(w & 0xffff0000u); }
__device__ __forceinline__ unsigned pk2(float lo, float hi) {
  f32x2 f = {lo, hi};
  return __builtin_bit_cast(unsigned, __builtin_convertvector(f, bf16x2));
}
__device__ __forceinline__ float dot_ones(unsigned a, float c) {
  return __builtin_amdgcn_fdot2_f32_bf16(__builtin_bit_cast(bf16x2, a), __builtin_bit_cast(bf16x2, 0x3f803f80u), c, false);
}
__device__ __forceinline__ unsigned pksubsat(unsigned a, unsigned b) {
  return __builtin_bit_cast(unsigned, __builtin_elementwise_sub_sat(__builtin_bit_cast(u16x2, a), __builtin_bit_cast(u16x2, b)));
}
__device__ __forceinline__ f32x4 mfma16(v4u a, v4u b, f32x4 c) {
  return __builtin_amdgcn_mfma_f32_16x16x32_bf16(__builtin_bit_cast(bf16x8, a), __builtin_bit_cast(bf16x8, b), c, 0, 0, 0);
}
__device__ __forceinline__ f32x16 mfma32(v4u a, v4u b, f32x16 c) {
  return __builtin_amdgcn_mfma_f32_32x32x16_bf16(__builtin_bit_cast(bf16x8, a), __builtin_bit_cast(bf16x8, b), c, 0, 0, 0);
}
__device__ __forceinline__ float max3(float a, float b, float c) {
  float d;
  asm("v_max3_f32 %0, %1, %2, %3" : "=v"(d) : "v"(a), "v"(b), "v"(c));
  return d;
}
__device__ __forceinline__ unsigned swz(unsigned row) { return ((row & 3) << 2) | ((row >> 2) & 3); }
#define BAR() do { asm volatile("s_waitcnt lgkmcnt(0)" ::: "memory"); __builtin_amdgcn_s_barrier(); asm volatile("" ::: "memory"); } while (0)
#define VMWAIT(n) do { asm volatile("" ::: "memory"); __builtin_amdgcn_s_waitcnt(0x0f70 | ((n) & 15) | (((n) >> 4) << 14)); asm volatile("" ::: "memory"); } while (0)
#define LGKM0() asm volatile("s_waitcnt lgkmcnt(0)" ::: "memory")
#define GLDS_(OP, sbase, voff, dst) asm volatile("s_mov_b32 m0, %2\n\ts_nop 0\n\t" OP :: "v"((unsigned)(voff)), "s"((const void*)(sbase)), "s"((unsigned)(dst)) : "memory", "m0")
#define GLDS(sbase, voff, dst) GLDS_("global_load_lds_dwordx4 %0, %1 nt", sbase, voff, dst)
#define GLDSX(sbase, voff, dst) GLDS_("global_load_lds_dwordx4 %0, %1", sbase, voff, dst)
#define GLDS4(sbase, voff, dst) GLDS_("global_load_lds_dword %0, %1 nt", sbase, voff, dst)
#define PFR(sbase, voff) asm volatile("global_load_dword %0, %1, %2" : "+v"(tpf) : "v"((unsigned)(voff)), "s"((const void*)(sbase)) : "memory")
#define WLOAD(p) __builtin_nontemporal_load(p)
#define LBYTE(off, v) (*(volatile LAS unsigned char*)(L + L_SY + (off)) = (v))
__device__ __forceinline__ v2u tr_read(LAS unsigned char* a) {
  return __builtin_bit_cast(v2u, __builtin_amdgcn_ds_read_tr16_b64_v4i16((LAS s16x4*)a));
}
__device__ __forceinline__ v4u tr_read2(LAS unsigned char* a, LAS unsigned char* b) {
  const v2u x = tr_read(a), y = tr_read(b);
  return (v4u){x.x, x.y, y.x, y.y};
}
template <int CTRL>
__device__ __forceinline__ unsigned dppu(unsigned v) { return __builtin_amdgcn_update_dpp(v, v, CTRL, 0xf, 0xf, false); }
template <int CTRL>
__device__ __forceinline__ float dppm(float v) { return u2f(dppu<CTRL>(f2u(v))); }
__device__ __forceinline__ float xsum16(float v) {
  auto s = __builtin_amdgcn_permlane16_swap(f2u(v), f2u(v), false, false);
  return u2f(s[0]) + u2f(s[1]);
}
__device__ __forceinline__ float xmax16(float v) {
  auto s = __builtin_amdgcn_permlane16_swap(f2u(v), f2u(v), false, false);
  return fmaxf(u2f(s[0]), u2f(s[1]));
}
__device__ __forceinline__ float xsum32(float v) {
  auto s = __builtin_amdgcn_permlane32_swap(f2u(v), f2u(v), false, false);
  return u2f(s[0]) + u2f(s[1]);
}
__device__ __forceinline__ float xmax32(float v) {
  auto s = __builtin_amdgcn_permlane32_swap(f2u(v), f2u(v), false, false);
  return fmaxf(u2f(s[0]), u2f(s[1]));
}
__device__ __forceinline__ bool rec_ok(v4u r, unsigned tag) { return __all((r.y == tag) & (r.w == tag)); }
__device__ __forceinline__ bool flg_ok(v4u r, unsigned tag) { return __all((r.x == tag) & (r.y == tag) & (r.z == tag) & (r.w == tag)); }
__device__ __forceinline__ unsigned lds_wait(LAS unsigned char* a, unsigned mask) {
  unsigned w = 0;
#pragma nounroll
  for (unsigned spin = 0; spin < (SPIN_CAP << 4); ++spin) {
    w = __builtin_amdgcn_readfirstlane(*(volatile LAS unsigned*)a);
    if ((w & mask) == mask) break;
    __builtin_amdgcn_s_sleep(1);
  }
  asm volatile("" ::: "memory");
  return w;
}

__device__ __forceinline__ unsigned lds_nz(LAS unsigned char* a) {
  unsigned w = 0;
#pragma nounroll
  for (unsigned spin = 0; spin < (SPIN_CAP << 4); ++spin) {
    w = __builtin_amdgcn_readfirstlane(*(volatile LAS unsigned*)a);
    if (w) break;
    __builtin_amdgcn_s_sleep(1);
  }
  asm volatile("" ::: "memory");
  return w;
}
__device__ __forceinline__ unsigned long long lds_peek64(LAS unsigned char* a) {
  const v2u w = *(volatile LAS v2u*)a;
  return (unsigned long long)__builtin_amdgcn_readfirstlane(w.x) | ((unsigned long long)__builtin_amdgcn_readfirstlane(w.y) << 32);
}
__device__ __forceinline__ unsigned long long lds_wait64(LAS unsigned char* a, unsigned long long mask) {
  unsigned long long w = 0;
#pragma nounroll
  for (unsigned spin = 0; spin < (SPIN_CAP << 4); ++spin) {
    w = lds_peek64(a);
    if ((w & mask) == mask) break;
    __builtin_amdgcn_s_sleep(1);
  }
  asm volatile("" ::: "memory");
  return w;
}
__device__ __forceinline__ unsigned lds_ticket(LAS unsigned char* a, int lane) {
  unsigned t = 0;
  if (lane == 0) t = __hip_atomic_fetch_add((LAS unsigned*)a, 1u, __ATOMIC_RELAXED, __HIP_MEMORY_SCOPE_WORKGROUP);
  return __builtin_amdgcn_readfirstlane(t);
}
#define STAMP(k) do { } while (0)

#define GLOAD(ADDR) __builtin_amdgcn_raw_buffer_load_b128(wsr, (ADDR), 0, AUX_SC1)
#define GFINISH(N, qr, ADDR, SLP)                                                                            \
  do {                                                                                                       \
    unsigned done = 0;                                                                                       \
    _Pragma("nounroll") for (unsigned spin = 0; spin < SPIN_CAP; ++spin) {                                   \
      _Pragma("unroll") for (int i = 0; i < N; ++i)                                                          \
        if (!((done >> i) & 1)) { if (rec_ok(qr[i], tag)) done |= 1u << i; }                                 \
      if (done == (1u << N) - 1u) break;                                                                     \
      if (SLP) __builtin_amdgcn_s_sleep(SLP);                                                                \
      _Pragma("unroll") for (int i = 0; i < N; ++i)                                                          \
        if (!((done >> i) & 1)) qr[i] = GLOAD(ADDR);                                                         \
    }                                                                                                        \
  } while (0)
#define OK_FLG(r) (((r).x == tag) & ((r).y == tag) & ((r).z == tag) & ((r).w == tag))
#define KEEPV(r) asm volatile("" :: "v"(r))
#define WAIT_UNTIL(t) do { _Pragma("nounroll") for (unsigned spin = 0; spin < SPIN_CAP; ++spin) { if ((unsigned)__builtin_amdgcn_s_memrealtime() - t00 >= (t)) break; __builtin_amdgcn_s_sleep(1); } } while (0)
#define GSTART(N, qr, ADDR) do { _Pragma("unroll") for (int i = 0; i < N; ++i) qr[i] = GLOAD(ADDR); } while (0)

__global__ void __launch_bounds__(NTHR, 2) fused_kernel(Params p) {
  extern __shared__ __attribute__((aligned(16))) unsigned char lds_raw[];
  STAMP(0);
  const unsigned tstart = (unsigned)__builtin_amdgcn_s_memrealtime();
  static_assert(L_DUMP == 0x26800 && L_X == 0x24000 && L_WQ == 0x20000 && L_WU == 0x18000 && L_SY == 0x260e0 && CODE_LINES <= 192, "volley");
  asm volatile(
      "s_load_dwordx8 s[40:47], %0, 0x0\n\t"
      "v_readfirstlane_b32 s48, %2\n\t"
      "v_and_b32 v13, 63, %2\n\t"
      "v_mov_b32 v10, 0\n\t"
      "v_mov_b32 v11, 0\n\t"
      "v_mov_b32 v12, 0x260e0\n\t"
      "s_getpc_b64 s[50:51]\n\t"
      "s_lshr_b32 s48, s48, 6\n\t"
      "s_cmp_lg_u32 s48, 1\n\t"
      "s_cbranch_scc1 7f\n\t"
      "ds_write2_b64 v12, v[10:11], v[10:11] offset1:1\n\t"
      "ds_write2_b64 v12, v[10:11], v[10:11] offset0:2 offset1:3\n\t"
      "s_waitcnt lgkmcnt(0)\n"
      "7:\n\t"
      "s_mov_b32 s54, 0\n\t"
      "s_and_b32 s49, %1, 7\n\t"
      "s_cmp_lg_u32 s48, 4\n\t"
      "s_cbranch_scc1 1f\n\t"
      "s_bfe_u32 s52, %1, 0x40003\n\t"
      "s_mul_i32 s52, s52, 12\n\t"
      "v_min_u32 v10, 11, v13\n\t"
      "v_add_u32 v10, s52, v10\n\t"
      "v_min_u32 v10, %3, v10\n\t"
      "v_lshlrev_b32 v10, 7, v10\n\t"
      "s_andn2_b32 s50, s50, 0x7f\n\t"
      "s_mov_b32 m0, 0x26800\n\t"
      "s_nop 0\n\t"
      "global_load_lds_dword v10, s[50:51]\n\t"
      "s_branch 6f\n"
      "1:\n\t"
      "s_cmp_lg_u32 s48, 0\n\t"
      "s_cbranch_scc1 2f\n\t"
      "v_lshrrev_b32 v10, 3, v13\n\t"
      "v_and_b32 v11, 7, v13\n\t"
      "v_bfe_u32 v12, v13, 3, 2\n\t"
      "v_lshrrev_b32 v13, 5, v13\n\t"
      "v_lshl_or_b32 v12, v12, 1, v13\n\t"
      "v_xor_b32 v11, v11, v12\n\t"
      "v_lshlrev_b32 v11, 4, v11\n\t"
      "v_lshl_or_b32 v10, v10, 13, v11\n\t"
      "s_lshl_b32 s49, s49, 10\n\t"
      "s_lshr_b32 s52, %1, 5\n\t"
      "s_lshl_b32 s52, s52, 7\n\t"
      "s_or_b32 s49, s49, s52\n\t"
      "s_bfe_u32 s52, %1, 0x20003\n\t"
      "s_lshl_b32 s52, s52, 20\n\t"
      "s_or_b32 s49, s49, s52\n\t"
      "s_mov_b32 m0, 0x20000\n\t"
      "s_mov_b32 s56, 0x24000\n\t"
      "s_mov_b32 s52, 0x10000\n\t"
      "s_waitcnt lgkmcnt(0)\n\t"
      "s_add_u32 s50, s44, s49\n\t"
      "s_addc_u32 s51, s45, 0\n\t"
      "s_branch 5f\n"
      "2:\n\t"
      "s_cmp_lg_u32 s48, 5\n\t"
      "s_cbranch_scc1 6f\n\t"
      "v_lshlrev_b32 v10, 4, v13\n\t"
      "s_mov_b32 m0, 0x24000\n\t"
      "s_mov_b32 s56, 0x26000\n\t"
      "s_movk_i32 s52, 0x420\n\t"
      "s_waitcnt lgkmcnt(0)\n\t"
      "s_mov_b64 s[50:51], s[40:41]\n"
      "5:\n\t"
      "v_xor_b32 v11, s54, v10\n\t"
      "global_load_lds_dwordx4 v11, s[50:51]\n\t"
      "s_add_u32 s54, s54, s52\n\t"
      "s_add_u32 m0, m0, 0x400\n\t"
      "s_cmp_lg_u32 m0, s56\n\t"
      "s_cbranch_scc1 5b\n\t"
      "s_barrier\n"
      "6:\n\t"
      "s_waitcnt lgkmcnt(0)"
      :: "s"(__builtin_amdgcn_kernarg_segment_ptr()), "s"(__builtin_amdgcn_workgroup_id_x()), "v"(__builtin_amdgcn_workitem_id_x()), "n"(CODE_LINES - 1)
      : "memory", "m0", "scc", "s40", "s41", "s42", "s43", "s44", "s45", "s46", "s47", "s48", "s49", "s50", "s51", "s52", "s54", "s56",
        "v10", "v11", "v12", "v13");
  asm volatile("" :: "s"(p.x), "s"(p.ckv));
  STAMP(15);
  LAS unsigned char* L = (LAS unsigned char*)lds_raw;
  const int tid = threadIdx.x, lane = tid & 63;
  const int wave = __builtin_amdgcn_readfirstlane(tid >> 6);
  const int wg = blockIdx.x, xg = wg & 7, r = wg >> 3;
  const int w4 = __builtin_amdgcn_readfirstlane((tid >> 6) & 3);
  const int bA = xg, chunk = r;
  const int hA = xg * 4 + (r >> 3), sl = r & 7;
  const int hB = ((xg + 1) & 7) * 4 + (r >> 3);
  const int bO = (xg + 7) & 7, hO = (hA + 28) & 31;
  const int ntask = hB < 12 ? 6 : 5, task0 = hB < 12 ? 6 * hB : 12 + 5 * hB;
  const __amdgpu_buffer_rsrc_t wsr = __builtin_amdgcn_make_buffer_rsrc((void*)p.ws, 0, WS_END, 0x00020000);
  const int l16 = lane & 15, g4 = lane >> 4;
  const int l32 = lane & 31, hf = lane >> 5;
  const unsigned r8 = lane >> 3, q8 = lane & 7;
  const unsigned t00 = tstart;
  unsigned tag, tagv;
#define TAGLOAD() tagv = __builtin_amdgcn_raw_buffer_load_b32(wsr, WS_EPOCH, 0, 0)
#define TAGSET() do { tag = __builtin_amdgcn_readfirstlane(tagv) + 1u; tag += tag == 0; } while (0)
#define TAGGET() tag = lds_nz(L + L_SY + 24)
  const unsigned pz = q8 ^ (((r8 & 3) << 1) | (r8 >> 2));
  unsigned tpf = 0;
  const unsigned fa0 = (8 * g4 + (l16 >> 2)) * 128 + ((((l16 & 3) >> 1) ^ ((l16 >> 2) << 1)) * 16) + (l16 & 1) * 8;
  const unsigned fa1 = (fa0 ^ 16) + 512;
#define FRAG(base, nt, s) tr_read2(L + (base) + (fa0 ^ ((nt) * 32)) + (s) * 4096, L + (base) + (fa1 ^ ((nt) * 32)) + (s) * 4096)
  const bool hon = 4 + g4 < ntask;
  const unsigned zl = swz(l32);
  v4u vf[16];
  unsigned va[2];
#pragma unroll
  for (int tt = 0; tt < 2; ++tt) {
    const unsigned row = 8 * g4 + 4 * tt + (l16 >> 2);
    va[tt] = L_CKV + (wave >> 1) * 32768 + row * 256 + ((((wave & 1) * 8 + ((l16 & 3) >> 1)) ^ swz(row)) * 16) + 8 * (l16 & 1);
  }
#define VLOAD() do { _Pragma("unroll") for (int i = 0; i < 16; ++i) vf[i] = tr_read2(L + (va[0] ^ ((i & 3) * 32)) + (i >> 2) * 8192, L + (va[1] ^ ((i & 3) * 32)) + (i >> 2) * 8192); } while (0)
  f32x16 sacc;
#define TAILV() do { \
    const unsigned cr = 2 * wave + (r8 >> 2) + (r8 & 3) * 16; \
    const u16* sv = p.wuv + ((size_t)(sl * 64) * NH + hA) * HD; \
    GLDS(sv, cr * 8192 + pz * 16, T_V + wave * 1024); \
    GLDS(sv + 64, cr * 8192 + pz * 16, T_V + 8192 + wave * 1024); \
    const u16* sw = p.wuv + ((size_t)(sl * 64) * NH + hO) * HD; \
    GLDS(sw, cr * 8192 + pz * 16, T_W + wave * 1024); \
    GLDS(sw + 64, cr * 8192 + pz * 16, T_W + 8192 + wave * 1024); \
  } while (0)
#define TAILO(w) do { \
    const u16* so = p.wo + ((size_t)hB * HD + (w) * 8) * DM + sl * 64; \
    GLDS(so, r8 * 1024 + pz * 16, T_O + (w) * 1024); \
    GLDS(so + 64 * DM, r8 * 1024 + pz * 16, T_O + 8192 + (w) * 1024); \
    GLDS(p.wd + (size_t)task0 * 8 * DM + sl * 64, min((w) * 8 + (int)r8, ntask * 8 - 1) * 1024 + pz * 16, T_D + (w) * 1024); \
  } while (0)

  if (wave >= 4) {
    if (wave >= 6) {
      const int v = wave - 6;
      const u16* srcb = p.ckv + ((size_t)bA * SK + chunk * 4) * DC;
      const unsigned vo0 = g4 * 1024 + (l16 ^ ((g4 << 2) | v)) * 16, vo1 = vo0 ^ 32;
      BAR();
      lds_wait(L + L_SY + 16, 1);
      bool early = true;
      STAMP(3);
#pragma nounroll
      for (int blk = 0; blk < 4; ++blk) {
        if (blk == 3) lds_wait(L + L_SY + 12, 0x00010100);
#pragma unroll
        for (int n = 0; n < 16; ++n) {
          GLDS(srcb + ((2 * n + v + chunk) & 31) * (128 * DC) + blk * 128, (n & 1) ? vo1 : vo0, L_CKV + blk * 32768 + (n * 2 + v) * 1024);
          if (early) {
            VMWAIT(WIN0 - 1);
            if (__builtin_amdgcn_readfirstlane(*(volatile LAS unsigned*)(L + L_SY + 16)) & EMASK) early = false;
          } else VMWAIT(WIN - 1);
          if (n == ((WIN - 2) & 15) && blk > (WIN - 2) / 16) LBYTE(v, (1 << (blk - (WIN - 2) / 16)) - 1);
        }
      }
      STAMP(1);
      if (WIN > 17) { VMWAIT(16); LBYTE(v, 7); }
      VMWAIT(0); LBYTE(v, 15);
      STAMP(2);
      lds_wait(L + L_SY, 0x0808);
      VLOAD();
    } else {
      if (wave == 4) {
        TAGLOAD();
        BAR();
        VMWAIT(0);
        TAGSET();
        *(volatile LAS unsigned*)(L + L_SY + 24) = tag;
        WAIT_UNTIL(TQ);
        STAMP(1);
        int dm = 0;
        {
          unsigned fo = WS_QF + xg * RS + (lane >> 1) * 256 + (lane & 1) * 32;
#pragma nounroll
          for (unsigned spin = 0; spin < SPIN_CAP; ++spin) {
            const v4u f = GLOAD(fo), g = GLOAD(fo + 16);
            if ((f.x == tag) & (f.z == tag) & (g.x == tag) & (g.z == tag)) {
              dm = max(max((int)(f.y - t00), (int)(f.w - t00)), max((int)(g.y - t00), (int)(g.w - t00)));
              fo = OOB;
            }
            if (__all(fo == OOB)) break;
            __builtin_amdgcn_s_sleep(8);
            asm volatile("" ::: "memory");
          }
        }
        STAMP(2);
        PFR(p.ws, min(lane, 5) * (2u << 20) + (2u << 20));
        PFR(p.wuv + ((size_t)(sl * 64) * NH + hA) * HD, 0);
        PFR(p.wo + (size_t)hB * HD * DM, 0);
        PFR(p.wd, 0);
        PFR(p.out, 0);
        float df = (float)min(max(dm, 0), 150);
        df = fmaxf(df, dppm<0x121>(df)); df = fmaxf(df, dppm<0x122>(df)); df = fmaxf(df, dppm<0x124>(df)); df = fmaxf(df, dppm<0x128>(df));
        const unsigned tg = TG + (unsigned)__builtin_amdgcn_readfirstlane((int)xmax32(xmax16(df)));
        lds_wait(L + L_SY + 12, 0x00000101);
        WAIT_UNTIL(tg);
        LBYTE(21, 1);
      } else {
        VMWAIT(0);
        LBYTE(2, 1);
        STAMP(1);
        const unsigned xb = L_X + (l16 & 7) * 1024;
        const v4u ones = {0x3f803f80u, 0x3f803f80u, 0x3f803f80u, 0x3f803f80u};
        v4u xs[8];
#pragma unroll
        for (int t = 0; t < 8; ++t) xs[t] = *(const LAS v4u*)(L + xb + (((8 * t + 4 * (l16 >> 3) + g4) ^ (2 * (l16 & 7))) * 16));
        f32x4 G = {0.f, 0.f, 0.f, 0.f}, S = {0.f, 0.f, 0.f, 0.f};
#pragma unroll
        for (int t = 0; t < 8; ++t) { G = mfma16(xs[t], xs[t], G); S = mfma16(ones, xs[t], S); }
        float dg = (l16 & 2) ? ((l16 & 1) ? G[3] : G[2]) : ((l16 & 1) ? G[1] : G[0]);
        dg = g4 == (l16 >> 2) ? dg : 0.f;
        dg = xsum32(xsum16(dg));
        dg += dppm<0x128>(dg);
        const float s0 = S[0];
        const float mean = (s0 + dppm<0x128>(s0)) * (1.f / DM);
        const float rstd = __builtin_amdgcn_rsqf(fmaxf(dg * (1.f / DM) - mean * mean, 0.f) + 1e-5f);
        if (lane < 8) *(LAS v2u*)(L + L_LN + lane * 8) = (v2u){f2u(mean), f2u(rstd)};
        LGKM0();
        LBYTE(2, 3);
        STAMP(2);
        lds_wait(L + L_SY + 20, 0x0100);
      }
      {
        const unsigned qo = WS_QA + bA * RS + w4 * 8192 + lane * 16;
        const unsigned qd = L_S + w4 * 8192 + g4 * 256;
        STAMP(3);
        unsigned todo = 3;
#pragma nounroll
        for (unsigned spin = 0; spin < SPIN_CAP; ++spin) {
          v4u qa[16];
          const unsigned q0 = (todo & 1) ? qo : OOB, q1 = (todo & 2) ? qo + 16384 : OOB;
#pragma unroll
          for (int i = 0; i < 16; ++i) qa[i] = GLOAD((i < 8 ? q0 : q1) + (i & 7) * 1024);
#pragma unroll
          for (int u = 0; u < 2; ++u) {
            if ((todo >> u) & 1) {
              unsigned m = 0;
#pragma unroll
              for (int i = 8 * u; i < 8 * u + 8; ++i) {
                m = max(max(m, qa[i].x), qa[i].y);
                m = max(max(m, qa[i].z), qa[i].w);
                if (!(i == 8 && wave == 5 && lane >= 14 && lane < 16))
                  *(volatile LAS v4u*)(L + qd + u * 16384 + (i & 7) * 1024 + ((l16 ^ ((g4 << 2) | (i & 3))) * 16)) = qa[i];
              }
              if (!__any(m == ~0u)) { LBYTE(wave + u * 2, 1); todo &= ~(1u << u); }
            }
          }
          if (!todo) break;
          __builtin_amdgcn_s_sleep(4);
          asm volatile("" ::: "memory");
        }
        STAMP(13);
      }
      lds_wait(L + L_SY, 0x0404);
      VLOAD();
      lds_wait64(L + L_SY, 0x0101010100000808ull);
      if (wave == 4) {
        const u16* sv = p.wuv + ((size_t)(sl * 64) * NH + hA) * HD;
        const u16* so = p.wo + ((size_t)hB * HD) * DM + sl * 64;
        PFR(sv, l32 * 8192 + hf * 128);
        PFR(sv + 32 * NH * HD, l32 * 8192 + hf * 128);
        const u16* sw = p.wuv + ((size_t)(sl * 64) * NH + hO) * HD;
        PFR(sw, l32 * 8192 + hf * 128);
        PFR(sw + 32 * NH * HD, l32 * 8192 + hf * 128);
        if (OV == 0 || (PM & 1)) PFR(so, lane * 1024);
        if (OV == 0 || (PM & 2)) PFR(so + 64 * DM, lane * 1024);
        if (OV == 0 || (PM & 4)) PFR(p.wd + (size_t)task0 * 8 * DM + sl * 64, min(lane, ntask * 8 - 1) * 1024);
      }
    }
  } else {
    if (wave == 0 || (wave == 1 && wg < NFFN)) {
      const unsigned st = wave ? L_WF : L_WQ;
      const int cg = (wg % 88) >> 2, mat = wg >= 88;
      const int kq1 = wave ? (wg & 3) : (sl & 3);
      if (wave) {
        const u16* src = (mat ? p.wu : p.wg) + (size_t)(kq1 * 128) * FF;
        const unsigned vo = (r8 * FF + min(cg * 64 + (int)pz * 8, FF - 8)) * 2;
        BAR();
        lds_wait(L + L_SY + 16, 0x0101);
#pragma unroll
        for (int i = 0; i < 16; ++i) GLDS(src + i * 8 * FF, vo, st + i * 1024);
      }
      lds_wait(L + L_SY, 0x010000);
      v4u xr[4];
#pragma unroll
      for (int s = 0; s < 4; ++s) {
        xr[s] = *(const LAS v4u*)(L + L_X + (l16 & 7) * 1024 + (((16 * kq1 + 4 * s + g4) ^ (2 * (l16 & 7))) * 16));
        if (l16 >= 8) xr[s] = (v4u){0x3f803f80u, 0x3f803f80u, 0x3f803f80u, 0x3f803f80u};
      }
      f32x4 acc[4];
      {
#pragma unroll
        for (int nt = 0; nt < 4; ++nt) acc[nt] = (f32x4){0.f, 0.f, 0.f, 0.f};
#pragma unroll
        for (int s = 0; s < 4; ++s) {
          if (s == 0) VMWAIT(12); else if (s == 1) VMWAIT(8); else if (s == 2) VMWAIT(4); else VMWAIT(0);
          if (s == 3) STAMP(1);
          v4u af[4];
#pragma unroll
          for (int nt = 0; nt < 4; ++nt) af[nt] = FRAG(st, nt, s);
#pragma unroll
          for (int nt = 0; nt < 4; ++nt) acc[nt] = mfma16(af[nt], xr[s], acc[nt]);
        }
      }
      TAGGET();
      lds_wait(L + L_SY, 0x020000);
      const v2u ln = *(const LAS v2u*)(L + L_LN + (l16 & 7) * 8);
      const float mn = u2f(ln.x), rs = u2f(ln.y);
      LGKM0();
      LBYTE(12 + wave, 1);
#pragma unroll
      for (int nt = 0; nt < 4; ++nt)
#pragma unroll
        for (int j = 0; j < 4; ++j) acc[nt][j] = (acc[nt][j] - mn * dppm<0x158>(acc[nt][j])) * rs;
      const unsigned ra = wave ? WS_GU + (cg * 8 + 2 * (l16 >> 3) + (g4 >> 1)) * 2048 + mat * 1024 + kq1 * 256 + (g4 & 1) * 128 + (l16 & 7) * 16
                               : WS_QP + xg * RS + (hA & 3) * 16384 + (((sl >> 2) * 4 + (kq1 >> 1)) * 2 + (g4 & 1)) * 1024 +
                                     (2 * (l16 >> 3) + (g4 >> 1)) * 256 + ((kq1 & 1) * 8 + (l16 & 7)) * 16;
#pragma unroll
      for (int i = 0; i < 2; ++i) {
        const unsigned a = pk2(acc[2 * i][0], acc[2 * i][1]), b = pk2(acc[2 * i][2], acc[2 * i][3]);
        const unsigned c = dppu<0x128>(pk2(acc[2 * i + 1][0], acc[2 * i + 1][1])), d = dppu<0x128>(pk2(acc[2 * i + 1][2], acc[2 * i + 1][3]));
        __builtin_amdgcn_raw_buffer_store_b128((v4u){l16 < 8 ? a : c, tag, l16 < 8 ? b : d, tag}, wsr, ra + i * (wave ? 8192 : 4096), 0, AUX_SC1);
      }
      asm volatile("" ::: "memory");
      LBYTE(17, 1);
      STAMP(2);
    } else if (wave == 1) {
      BAR();
      LBYTE(13, 1);
    } else if (wave == 2) {
      {
        const u16* src = p.wuk + ((size_t)(sl * 64) * NH + hA) * HD;
        const unsigned vo = g4 * 8192 + (l16 ^ g4) * 16;
        BAR();
#pragma unroll
        for (int n = 0; n < 16; ++n) GLDS(src + n * 4 * NH * HD, vo ^ ((n & 3) * 64), L_WU + n * 1024);
        LBYTE(16, 1);
      }
      VMWAIT(0);
      STAMP(1);
      v4u wf[16];
#pragma unroll
      for (int n = 0; n < 16; ++n) wf[n] = *(const LAS v4u*)(L + L_WU + ((n >> 2) * 16 + l16) * 256 + (((4 * (n & 3) + g4) ^ l16) * 16));
      LGKM0();
      LBYTE(14, 1);
      TAGGET();
      WAIT_UNTIL(TP);
      v4u qr[16];
      {
        bool todo = true;
#pragma nounroll
        for (unsigned spin = 0; spin < SPIN_CAP; ++spin) {
          if (todo) {
            GSTART(16, qr, WS_QP + xg * RS + (hA & 3) * 16384 + i * 1024 + lane * 16);
            unsigned bad = 0;
#pragma unroll
            for (int i = 0; i < 16; ++i) bad |= (qr[i].y ^ tag) | (qr[i].w ^ tag);
            todo = bad != 0;
          }
          if (!__any(todo)) break;
          __builtin_amdgcn_s_sleep(4);
          asm volatile("" ::: "memory");
        }
      }
      STAMP(2);
      const float scale = 0.08838834764831845f * 1.4426950408889634f;
      unsigned yp[2][2];
#pragma unroll
      for (int cp = 0; cp < 2; ++cp) {
        f32x4 a0 = {0.f, 0.f, 0.f, 0.f}, a1 = {0.f, 0.f, 0.f, 0.f};
#pragma unroll
        for (int k = 0; k < 8; ++k) {
          const v4u bq = {qr[2 * k].x, qr[2 * k].z, qr[2 * k + 1].x, qr[2 * k + 1].z};
          a0 = mfma16(wf[cp * 8 + (k >> 1)], bq, a0);
          a1 = mfma16(wf[cp * 8 + 4 + (k >> 1)], bq, a1);
        }
        float y[4];
#pragma unroll
        for (int j = 0; j < 4; ++j) {
          const float keep = l16 < 8 ? a0[j] : a1[j], give = l16 < 8 ? a1[j] : a0[j];
          y[j] = (keep + dppm<0x128>(give)) * scale;
        }
        yp[cp][0] = pk2(y[0], y[1]); yp[cp][1] = pk2(y[2], y[3]);
      }
      lds_wait(L + L_SY + 20, 0x010000);
      {
        auto s0 = __builtin_amdgcn_permlane16_swap(yp[0][0], yp[1][0], false, false);
        auto s1 = __builtin_amdgcn_permlane16_swap(yp[0][1], yp[1][1], false, false);
        __builtin_amdgcn_raw_buffer_store_b128((v4u){min(s0[0], ~1u), min(s1[0], ~1u), min(s0[1], ~1u), min(s1[1], ~1u)}, wsr,
                                               WS_QA + (l16 & 7) * RS + ((sl >> 1) * 32 + hA) * 256 + (sl & 1) * 128 +
                                                   (2 * (g4 & 1) + (l16 >> 3)) * 32 + (g4 >> 1) * 16, 0, AUX_SC1);
      }
      asm volatile("" ::: "memory");
      LBYTE(18, 1);
      STAMP(12);
    } else {
      __builtin_amdgcn_raw_buffer_store_b128((v4u){~0u, ~0u, ~0u, ~0u}, wsr, WS_QA + r8 * RS + ((sl >> 1) * 32 + hA) * 256 + (sl & 1) * 128 + q8 * 16, 0, AUX_SC1);
#pragma unroll
      for (int i = 0; i < 2; ++i)
        __builtin_amdgcn_raw_buffer_store_b128((v4u){~0u, ~0u, ~0u, ~0u}, wsr, WS_CX + ((hA * 8 + 4 * i + g4) * 8 + sl) * 256 + l16 * 16, 0, AUX_SC1);
      BAR();
      VMWAIT(0);
      LBYTE(22, 1);
      TAGGET();
      if (lane < 8) __builtin_amdgcn_raw_buffer_store_b64((v2u){tag, t00}, wsr, WS_QF + lane * RS + hA * 256 + sl * 8, 0, AUX_SC1);
      STAMP(12);
    }
    {
#pragma unroll
      for (int i = 0; i < 16; ++i) sacc[i] = 0.f;
      const unsigned tz = (hf ^ zl) * 16;
      const unsigned kb = L_CKV + (wave * 32 + l32) * 256 + tz;
      const unsigned ho = WS_QA + bA * RS + 3 * 8192 + 224 + hf * 16;
      v4u qa[8], qc[8], kf[32];
      unsigned ka[8];
#pragma unroll
      for (int s = 0; s < 8; ++s) ka[s] = kb ^ (s * 32);
      const unsigned qd = L_S - wave * 8192;
#define DSR(dst, addr, off) asm volatile("ds_read_b128 %0, %1 offset:%2" : "=v"(dst) : "v"(addr), "n"(off) : "memory")
#define DSRU(dst, addr, off) asm volatile("ds_read_b128 %0, %1 offset:%2" : "+v"(dst) : "v"(addr), "n"(off) : "memory")
#define KREAD(b) do { _Pragma("unroll") for (int s = 0; s < 8; ++s) DSR(kf[8 * (b) + s], ka[s] + ((b) >> 1) * 65536, ((b) & 1) * 32768); } while (0)
#define KREAD3() do { _Pragma("unroll") for (int s = 0; s < 8; ++s) DSRU(kf[24 + s], ka[s] + 65536, 32768); } while (0)
#define QREAD(q, b) do { _Pragma("unroll") for (int s = 0; s < 8; ++s) DSR(q[s], ka[s] + qd, (b) * 8192); } while (0)
#define LGW(n, x) asm volatile("s_waitcnt lgkmcnt(%1)" : "+v"(x) : "n"(n) : "memory")
#define QKB(q, b) do { _Pragma("unroll") for (int s = 0; s < 8; ++s) { LGW(15 - s, q[s]); sacc = mfma32(kf[8 * (b) + s], q[s], sacc); __builtin_amdgcn_sched_barrier(0); } } while (0)
      lds_wait(L + L_SY, 0x0101);
      KREAD(0);
      lds_wait(L + L_SY, 0x0202);
      KREAD(1);
      lds_wait(L + L_SY + 20, 0x0100);
      v4u hole = GLOAD(ho);
      lds_wait(L + L_SY, 0x0404);
      KREAD(2);
#pragma unroll
      for (int s = 0; s < 8; ++s) asm volatile("" : "=v"(kf[24 + s]));
      unsigned long long have = 0;
#pragma nounroll
      for (unsigned spin = 0; spin < (SPIN_CAP << 4); ++spin) {
        have = lds_peek64(L + L_SY);
        if ((have & 0x0808) == 0x0808 || (unsigned)(have >> 32) == 0x01010101u) break;
        __builtin_amdgcn_s_sleep(1);
      }
      asm volatile("" ::: "memory");
      const bool k3 = (have & 0x0808) == 0x0808;
      if (k3) {
        KREAD3();
        lds_wait(L + L_SY + 4, 0x01010101);
      }
      STAMP(4);
      QREAD(qa, 0);
      QREAD(qc, 1);
      QKB(qa, 0);
      QREAD(qa, 2);
      QKB(qc, 1);
      QREAD(qc, 3);
      QKB(qa, 2);
      LBYTE(8 + wave, 1);
#pragma nounroll
      for (unsigned spin = 0; spin < SPIN_CAP; ++spin) {
        if (__all(max(max(hole.x, hole.y), max(hole.z, hole.w)) != ~0u)) break;
        hole = GLOAD(ho);
      }
#pragma unroll
      for (int s = 0; s < 8; ++s) LGW(0, qc[s]);
      if (l32 == 0) qc[7] = hole;
      VLOAD();
      if (!k3) {
        lds_wait(L + L_SY, 0x0808);
        KREAD3();
        LGKM0();
      }
#pragma unroll
      for (int s = 0; s < 8; ++s) asm volatile("" : "+v"(kf[24 + s]));
#pragma unroll
      for (int s = 0; s < 8; ++s) sacc = mfma32(kf[24 + s], qc[s], sacc);
      lds_wait(L + L_SY + 8, 0x01010101);
      STAMP(5);
      float mx = __builtin_ceilf(sacc[0]);
#pragma unroll
      for (int i = 1; i < 15; i += 2) mx = max3(mx, sacc[i], sacc[i + 1]);
      mx = max3(mx, sacc[15], sacc[15]);
      *(LAS float*)(L + L_ST + l32 * 32 + wave * 8 + hf * 4) = __builtin_ceilf(mx);
    }
  }
  BAR();
  float M = 0.f;
  if (wave < 4) {
    const f32x4 m0 = *(const LAS f32x4*)(L + L_ST + l32 * 32), m1 = *(const LAS f32x4*)(L + L_ST + l32 * 32 + 16);
    M = max3(max3(m0[0], m0[1], m0[2]), max3(m0[3], m1[0], m1[1]), max3(m1[2], m1[3], m1[3]));
    unsigned pp[8];
#pragma unroll
    for (int q = 0; q < 4; ++q) {
      pp[2 * q] = pk2(__builtin_amdgcn_exp2f(sacc[4 * q] - M), __builtin_amdgcn_exp2f(sacc[4 * q + 1] - M));
      pp[2 * q + 1] = pk2(__builtin_amdgcn_exp2f(sacc[4 * q + 2] - M), __builtin_amdgcn_exp2f(sacc[4 * q + 3] - M));
      *(LAS v2u*)(L + L_P + l32 * 256 + (((wave * 4 + q) ^ l16) * 16) + hf * 8) = (v2u){pp[2 * q], pp[2 * q + 1]};
    }
    float ls = 0.f;
#pragma unroll
    for (int q = 0; q < 8; ++q) ls = dot_ones(pp[q], ls);
    *(LAS float*)(L + L_ST + 1024 + l32 * 32 + wave * 8 + hf * 4) = ls;
    STAMP(3);
  }
  BAR();
  if (wave < 4 || wave > 5) STAMP(13);
  {
    v4u pf[8];
    {
      const unsigned pbs = L_P + l16 * 256 + (g4 ^ l16) * 16;
#pragma unroll
      for (int i = 0; i < 8; ++i) pf[i] = *(const LAS v4u*)(L + (pbs ^ ((i & 3) * 64)) + (i >> 2) * 4096);
    }
    tagv = *(const LAS unsigned*)(L + L_SY + 24);
    f32x4 o[2][4];
#pragma unroll
    for (int i = 0; i < 8; ++i) o[i >> 2][i & 3] = (f32x4){0.f, 0.f, 0.f, 0.f};
    const unsigned pbase = WS_PART + ((((bA * 32 + 4 * g4) * 8 + wave) * 32 + (chunk ^ (wave * 4 + g4))) * 16 + l16) * 8;
#define PSTORE(hh, j) __builtin_amdgcn_raw_buffer_store_b64((v2u){pk2(o[hh][0][j], o[hh][1][j]), pk2(o[hh][2][j], o[hh][3][j])}, wsr, pbase + (16 * (hh) + (j)) * (8 * 32 * 128), 0, AUX_SC1)
#pragma unroll
    for (int i = 0; i < 16; ++i) o[0][i & 3] = mfma16(pf[i >> 2], vf[i], o[0][i & 3]);
#pragma unroll
    for (int s = 0; s < 4; ++s) {
      __builtin_amdgcn_sched_barrier(0);
#pragma unroll
      for (int t = 0; t < 4; ++t) o[1][t] = mfma16(pf[4 + s], vf[4 * s + t], o[1][t]);
      __builtin_amdgcn_sched_barrier(0);
      PSTORE(0, s);
    }
    __builtin_amdgcn_sched_barrier(0);
    STAMP(6);
#pragma unroll
    for (int j = 0; j < 4; ++j) PSTORE(1, j);
    if (wave == 3) {
      const f32x4 l0 = *(const LAS f32x4*)(L + L_ST + 1024 + l32 * 32), l1 = *(const LAS f32x4*)(L + L_ST + 1024 + l32 * 32 + 16);
      const float lt = ((l0[0] + l0[1]) + (l0[2] + l0[3])) + ((l1[0] + l1[1]) + (l1[2] + l1[3]));
      if (lane < 32)
        __builtin_amdgcn_raw_buffer_store_b64((v2u){f2u(M), f2u(lt)}, wsr, WS_FL + ((((lane >> 2) * 8 + bA) * 32 + chunk) * 4 + (lane & 3)) * 8, 0, AUX_SC1);
    }
  }
  tag = __builtin_amdgcn_readfirstlane(tagv);
  STAMP(7);
  v4u wv[4];
#define ALOAD(dst, addr) asm volatile("buffer_load_dwordx4 %0, %1, %2, 0 offen sc1" : "=v"(dst) : "v"(addr), "s"(wsr) : "memory")
  {
    const int bC = wave == xg ? bO : wave, hC = wave == xg ? hO : hA;
    v4u gr[2];
    const unsigned gq = wave < ntask && NFFN ? WS_GU + (task0 + wave) * 2048 + lane * 16 : OOB;
    unsigned fa = lane < 8 && lane != xg ? WS_PF + lane * RS + (bA * (32 / FPL) + (unsigned)chunk / FPL) * 256 + ((unsigned)chunk % FPL) * 4 : OOB;
    unsigned fo = lane < 8 ? WS_PF + xg * RS + (bC * (32 / FPL) + lane / (FPL / 4)) * 256 + (lane % (FPL / 4)) * 16 : OOB;
    asm volatile("" : "+v"(fa), "+v"(fo));
    VMWAIT(0);
    if ((lds_ticket(L + L_SY + 28, lane) & 255) == 7) __builtin_amdgcn_raw_buffer_store_b32(tag, wsr, fa, 0, AUX_SC1);
    STAMP(8);
    KEEPV(tpf);
    TAILV();
    if (OV == 0) TAILO(wave);
    ALOAD(gr[0], gq);
    ALOAD(gr[1], gq + 1024);
    {
      v4u f;
      ALOAD(f, fo);
      VMWAIT(1);
      asm volatile("" : "+v"(gr[0]), "+v"(gr[1]));
      if (lane == 0) __hip_atomic_fetch_add((LAS unsigned*)(L + L_SY + 28), 0x100u, __ATOMIC_RELAXED, __HIP_MEMORY_SCOPE_WORKGROUP);
      if (wave < ntask && NFFN) {
#pragma nounroll
        for (unsigned spin = 0; spin < SPIN_CAP; ++spin) {
          if (__all((gr[0].y == tag) & (gr[0].w == tag) & (gr[1].y == tag) & (gr[1].w == tag))) break;
          ALOAD(gr[0], gq);
          ALOAD(gr[1], gq + 1024);
          VMWAIT(0);
          asm volatile("" : "+v"(gr[0]), "+v"(gr[1]));
        }
        const unsigned e = (l16 >> 2) + 4 * (l16 & 1);
        const unsigned v = (l16 & 2) ? 0u : ((e & 1) ? 0x3f800000u : 0x00003f80u);
        f32x4 a = {0.f, 0.f, 0.f, 0.f};
        a = mfma16((v4u){(e >> 1) == 0 ? v : 0u, (e >> 1) == 1 ? v : 0u, (e >> 1) == 2 ? v : 0u, (e >> 1) == 3 ? v : 0u}, (v4u){gr[0].x, gr[0].z, gr[1].x, gr[1].z}, a);
        const float gv = a[0], uv = a[1];
        const float yy = 0.7978845608028654f * (gv + 0.044715f * gv * gv * gv);
        const float th = 1.f - 2.f / (__expf(2.f * yy) + 1.f);
        *(LAS u16*)(L + L_HM + wave * 128 + (l16 & 7) * 16 + ((l16 >> 3) * 4 + g4) * 2) = (u16)pk2(0.5f * gv * (1.f + th) * uv, 0.f);
      }
#pragma nounroll
      for (unsigned spin = 0; spin < SPIN_CAP; ++spin) {
        VMWAIT(0);
        asm volatile("" : "+v"(f));
        if (OK_FLG(f)) fo = OOB;
        if (__all(fo == OOB)) break;
        if (QM && spin == QS && ((QM >> wave) & 1)) {
          const u16* so = p.wo + ((size_t)hB * HD + wave * 64) * DM + sl * 64;
          if (wave < 2) PFR(so, lane * 1024);
          else PFR(p.wd + (size_t)task0 * 8 * DM + sl * 64, min(lane, ntask * 8 - 1) * 1024);
        }
        __builtin_amdgcn_s_sleep(PSLP);
        ALOAD(f, fo);
      }
      asm volatile("" ::: "memory");
    }
    KEEPV(tpf);
    STAMP(14);
    v2u pr[9];
    const unsigned po = WS_PART + ((((bC * 32 + hC) * 8 + sl) * 32) * 16 + lane) * 8;
    pr[8] = __builtin_amdgcn_raw_buffer_load_b64(wsr, WS_FL + ((((hC >> 2) * 8 + bC) * 32 + l32) * 4 + (hA & 3)) * 8, 0, AUX_SC1);
#pragma unroll
    for (int i = 0; i < 8; ++i) pr[i] = __builtin_amdgcn_raw_buffer_load_b64(wsr, po + i * 512, 0, AUX_SC1);
    if (OV == 5 && lane == 0) __hip_atomic_fetch_add((LAS unsigned*)(L + L_SY + 28), 0x1000000u, __ATOMIC_RELAXED, __HIP_MEMORY_SCOPE_WORKGROUP);
    lds_wait(L + L_SY + 28, 0x800);
#pragma unroll
    for (int i = 0; i < 4; ++i) { wv[i] = FRAG((i & 2 ? T_W : T_V) + (wave >> 2) * 8192, wave & 3, i & 1); asm volatile("" : "+v"(wv[i])); }
    const float mj = u2f(pr[8].x);
    float M = mj;
    M = fmaxf(M, dppm<0x121>(M)); M = fmaxf(M, dppm<0x122>(M)); M = fmaxf(M, dppm<0x124>(M)); M = fmaxf(M, dppm<0x128>(M));
    M = xmax16(M);
    const float wj = bfhi(f2u(__builtin_amdgcn_exp2f(mj - M)));
    float Ls = wj * u2f(pr[8].y);
    Ls += dppm<0x121>(Ls); Ls += dppm<0x122>(Ls); Ls += dppm<0x124>(Ls); Ls += dppm<0x128>(Ls);
    Ls = xsum16(Ls);
    const float inv = __builtin_amdgcn_rcpf(Ls);
    const unsigned e4 = l16 & 3;
    const unsigned sh = (e4 & 1) ? 0u : 16u, k0 = e4 < 2 ? ~0u : 0u;
    unsigned wa[16];
#pragma unroll
    for (int u = 0; u < 8; ++u) {
      const unsigned w = (unsigned)__builtin_amdgcn_ds_bpermute(((g4 + 4 * u) ^ (sl * 4 + ((hC >> 2) & 3))) * 4, f2u(wj)) >> sh;
      wa[2 * u] = w & k0; wa[2 * u + 1] = w & ~k0;
    }
    f32x4 acc = {0.f, 0.f, 0.f, 0.f};
#pragma unroll
    for (int u = 0; u < 4; ++u)
      acc = mfma16((v4u){wa[4 * u], wa[4 * u + 1], wa[4 * u + 2], wa[4 * u + 3]}, (v4u){pr[2 * u].x, pr[2 * u].y, pr[2 * u + 1].x, pr[2 * u + 1].y}, acc);
    if (lane < 16) *(LAS v2u*)(L + L_CL + wave * 144 + lane * 8) = (v2u){pk2(acc[0] * inv, acc[1] * inv), pk2(acc[2] * inv, acc[3] * inv)};
    if (OV == 5 && wave < 4) { lds_wait(L + L_SY + 28, 0x8000000); if (OD) __builtin_amdgcn_s_sleep(OD); TAILO(wave); TAILO(wave + 4); }
    BAR();
  }
  v4u ao[4];
  f32x4 accE = {0.f, 0.f, 0.f, 0.f};
  const unsigned one = (l16 & 1) ? 0x3f800000u : 0x00003f80u;
  const unsigned s0 = (l16 & 2) ? 0u : one, s1 = (l16 & 2) ? one : 0u;
  {
    const v4u b0 = *(const LAS v4u*)(L + L_CL + (l16 & 7) * 144 + g4 * 16), b1 = *(const LAS v4u*)(L + L_CL + (l16 & 7) * 144 + 64 + g4 * 16);
    f32x4 acc = {0.f, 0.f, 0.f, 0.f};
    acc = mfma16(wv[0], b0, acc);
    acc = mfma16(wv[1], b1, acc);
    f32x4 acw = {0.f, 0.f, 0.f, 0.f};
    acw = mfma16(wv[2], b0, acw);
    acw = mfma16(wv[3], b1, acw);
    if (l16 == xg) acc = acw;
    if (l16 < 8)
      __builtin_amdgcn_raw_buffer_store_b64((v2u){min(pk2(acc[0], acc[1]), ~1u), min(pk2(acc[2], acc[3]), ~1u)}, wsr,
                                            WS_CX + (((((l16 == xg ? hO : hA) * 8 + wave) * 8 + sl) * 8 + (l16 == xg ? bO : l16)) * 4 + g4) * 8, 0, AUX_SC1);
    STAMP(9);
    if (OV == 4 && wave < 4 && !NOO) { TAILO(wave); TAILO(wave + 4); }
#define OFRAG() do { \
      _Pragma("unroll") for (int ks = 0; ks < 4; ++ks) ao[ks] = FRAG(T_O, wave, ks); \
      const v4u hm0 = *(const LAS v4u*)(L + L_HM + g4 * 128 + (l16 & 7) * 16), hm1 = *(const LAS v4u*)(L + L_HM + 512 + g4 * 128 + (l16 & 7) * 16); \
      accE = mfma16(FRAG(T_D, wave, 0), hm0, accE); \
      accE = mfma16(FRAG(T_D, wave, 1), hon ? hm1 : (v4u){0u, 0u, 0u, 0u}, accE); \
    } while (0)
    if (wave < 4) {
      VMWAIT(0);
      if (lane == 0) __hip_atomic_fetch_add((LAS unsigned*)(L + L_SY + 28), 0x10000u, __ATOMIC_RELAXED, __HIP_MEMORY_SCOPE_WORKGROUP);
      lds_wait(L + L_SY + 28, 0x40000);
      OFRAG();
      STAMP(10);
    } else {
      v4u cr[4];
      const unsigned co = WS_CX + (hB * 8 + 2 * w4) * 2048 + lane * 16;
      if (CDELAY) __builtin_amdgcn_s_sleep(CDELAY);
#pragma nounroll
      for (unsigned spin = 0; spin < SPIN_CAP; ++spin) {
        unsigned m = 0;
#pragma unroll
        for (int i = 0; i < 4; ++i) cr[i] = GLOAD(co + i * 1024);
#pragma unroll
        for (int i = 0; i < 4; ++i) m = max(max(max(m, cr[i].x), cr[i].y), max(cr[i].z, cr[i].w));
        if (!__any(m == ~0u)) break;
        __builtin_amdgcn_s_sleep(1);
      }
      STAMP(10);
      const v4u se = {(l16 >> 1) == 0 ? one : 0u, (l16 >> 1) == 1 ? one : 0u, (l16 >> 1) == 2 ? one : 0u, (l16 >> 1) == 3 ? one : 0u};
#pragma unroll
      for (int u = 0; u < 2; ++u) {
        f32x4 a = {0.f, 0.f, 0.f, 0.f};
        a = mfma16(se, cr[2 * u], a);
        a = mfma16(se, cr[2 * u + 1], a);
        if (lane < 32) *(LAS v2u*)(L + L_CX + (l16 >> 1) * 272 + (2 * w4 + u) * 32 + (l16 & 1) * 16 + g4 * 8) = (v2u){pk2(a[0], a[1]), pk2(a[2], a[3])};
      }
    }
    BAR();
  }
  if (wave < 4) {
#pragma unroll
    for (int ks = 0; ks < 4; ++ks) accE = mfma16(ao[ks], *(const LAS v4u*)(L + L_CX + (l16 & 7) * 272 + ks * 64 + g4 * 16), accE);
    if (l16 < 8)
      __builtin_amdgcn_raw_buffer_store_b128((v4u){pk2(accE[0], accE[1]), tag, pk2(accE[2], accE[3]), tag}, wsr,
                                             WS_OG + ((((sl * 4 + wave) * 32 + hB) * 8 + l16) * 4 + g4) * 16, 0, AUX_SC1);
    STAMP(11);
  } else if (wave == 4) {
    const unsigned oo = WS_OG + ((((wg & 31) * 32 + (lane >> 2)) * 8 + (wg >> 5)) * 4 + (lane & 3)) * 16;
    const v2u xw = *(const GAS v2u*)(p.x + wg * 16 + (lane & 3) * 4);
    v4u r0, r1;
    if (ODELAY) __builtin_amdgcn_s_sleep(ODELAY);
#pragma nounroll
    for (unsigned spin = 0; spin < SPIN_CAP; ++spin) {
      r0 = GLOAD(oo);
      r1 = GLOAD(oo + 16 * 512);
      if (__all((r0.y == tag) & (r0.w == tag) & (r1.y == tag) & (r1.w == tag))) break;
      __builtin_amdgcn_s_sleep(1);
    }
    f32x4 acc = {0.f, 0.f, 0.f, 0.f};
    acc = mfma16((v4u){s0, s1, s0, s1}, (v4u){r0.x, r0.z, r1.x, r1.z}, acc);
#pragma unroll
    for (int j = 0; j < 4; ++j) { acc[j] += dppm<0x124>(acc[j]); acc[j] += dppm<0x128>(acc[j]); }
    if (lane < 4)
      *(GAS v2u*)(p.out + wg * 16 + lane * 4) = (v2u){pk2(bflo(xw.x) + acc[0], bfhi(xw.x) + acc[1]), pk2(bflo(xw.y) + acc[2], bfhi(xw.y) + acc[3])};
    if (wg == 0 && lane == 0) __builtin_amdgcn_raw_buffer_store_b32(tag, wsr, WS_EPOCH, 0, AUX_SC1);
    STAMP(11);
  }
}

extern "C" void kernel_launch(void* const* d_in, const int* in_sizes, int n_in, void* d_out, int out_size, void* d_ws, size_t ws_size,
                              hipStream_t stream) {
  Params p;
  p.x = (const u16*)d_in[0]; p.ckv = (const u16*)d_in[1]; p.wq = (const u16*)d_in[2]; p.wuk = (const u16*)d_in[3];
  p.wuv = (const u16*)d_in[4]; p.wo = (const u16*)d_in[5]; p.wg = (const u16*)d_in[7]; p.wu = (const u16*)d_in[8]; p.wd = (const u16*)d_in[9];
  p.out = (u16*)d_out; p.ws = (unsigned char*)d_ws;
  (void)hipFuncSetAttribute((const void*)fused_kernel, hipFuncAttributeMaxDynamicSharedMemorySize, LDS_BYTES);
  fused_kernel<<<NWG, NTHR, LDS_BYTES, stream>>>(p);
}
```

```cpp
#include <hip/hip_runtime.h>
#include <hip/hip_bf16.h>
#include <stdint.h>

#define STAMPS 0
#define CODE_LINES 166
#define WIN 14
#define CDELAY 8
#define ODELAY 8
#define NFFN 176
#define WIN0 6
#define EMASK 0x010000
#define PSLP 1
#define TF 0
#define FPL 32
#define TP 310
#define TQ 310
#define OV 5
#define PM 0
#define QS 0
#define CXN 8
#define NOO 0
#define OD 0
#define QM 0
#define PROBE 1
#define W3 1
#define PDLY 0
#define PTAB 0x2a763198541a7631ull
#define TG 560

typedef unsigned v4u __attribute__((ext_vector_type(4)));
typedef unsigned v2u __attribute__((ext_vector_type(2)));
typedef float f32x2 __attribute__((ext_vector_type(2)));
typedef float f32x4 __attribute__((ext_vector_type(4)));
typedef float f32x16 __attribute__((ext_vector_type(16)));
typedef short bf16x8 __attribute__((ext_vector_type(8)));
typedef short s16x4 __attribute__((ext_vector_type(4)));
typedef unsigned short u16x2 __attribute__((ext_vector_type(2)));
typedef __bf16 bf16x2 __attribute__((ext_vector_type(2)));
typedef unsigned short u16;
#define LAS __attribute__((address_space(3)))
#define GAS __attribute__((address_space(1)))

constexpr int NB = 8, NH = 32, SK = 4096, HD = 128, DM = 512, DC = 512, FF = 1376;
constexpr int NWG = 256, NTHR = 512;
constexpr unsigned WS_EPOCH = 0;
#define RS 262144
constexpr unsigned WS_QF = 4096;
constexpr unsigned WS_PF = WS_QF + 8192;
constexpr unsigned WS_QA = WS_PF + 8192;
constexpr unsigned WS_QP = WS_QA + 32768;
static_assert(WS_QP + 65536 <= RS, "ws map");
constexpr unsigned WS_GU = 8 * RS;
constexpr unsigned WS_FL = WS_GU + 176 * 2048;
constexpr unsigned WS_CX = WS_FL + 8 * 8 * 32 * 4 * 16;
constexpr unsigned WS_OG = WS_CX + 32 * 8 * 8 * 8 * 4 * 16;
constexpr unsigned WS_PART = 6u << 20;
constexpr unsigned WS_END = WS_PART + (8u << 20);
constexpr unsigned WS_STAMP = 32u << 20;
static_assert(WS_OG + 32 * 32 * 8 * 4 * 16 <= WS_PART, "ws map");
constexpr unsigned L_CKV = 0;
constexpr unsigned L_S = 128 * 1024;
constexpr unsigned L_SY = L_S + 24576 + 224;
constexpr unsigned L_WQ = L_S, L_WU = 98304, L_WF = 114688;
constexpr unsigned L_X = L_S + 16384;
constexpr unsigned L_LN = L_S + 25600;
constexpr unsigned L_DUMP = L_S + 26624;
constexpr unsigned T_O = 0, T_V = 16384, T_D = 32768, T_W = 49152;
constexpr unsigned L_P = L_S;
constexpr unsigned L_ST = L_S + 8192;
constexpr unsigned L_CL = L_S + 16384;
constexpr unsigned L_CX = L_S + 16384 + 2048;
constexpr unsigned L_HM = L_S + 16384 + 4608;
constexpr unsigned LDS_BYTES = 160 * 1024;
constexpr int AUX_NT = 2, AUX_SC1 = 16;
constexpr unsigned SPIN_CAP = 1u << 18;
constexpr unsigned OOB = 0x80000000u;

struct Params {
  const u16 *x, *ckv, *wq, *wuk, *wuv, *wo, *wg, *wu, *wd;
  u16* out;
  unsigned char* ws;
};

__device__ __forceinline__ float u2f(unsigned u) { return __uint_as_float(u); }
__device__ __forceinline__ unsigned f2u(float f) { return __float_as_uint(f); }
__device__ __forceinline__ float bflo(unsigned w) { return u2f(w << 16); }
__device__ __forceinline__ float bfhi(unsigned w) { return u2f(w & 0xffff0000u); }
__device__ __forceinline__ unsigned pk2(float lo, float hi) {
  f32x2 f = {lo, hi};
  return __builtin_bit_cast(unsigned, __builtin_convertvector(f, bf16x2));
}
__device__ __forceinline__ float dot_ones(unsigned a, float c) {
  return __builtin_amdgcn_fdot2_f32_bf16(__builtin_bit_cast(bf16x2, a), __builtin_bit_cast(bf16x2, 0x3f803f80u), c, false);
}
__device__ __forceinline__ unsigned pksubsat(unsigned a, unsigned b) {
  return __builtin_bit_cast(unsigned, __builtin_elementwise_sub_sat(__builtin_bit_cast(u16x2, a), __builtin_bit_cast(u16x2, b)));
}
__device__ __forceinline__ f32x4 mfma16(v4u a, v4u b, f32x4 c) {
  return __builtin_amdgcn_mfma_f32_16x16x32_bf16(__builtin_bit_cast(bf16x8, a), __builtin_bit_cast(bf16x8, b), c, 0, 0, 0);
}
__device__ __forceinline__ f32x16 mfma32(v4u a, v4u b, f32x16 c) {
  return __builtin_amdgcn_mfma_f32_32x32x16_bf16(__builtin_bit_cast(bf16x8, a), __builtin_bit_cast(bf16x8, b), c, 0, 0, 0);
}
__device__ __forceinline__ float max3(float a, float b, float c) {
  float d;
  asm("v_max3_f32 %0, %1, %2, %3" : "=v"(d) : "v"(a), "v"(b), "v"(c));
  return d;
}
__device__ __forceinline__ unsigned swz(unsigned row) { return ((row & 3) << 2) | ((row >> 2) & 3); }
#define BAR() do { asm volatile("s_waitcnt lgkmcnt(0)" ::: "memory"); __builtin_amdgcn_s_barrier(); asm volatile("" ::: "memory"); } while (0)
#define VMWAIT(n) do { asm volatile("" ::: "memory"); __builtin_amdgcn_s_waitcnt(0x0f70 | ((n) & 15) | (((n) >> 4) << 14)); asm volatile("" ::: "memory"); } while (0)
#define LGKM0() asm volatile("s_waitcnt lgkmcnt(0)" ::: "memory")
#define GLDS_(OP, sbase, voff, dst) asm volatile("s_mov_b32 m0, %2\n\ts_nop 0\n\t" OP :: "v"((unsigned)(voff)), "s"((const void*)(sbase)), "s"((unsigned)(dst)) : "memory", "m0")
#define GLDS(sbase, voff, dst) GLDS_("global_load_lds_dwordx4 %0, %1 nt", sbase, voff, dst)
#define GLDSX(sbase, voff, dst) GLDS_("global_load_lds_dwordx4 %0, %1", sbase, voff, dst)
#define GLDS4(sbase, voff, dst) GLDS_("global_load_lds_dword %0, %1 nt", sbase, voff, dst)
#define PFR(sbase, voff) asm volatile("global_load_dword %0, %1, %2" : "+v"(tpf) : "v"((unsigned)(voff)), "s"((const void*)(sbase)) : "memory")
#define WLOAD(p) __builtin_nontemporal_load(p)
#define LBYTE(off, v) (*(volatile LAS unsigned char*)(L + L_SY + (off)) = (v))
__device__ __forceinline__ v2u tr_read(LAS unsigned char* a) {
  return __builtin_bit_cast(v2u, __builtin_amdgcn_ds_read_tr16_b64_v4i16((LAS s16x4*)a));
}
__device__ __forceinline__ v4u tr_read2(LAS unsigned char* a, LAS unsigned char* b) {
  const v2u x = tr_read(a), y = tr_read(b);
  return (v4u){x.x, x.y, y.x, y.y};
}
template <int CTRL>
__device__ __forceinline__ unsigned dppu(unsigned v) { return __builtin_amdgcn_update_dpp(v, v, CTRL, 0xf, 0xf, false); }
template <int CTRL>
__device__ __forceinline__ float dppm(float v) { return u2f(dppu<CTRL>(f2u(v))); }
__device__ __forceinline__ float xsum16(float v) {
  auto s = __builtin_amdgcn_permlane16_swap(f2u(v), f2u(v), false, false);
  return u2f(s[0]) + u2f(s[1]);
}
__device__ __forceinline__ float xmax16(float v) {
  auto s = __builtin_amdgcn_permlane16_swap(f2u(v), f2u(v), false, false);
  return fmaxf(u2f(s[0]), u2f(s[1]));
}
__device__ __forceinline__ float xsum32(float v) {
  auto s = __builtin_amdgcn_permlane32_swap(f2u(v), f2u(v), false, false);
  return u2f(s[0]) + u2f(s[1]);
}
__device__ __forceinline__ float xmax32(float v) {
  auto s = __builtin_amdgcn_permlane32_swap(f2u(v), f2u(v), false, false);
  return fmaxf(u2f(s[0]), u2f(s[1]));
}
__device__ __forceinline__ bool rec_ok(v4u r, unsigned tag) { return __all((r.y == tag) & (r.w == tag)); }
__device__ __forceinline__ bool flg_ok(v4u r, unsigned tag) { return __all((r.x == tag) & (r.y == tag) & (r.z == tag) & (r.w == tag)); }
__device__ __forceinline__ unsigned lds_wait(LAS unsigned char* a, unsigned mask) {
  unsigned w = 0;
#pragma nounroll
  for (unsigned spin = 0; spin < (SPIN_CAP << 4); ++spin) {
    w = __builtin_amdgcn_readfirstlane(*(volatile LAS unsigned*)a);
    if ((w & mask) == mask) break;
    __builtin_amdgcn_s_sleep(1);
  }
  asm volatile("" ::: "memory");
  return w;
}

__device__ __forceinline__ unsigned lds_nz(LAS unsigned char* a) {
  unsigned w = 0;
#pragma nounroll
  for (unsigned spin = 0; spin < (SPIN_CAP << 4); ++spin) {
    w = __builtin_amdgcn_readfirstlane(*(volatile LAS unsigned*)a);
    if (w) break;
    __builtin_amdgcn_s_sleep(1);
  }
  asm volatile("" ::: "memory");
  return w;
}
__device__ __forceinline__ unsigned long long lds_peek64(LAS unsigned char* a) {
  const v2u w = *(volatile LAS v2u*)a;
  return (unsigned long long)__builtin_amdgcn_readfirstlane(w.x) | ((unsigned long long)__builtin_amdgcn_readfirstlane(w.y) << 32);
}
__device__ __forceinline__ unsigned long long lds_wait64(LAS unsigned char* a, unsigned long long mask) {
  unsigned long long w = 0;
#pragma nounroll
  for (unsigned spin = 0; spin < (SPIN_CAP << 4); ++spin) {
    w = lds_peek64(a);
    if ((w & mask) == mask) break;
    __builtin_amdgcn_s_sleep(1);
  }
  asm volatile("" ::: "memory");
  return w;
}
__device__ __forceinline__ unsigned lds_ticket(LAS unsigned char* a, int lane) {
  unsigned t = 0;
  if (lane == 0) t = __hip_atomic_fetch_add((LAS unsigned*)a, 1u, __ATOMIC_RELAXED, __HIP_MEMORY_SCOPE_WORKGROUP);
  return __builtin_amdgcn_readfirstlane(t);
}
#define STAMP(k) do { } while (0)

#define GLOAD(ADDR) __builtin_amdgcn_raw_buffer_load_b128(wsr, (ADDR), 0, AUX_SC1)
#define GFINISH(N, qr, ADDR, SLP)                                                                            \
  do {                                                                                                       \
    unsigned done = 0;                                                                                       \
    _Pragma("nounroll") for (unsigned spin = 0; spin < SPIN_CAP; ++spin) {                                   \
      _Pragma("unroll") for (int i = 0; i < N; ++i)                                                          \
        if (!((done >> i) & 1)) { if (rec_ok(qr[i], tag)) done |= 1u << i; }                                 \
      if (done == (1u << N) - 1u) break;                                                                     \
      if (SLP) __builtin_amdgcn_s_sleep(SLP);                                                                \
      _Pragma("unroll") for (int i = 0; i < N; ++i)                                                          \
        if (!((done >> i) & 1)) qr[i] = GLOAD(ADDR);                                                         \
    }                                                                                                        \
  } while (0)
#define OK_FLG(r) (((r).x == tag) & ((r).y == tag) & ((r).z == tag) & ((r).w == tag))
#define KEEPV(r) asm volatile("" :: "v"(r))
#define WAIT_UNTIL(t) do { _Pragma("nounroll") for (unsigned spin = 0; spin < SPIN_CAP; ++spin) { if ((unsigned)__builtin_amdgcn_s_memrealtime() - t00 >= (t)) break; __builtin_amdgcn_s_sleep(1); } } while (0)
#define GSTART(N, qr, ADDR) do { _Pragma("unroll") for (int i = 0; i < N; ++i) qr[i] = GLOAD(ADDR); } while (0)

__global__ void __launch_bounds__(NTHR, 2) fused_kernel(Params p) {
  extern __shared__ __attribute__((aligned(16))) unsigned char lds_raw[];
  STAMP(0);
  const unsigned tstart = (unsigned)__builtin_amdgcn_s_memrealtime();
  static_assert(L_DUMP == 0x26800 && L_X == 0x24000 && L_WQ == 0x20000 && L_WU == 0x18000 && L_SY == 0x260e0 && CODE_LINES <= 192 && WS_END == 0xe00000, "volley");
  unsigned epoch;
  asm volatile(
      "s_load_dwordx8 s[40:47], %[ka], 0x0\n\t"
      "v_readfirstlane_b32 s48, %[tid]\n\t"
      "v_and_b32 v13, 63, %[tid]\n\t"
      "v_mov_b32 v10, 0\n\t"
      "v_mov_b32 v11, 0\n\t"
      "v_mov_b32 v12, 0x260e0\n\t"
      "s_getpc_b64 s[50:51]\n\t"
      "s_lshr_b32 s48, s48, 6\n\t"
      "s_cmp_lg_u32 s48, 1\n\t"
      "s_cbranch_scc1 7f\n\t"
      "ds_write2_b64 v12, v[10:11], v[10:11] offset1:1\n\t"
      "ds_write2_b64 v12, v[10:11], v[10:11] offset0:2 offset1:3\n\t"
      "s_waitcnt lgkmcnt(0)\n"
      "7:\n\t"
      "s_mov_b32 s54, 0\n\t"
      "s_and_b32 s49, %[wg], 7\n\t"
      "s_cmp_lg_u32 s48, 4\n\t"
      "s_cbranch_scc1 1f\n\t"
      "s_load_dwordx2 s[62:63], %[ka], 0x50\n\t"
      "s_mov_b32 s61, 0\n\t"
      "s_bfe_u32 s52, %[wg], 0x40003\n\t"
      "s_mul_i32 s52, s52, 12\n\t"
      "v_min_u32 v10, 11, v13\n\t"
      "v_add_u32 v10, s52, v10\n\t"
      "v_min_u32 v10, %[cl], v10\n\t"
      "v_lshlrev_b32 v10, 7, v10\n\t"
      "s_andn2_b32 s50, s50, 0x7f\n\t"
      "s_mov_b32 m0, 0x26800\n\t"
      "s_nop 0\n\t"
      "global_load_lds_dword v10, s[50:51]\n\t"
      "s_branch 4f\n"
      "1:\n\t"
      "s_cmp_lg_u32 s48, 0\n\t"
      "s_cbranch_scc1 2f\n\t"
      "v_lshrrev_b32 v10, 3, v13\n\t"
      "v_and_b32 v11, 7, v13\n\t"
      "v_bfe_u32 v12, v13, 3, 2\n\t"
      "v_lshrrev_b32 v13, 5, v13\n\t"
      "v_lshl_or_b32 v12, v12, 1, v13\n\t"
      "v_xor_b32 v11, v11, v12\n\t"
      "v_lshlrev_b32 v11, 4, v11\n\t"
      "v_lshl_or_b32 v10, v10, 13, v11\n\t"
      "s_lshl_b32 s49, s49, 10\n\t"
      "s_lshr_b32 s52, %[wg], 5\n\t"
      "s_lshl_b32 s52, s52, 7\n\t"
      "s_or_b32 s49, s49, s52\n\t"
      "s_bfe_u32 s52, %[wg], 0x20003\n\t"
      "s_lshl_b32 s52, s52, 20\n\t"
      "s_or_b32 s49, s49, s52\n\t"
      "s_mov_b32 m0, 0x20000\n\t"
      "s_mov_b32 s56, 0x24000\n\t"
      "s_mov_b32 s52, 0x10000\n\t"
      "s_waitcnt lgkmcnt(0)\n\t"
      "s_add_u32 s50, s44, s49\n\t"
      "s_addc_u32 s51, s45, 0\n\t"
      "s_branch 5f\n"
      "2:\n\t"
      "s_cmp_lg_u32 s48, 5\n\t"
      "s_cbranch_scc1 6f\n\t"
      "v_lshlrev_b32 v10, 4, v13\n\t"
      "s_mov_b32 m0, 0x24000\n\t"
      "s_mov_b32 s56, 0x26000\n\t"
      "s_movk_i32 s52, 0x420\n\t"
      "s_waitcnt lgkmcnt(0)\n\t"
      "s_mov_b64 s[50:51], s[40:41]\n"
      "5:\n\t"
      "v_xor_b32 v11, s54, v10\n\t"
      "global_load_lds_dwordx4 v11, s[50:51]\n\t"
      "s_add_u32 s54, s54, s52\n\t"
      "s_add_u32 m0, m0, 0x400\n\t"
      "s_cmp_lg_u32 m0, s56\n\t"
      "s_cbranch_scc1 5b\n\t"
      "s_barrier\n"
      "6:\n\t"
      "s_cmp_lt_u32 s48, 6\n\t"
      "s_cbranch_scc1 9f\n\t"
      "s_cmp_eq_u32 s48, 7\n\t"
      "s_cbranch_scc1 8f\n\t"
      "s_lshr_b32 s60, %[wg], 2\n\t"
      "s_lshl_b32 s61, s48, 5\n\t"
      "s_mov_b32 s62, %[t0]\n\t"
      "s_mov_b32 s63, %[t1]\n\t"
      "s_xor_b32 s60, s60, s61\n\t"
      "s_and_b32 s60, s60, 60\n\t"
      "s_lshr_b64 s[62:63], s[62:63], s60\n\t"
      "s_and_b32 s60, s62, 15\n\t"
      "s_lshl_b32 s61, s60, 3\n\t"
      "s_load_dwordx2 s[62:63], %[ka], s61\n\t"
      "s_mov_b32 s61, 0x3ffffc\n\t"
      "s_cmp_ge_u32 s60, 6\n\t"
      "s_cselect_b32 s61, 0x157ffc, s61\n\t"
      "s_cmp_eq_u32 s60, 9\n\t"
      "s_cselect_b32 s61, 0x1ffc, s61\n\t"
      "s_cmp_eq_u32 s60, 10\n\t"
      "s_cselect_b32 s61, 0xdffffc, s61\n\t"
      "s_bitcmp1_b32 %[wg], 3\n\t"
      "s_cselect_b32 s61, s61, 0\n\t"
      "s_lshl_b32 s49, s49, 22\n\t"
      "s_cmp_eq_u32 s60, 1\n\t"
      "s_cselect_b32 s49, s49, 0\n\t"
      "s_add_u32 s61, s61, s49\n"
      "4:\n\t"
      "s_waitcnt lgkmcnt(0)\n\t"
      "s_add_u32 s62, s62, s61\n\t"
      "s_addc_u32 s63, s63, 0\n\t"
      "s_load_dword %[ep], s[62:63], 0x0\n"
      "8:\n\t"
      "s_barrier\n"
      "9:\n\t"
      "s_waitcnt lgkmcnt(0)"
      : [ep] "=&s"(epoch)
      : [ka] "s"(__builtin_amdgcn_kernarg_segment_ptr()), [wg] "s"(__builtin_amdgcn_workgroup_id_x()), [tid] "v"(__builtin_amdgcn_workitem_id_x()),
        [cl] "n"(CODE_LINES - 1), [t0] "n"((unsigned)(PTAB & 0xffffffffull)), [t1] "n"((unsigned)(PTAB >> 32))
      : "memory", "m0", "scc", "s40", "s41", "s42", "s43", "s44", "s45", "s46", "s47", "s48", "s49", "s50", "s51", "s52", "s54", "s56",
        "s60", "s61", "s62", "s63", "v10", "v11", "v12", "v13");
  asm volatile("" :: "s"(p.x), "s"(p.ckv));
  STAMP(15);
  LAS unsigned char* L = (LAS unsigned char*)lds_raw;
  const int tid = threadIdx.x, lane = tid & 63;
  const int wave = __builtin_amdgcn_readfirstlane(tid >> 6);
  const int wg = blockIdx.x, xg = wg & 7, r = wg >> 3;
  const int w4 = __builtin_amdgcn_readfirstlane((tid >> 6) & 3);
  const int bA = xg, chunk = r;
  const int hA = xg * 4 + (r >> 3), sl = r & 7;
  const int hB = ((xg + 1) & 7) * 4 + (r >> 3);
  const int bO = (xg + 7) & 7, hO = (hA + 28) & 31;
  const int ntask = hB < 12 ? 6 : 5, task0 = hB < 12 ? 6 * hB : 12 + 5 * hB;
  const __amdgpu_buffer_rsrc_t wsr = __builtin_amdgcn_make_buffer_rsrc((void*)p.ws, 0, WS_END, 0x00020000);
  const int l16 = lane & 15, g4 = lane >> 4;
  const int l32 = lane & 31, hf = lane >> 5;
  const unsigned r8 = lane >> 3, q8 = lane & 7;
  const unsigned t00 = tstart;
  unsigned tag, tagv;
#define TAGLOAD() tagv = __builtin_amdgcn_raw_buffer_load_b32(wsr, WS_EPOCH, 0, 0)
#define TAGSET() do { tag = __builtin_amdgcn_readfirstlane(tagv) + 1u; tag += tag == 0; } while (0)
#define TAGGET() tag = lds_nz(L + L_SY + 24)
  const unsigned pz = q8 ^ (((r8 & 3) << 1) | (r8 >> 2));
  unsigned tpf = 0;
  const unsigned fa0 = (8 * g4 + (l16 >> 2)) * 128 + ((((l16 & 3) >> 1) ^ ((l16 >> 2) << 1)) * 16) + (l16 & 1) * 8;
  const unsigned fa1 = (fa0 ^ 16) + 512;
#define FRAG(base, nt, s) tr_read2(L + (base) + (fa0 ^ ((nt) * 32)) + (s) * 4096, L + (base) + (fa1 ^ ((nt) * 32)) + (s) * 4096)
  const bool hon = 4 + g4 < ntask;
  const unsigned zl = swz(l32);
  v4u vf[16];
  unsigned va[2];
#pragma unroll
  for (int tt = 0; tt < 2; ++tt) {
    const unsigned row = 8 * g4 + 4 * tt + (l16 >> 2);
    va[tt] = L_CKV + (wave >> 1) * 32768 + row * 256 + ((((wave & 1) * 8 + ((l16 & 3) >> 1)) ^ swz(row)) * 16) + 8 * (l16 & 1);
  }
#define VLOAD() do { _Pragma("unroll") for (int i = 0; i < 16; ++i) vf[i] = tr_read2(L + (va[0] ^ ((i & 3) * 32)) + (i >> 2) * 8192, L + (va[1] ^ ((i & 3) * 32)) + (i >> 2) * 8192); } while (0)
  f32x16 sacc;
#define TAILV() do { \
    const unsigned cr = 2 * wave + (r8 >> 2) + (r8 & 3) * 16; \
    const u16* sv = p.wuv + ((size_t)(sl * 64) * NH + hA) * HD; \
    GLDS(sv, cr * 8192 + pz * 16, T_V + wave * 1024); \
    GLDS(sv + 64, cr * 8192 + pz * 16, T_V + 8192 + wave * 1024); \
    const u16* sw = p.wuv + ((size_t)(sl * 64) * NH + hO) * HD; \
    GLDS(sw, cr * 8192 + pz * 16, T_W + wave * 1024); \
    GLDS(sw + 64, cr * 8192 + pz * 16, T_W + 8192 + wave * 1024); \
  } while (0)
#define TAILO(w) do { \
    const u16* so = p.wo + ((size_t)hB * HD + (w) * 8) * DM + sl * 64; \
    GLDS(so, r8 * 1024 + pz * 16, T_O + (w) * 1024); \
    GLDS(so + 64 * DM, r8 * 1024 + pz * 16, T_O + 8192 + (w) * 1024); \
    GLDS(p.wd + (size_t)task0 * 8 * DM + sl * 64, min((w) * 8 + (int)r8, ntask * 8 - 1) * 1024 + pz * 16, T_D + (w) * 1024); \
  } while (0)

  if (wave >= 4) {
    if (wave >= 6) {
      const int v = wave - 6;
      const u16* srcb = p.ckv + ((size_t)bA * SK + chunk * 4) * DC;
      const unsigned vo0 = g4 * 1024 + (l16 ^ ((g4 << 2) | v)) * 16, vo1 = vo0 ^ 32;
      if (!PROBE) BAR();
      lds_wait(L + L_SY + 16, 1);
      bool early = true;
      STAMP(3);
#pragma nounroll
      for (int blk = 0; blk < 4; ++blk) {
        if (blk == 3) lds_wait(L + L_SY + 12, 0x00010100);
#pragma unroll
        for (int n = 0; n < 16; ++n) {
          GLDS(srcb + ((2 * n + v + chunk) & 31) * (128 * DC) + blk * 128, (n & 1) ? vo1 : vo0, L_CKV + blk * 32768 + (n * 2 + v) * 1024);
          if (early) {
            VMWAIT(WIN0 - 1);
            if (__builtin_amdgcn_readfirstlane(*(volatile LAS unsigned*)(L + L_SY + 16)) & EMASK) early = false;
          } else VMWAIT(WIN - 1);
          if (n == ((WIN - 2) & 15) && blk > (WIN - 2) / 16) LBYTE(v, (1 << (blk - (WIN - 2) / 16)) - 1);
        }
        if (blk == 0) STAMP(4);
        if (blk == 1) STAMP(5);
        if (blk == 2) STAMP(12);
      }
      STAMP(1);
      if (WIN > 17) { VMWAIT(16); LBYTE(v, 7); }
      VMWAIT(0); LBYTE(v, 15);
      STAMP(2);
      lds_wait(L + L_SY, 0x0808);
      VLOAD();
    } else {
      if (wave == 4) {
        tag = epoch + 1u;
        tag += tag == 0;
        *(volatile LAS unsigned*)(L + L_SY + 24) = tag;
        VMWAIT(0);
        WAIT_UNTIL(TQ);
        STAMP(1);
        int dm = 0;
        {
          unsigned fo = WS_QF + xg * RS + (lane >> 1) * 256 + (lane & 1) * 32;
#pragma nounroll
          for (unsigned spin = 0; spin < SPIN_CAP; ++spin) {
            const v4u f = GLOAD(fo), g = GLOAD(fo + 16);
            if ((f.x == tag) & (f.z == tag) & (g.x == tag) & (g.z == tag)) {
              dm = max(max((int)(f.y - t00), (int)(f.w - t00)), max((int)(g.y - t00), (int)(g.w - t00)));
              fo = OOB;
            }
            if (__all(fo == OOB)) break;
            __builtin_amdgcn_s_sleep(8);
            asm volatile("" ::: "memory");
          }
        }
        STAMP(2);
        PFR(p.ws, min(lane, 5) * (2u << 20) + (2u << 20));
        PFR(p.wuv + ((size_t)(sl * 64) * NH + hA) * HD, 0);
        PFR(p.wo + (size_t)hB * HD * DM, 0);
        PFR(p.wd, 0);
        PFR(p.out, 0);
        float df = (float)min(max(dm, 0), 150);
        df = fmaxf(df, dppm<0x121>(df)); df = fmaxf(df, dppm<0x122>(df)); df = fmaxf(df, dppm<0x124>(df)); df = fmaxf(df, dppm<0x128>(df));
        const unsigned tg = TG + (unsigned)__builtin_amdgcn_readfirstlane((int)xmax32(xmax16(df)));
        lds_wait(L + L_SY + 12, 0x00000101);
        WAIT_UNTIL(tg);
        LBYTE(21, 1);
      } else {
        VMWAIT(0);
        LBYTE(2, 1);
        STAMP(1);
        const unsigned xb = L_X + (l16 & 7) * 1024;
        const v4u ones = {0x3f803f80u, 0x3f803f80u, 0x3f803f80u, 0x3f803f80u};
        v4u xs[8];
#pragma unroll
        for (int t = 0; t < 8; ++t) xs[t] = *(const LAS v4u*)(L + xb + (((8 * t + 4 * (l16 >> 3) + g4) ^ (2 * (l16 & 7))) * 16));
        f32x4 G = {0.f, 0.f, 0.f, 0.f}, S = {0.f, 0.f, 0.f, 0.f};
#pragma unroll
        for (int t = 0; t < 8; ++t) { G = mfma16(xs[t], xs[t], G); S = mfma16(ones, xs[t], S); }
        float dg = (l16 & 2) ? ((l16 & 1) ? G[3] : G[2]) : ((l16 & 1) ? G[1] : G[0]);
        dg = g4 == (l16 >> 2) ? dg : 0.f;
        dg = xsum32(xsum16(dg));
        dg += dppm<0x128>(dg);
        const float s0 = S[0];
        const float mean = (s0 + dppm<0x128>(s0)) * (1.f / DM);
        const float rstd = __builtin_amdgcn_rsqf(fmaxf(dg * (1.f / DM) - mean * mean, 0.f) + 1e-5f);
        if (lane < 8) *(LAS v2u*)(L + L_LN + lane * 8) = (v2u){f2u(mean), f2u(rstd)};
        LGKM0();
        LBYTE(2, 3);
        STAMP(2);
        lds_wait(L + L_SY + 20, 0x0100);
      }
      {
        const unsigned qo = WS_QA + bA * RS + w4 * 8192 + lane * 16;
        const unsigned qd = L_S + w4 * 8192 + g4 * 256;
        STAMP(3);
        unsigned todo = 3;
#pragma nounroll
        for (unsigned spin = 0; spin < SPIN_CAP; ++spin) {
          v4u qa[16];
          const unsigned q0 = (todo & 1) ? qo : OOB, q1 = (todo & 2) ? qo + 16384 : OOB;
#pragma unroll
          for (int i = 0; i < 16; ++i) qa[i] = GLOAD((i < 8 ? q0 : q1) + (i & 7) * 1024);
#pragma unroll
          for (int u = 0; u < 2; ++u) {
            if ((todo >> u) & 1) {
              unsigned m = 0;
#pragma unroll
              for (int i = 8 * u; i < 8 * u + 8; ++i) {
                m = max(max(m, qa[i].x), qa[i].y);
                m = max(max(m, qa[i].z), qa[i].w);
                if (!(i == 8 && wave == 5 && lane >= 14 && lane < 16))
                  *(volatile LAS v4u*)(L + qd + u * 16384 + (i & 7) * 1024 + ((l16 ^ ((g4 << 2) | (i & 3))) * 16)) = qa[i];
              }
              if (!__any(m == ~0u)) { LBYTE(wave + u * 2, 1); todo &= ~(1u << u); }
            }
          }
          if (!todo) break;
          __builtin_amdgcn_s_sleep(4);
          asm volatile("" ::: "memory");
        }
        STAMP(13);
      }
      lds_wait(L + L_SY, 0x0404);
      VLOAD();
      lds_wait64(L + L_SY, 0x0101010100000808ull);
      if (wave == 4) {
        const u16* sv = p.wuv + ((size_t)(sl * 64) * NH + hA) * HD;
        const u16* so = p.wo + ((size_t)hB * HD) * DM + sl * 64;
        PFR(sv, l32 * 8192 + hf * 128);
        PFR(sv + 32 * NH * HD, l32 * 8192 + hf * 128);
        const u16* sw = p.wuv + ((size_t)(sl * 64) * NH + hO) * HD;
        PFR(sw, l32 * 8192 + hf * 128);
        PFR(sw + 32 * NH * HD, l32 * 8192 + hf * 128);
        if (OV == 0 || (PM & 1)) PFR(so, lane * 1024);
        if (OV == 0 || (PM & 2)) PFR(so + 64 * DM, lane * 1024);
        if (OV == 0 || (PM & 4)) PFR(p.wd + (size_t)task0 * 8 * DM + sl * 64, min(lane, ntask * 8 - 1) * 1024);
      }
    }
  } else {
    if (wave == 0 || (wave == 1 && wg < NFFN)) {
      const unsigned st = wave ? L_WF : L_WQ;
      const int cg = (wg % 88) >> 2, mat = wg >= 88;
      const int kq1 = wave ? (wg & 3) : (sl & 3);
      if (wave) {
        const u16* src = (mat ? p.wu : p.wg) + (size_t)(kq1 * 128) * FF;
        const unsigned vo = (r8 * FF + min(cg * 64 + (int)pz * 8, FF - 8)) * 2;
        BAR();
        lds_wait(L + L_SY + 16, 0x0101);
#pragma unroll
        for (int i = 0; i < 16; ++i) GLDS(src + i * 8 * FF, vo, st + i * 1024);
      }
      lds_wait(L + L_SY, 0x010000);
      v4u xr[4];
#pragma unroll
      for (int s = 0; s < 4; ++s) {
        xr[s] = *(const LAS v4u*)(L + L_X + (l16 & 7) * 1024 + (((16 * kq1 + 4 * s + g4) ^ (2 * (l16 & 7))) * 16));
        if (l16 >= 8) xr[s] = (v4u){0x3f803f80u, 0x3f803f80u, 0x3f803f80u, 0x3f803f80u};
      }
      f32x4 acc[4];
      {
#pragma unroll
        for (int nt = 0; nt < 4; ++nt) acc[nt] = (f32x4){0.f, 0.f, 0.f, 0.f};
#pragma unroll
        for (int s = 0; s < 4; ++s) {
          if (s == 0) VMWAIT(12); else if (s == 1) VMWAIT(8); else if (s == 2) VMWAIT(4); else VMWAIT(0);
          if (s == 3) STAMP(1);
          v4u af[4];
#pragma unroll
          for (int nt = 0; nt < 4; ++nt) af[nt] = FRAG(st, nt, s);
#pragma unroll
          for (int nt = 0; nt < 4; ++nt) acc[nt] = mfma16(af[nt], xr[s], acc[nt]);
        }
      }
      TAGGET();
      lds_wait(L + L_SY, 0x020000);
      const v2u ln = *(const LAS v2u*)(L + L_LN + (l16 & 7) * 8);
      const float mn = u2f(ln.x), rs = u2f(ln.y);
      LGKM0();
      LBYTE(12 + wave, 1);
#pragma unroll
      for (int nt = 0; nt < 4; ++nt)
#pragma unroll
        for (int j = 0; j < 4; ++j) acc[nt][j] = (acc[nt][j] - mn * dppm<0x158>(acc[nt][j])) * rs;
      const unsigned ra = wave ? WS_GU + (cg * 8 + 2 * (l16 >> 3) + (g4 >> 1)) * 2048 + mat * 1024 + kq1 * 256 + (g4 & 1) * 128 + (l16 & 7) * 16
                               : WS_QP + xg * RS + (hA & 3) * 16384 + (((sl >> 2) * 4 + (kq1 >> 1)) * 2 + (g4 & 1)) * 1024 +
                                     (2 * (l16 >> 3) + (g4 >> 1)) * 256 + ((kq1 & 1) * 8 + (l16 & 7)) * 16;
#pragma unroll
      for (int i = 0; i < 2; ++i) {
        const unsigned a = pk2(acc[2 * i][0], acc[2 * i][1]), b = pk2(acc[2 * i][2], acc[2 * i][3]);
        const unsigned c = dppu<0x128>(pk2(acc[2 * i + 1][0], acc[2 * i + 1][1])), d = dppu<0x128>(pk2(acc[2 * i + 1][2], acc[2 * i + 1][3]));
        __builtin_amdgcn_raw_buffer_store_b128((v4u){l16 < 8 ? a : c, tag, l16 < 8 ? b : d, tag}, wsr, ra + i * (wave ? 8192 : 4096), 0, AUX_SC1);
      }
      asm volatile("" ::: "memory");
      LBYTE(17, 1);
      STAMP(2);
    } else if (wave == 1) {
      BAR();
      LBYTE(13, 1);
    } else if (wave == 2) {
      {
        const u16* src = p.wuk + ((size_t)(sl * 64) * NH + hA) * HD;
        const unsigned vo = g4 * 8192 + (l16 ^ g4) * 16;
        BAR();
#pragma unroll
        for (int n = 0; n < 16; ++n) GLDS(src + n * 4 * NH * HD, vo ^ ((n & 3) * 64), L_WU + n * 1024);
        LBYTE(16, 1);
      }
      VMWAIT(0);
      STAMP(1);
      v4u wf[16];
#pragma unroll
      for (int n = 0; n < 16; ++n) wf[n] = *(const LAS v4u*)(L + L_WU + ((n >> 2) * 16 + l16) * 256 + (((4 * (n & 3) + g4) ^ l16) * 16));
      LGKM0();
      LBYTE(14, 1);
      TAGGET();
      WAIT_UNTIL(TP);
      v4u qr[16];
      {
        bool todo = true;
#pragma nounroll
        for (unsigned spin = 0; spin < SPIN_CAP; ++spin) {
          if (todo) {
            GSTART(16, qr, WS_QP + xg * RS + (hA & 3) * 16384 + i * 1024 + lane * 16);
            unsigned bad = 0;
#pragma unroll
            for (int i = 0; i < 16; ++i) bad |= (qr[i].y ^ tag) | (qr[i].w ^ tag);
            todo = bad != 0;
          }
          if (!__any(todo)) break;
          __builtin_amdgcn_s_sleep(4);
          asm volatile("" ::: "memory");
        }
      }
      STAMP(2);
      const float scale = 0.08838834764831845f * 1.4426950408889634f;
      unsigned yp[2][2];
#pragma unroll
      for (int cp = 0; cp < 2; ++cp) {
        f32x4 a0 = {0.f, 0.f, 0.f, 0.f}, a1 = {0.f, 0.f, 0.f, 0.f};
#pragma unroll
        for (int k = 0; k < 8; ++k) {
          const v4u bq = {qr[2 * k].x, qr[2 * k].z, qr[2 * k + 1].x, qr[2 * k + 1].z};
          a0 = mfma16(wf[cp * 8 + (k >> 1)], bq, a0);
          a1 = mfma16(wf[cp * 8 + 4 + (k >> 1)], bq, a1);
        }
        float y[4];
#pragma unroll
        for (int j = 0; j < 4; ++j) {
          const float keep = l16 < 8 ? a0[j] : a1[j], give = l16 < 8 ? a1[j] : a0[j];
          y[j] = (keep + dppm<0x128>(give)) * scale;
        }
        yp[cp][0] = pk2(y[0], y[1]); yp[cp][1] = pk2(y[2], y[3]);
      }
      lds_wait(L + L_SY + 20, 0x010000);
      {
        auto s0 = __builtin_amdgcn_permlane16_swap(yp[0][0], yp[1][0], false, false);
        auto s1 = __builtin_amdgcn_permlane16_swap(yp[0][1], yp[1][1], false, false);
        __builtin_amdgcn_raw_buffer_store_b128((v4u){min(s0[0], ~1u), min(s1[0], ~1u), min(s0[1], ~1u), min(s1[1], ~1u)}, wsr,
                                               WS_QA + (l16 & 7) * RS + ((sl >> 1) * 32 + hA) * 256 + (sl & 1) * 128 +
                                                   (2 * (g4 & 1) + (l16 >> 3)) * 32 + (g4 >> 1) * 16, 0, AUX_SC1);
      }
      asm volatile("" ::: "memory");
      LBYTE(18, 1);
      STAMP(12);
    } else {
      if (W3) BAR();
      if (W3 == 2) lds_wait(L + L_SY + 16, 1);
      __builtin_amdgcn_raw_buffer_store_b128((v4u){~0u, ~0u, ~0u, ~0u}, wsr, WS_QA + r8 * RS + ((sl >> 1) * 32 + hA) * 256 + (sl & 1) * 128 + q8 * 16, 0, AUX_SC1);
#pragma unroll
      for (int i = 0; i < 2; ++i)
        __builtin_amdgcn_raw_buffer_store_b128((v4u){~0u, ~0u, ~0u, ~0u}, wsr, WS_CX + ((hA * 8 + 4 * i + g4) * 8 + sl) * 256 + l16 * 16, 0, AUX_SC1);
      if (!W3) BAR();
      VMWAIT(0);
      LBYTE(22, 1);
      TAGGET();
      if (lane < 8) __builtin_amdgcn_raw_buffer_store_b64((v2u){tag, t00}, wsr, WS_QF + lane * RS + hA * 256 + sl * 8, 0, AUX_SC1);
      STAMP(12);
    }
    {
#pragma unroll
      for (int i = 0; i < 16; ++i) sacc[i] = 0.f;
      const unsigned tz = (hf ^ zl) * 16;
      const unsigned kb = L_CKV + (wave * 32 + l32) * 256 + tz;
      const unsigned ho = WS_QA + bA * RS + 3 * 8192 + 224 + hf * 16;
      v4u qa[8], qc[8], kf[32];
      unsigned ka[8];
#pragma unroll
      for (int s = 0; s < 8; ++s) ka[s] = kb ^ (s * 32);
      const unsigned qd = L_S - wave * 8192;
#define DSR(dst, addr, off) asm volatile("ds_read_b128 %0, %1 offset:%2" : "=v"(dst) : "v"(addr), "n"(off) : "memory")
#define DSRU(dst, addr, off) asm volatile("ds_read_b128 %0, %1 offset:%2" : "+v"(dst) : "v"(addr), "n"(off) : "memory")
#define KREAD(b) do { _Pragma("unroll") for (int s = 0; s < 8; ++s) DSR(kf[8 * (b) + s], ka[s] + ((b) >> 1) * 65536, ((b) & 1) * 32768); } while (0)
#define KREAD3() do { _Pragma("unroll") for (int s = 0; s < 8; ++s) DSRU(kf[24 + s], ka[s] + 65536, 32768); } while (0)
#define QREAD(q, b) do { _Pragma("unroll") for (int s = 0; s < 8; ++s) DSR(q[s], ka[s] + qd, (b) * 8192); } while (0)
#define LGW(n, x) asm volatile("s_waitcnt lgkmcnt(%1)" : "+v"(x) : "n"(n) : "memory")
#define QKB(q, b) do { _Pragma("unroll") for (int s = 0; s < 8; ++s) { LGW(15 - s, q[s]); sacc = mfma32(kf[8 * (b) + s], q[s], sacc); __builtin_amdgcn_sched_barrier(0); } } while (0)
      lds_wait(L + L_SY, 0x0101);
      KREAD(0);
      lds_wait(L + L_SY, 0x0202);
      KREAD(1);
      lds_wait(L + L_SY + 20, 0x0100);
      v4u hole = GLOAD(ho);
      lds_wait(L + L_SY, 0x0404);
      KREAD(2);
#pragma unroll
      for (int s = 0; s < 8; ++s) asm volatile("" : "=v"(kf[24 + s]));
      unsigned long long have = 0;
#pragma nounroll
      for (unsigned spin = 0; spin < (SPIN_CAP << 4); ++spin) {
        have = lds_peek64(L + L_SY);
        if ((have & 0x0808) == 0x0808 || (unsigned)(have >> 32) == 0x01010101u) break;
        __builtin_amdgcn_s_sleep(1);
      }
      asm volatile("" ::: "memory");
      const bool k3 = (have & 0x0808) == 0x0808;
      if (k3) {
        KREAD3();
        lds_wait(L + L_SY + 4, 0x01010101);
      }
      STAMP(4);
      QREAD(qa, 0);
      QREAD(qc, 1);
      QKB(qa, 0);
      QREAD(qa, 2);
      QKB(qc, 1);
      QREAD(qc, 3);
      QKB(qa, 2);
      LBYTE(8 + wave, 1);
#pragma nounroll
      for (unsigned spin = 0; spin < SPIN_CAP; ++spin) {
        if (__all(max(max(hole.x, hole.y), max(hole.z, hole.w)) != ~0u)) break;
        hole = GLOAD(ho);
      }
#pragma unroll
      for (int s = 0; s < 8; ++s) LGW(0, qc[s]);
      if (l32 == 0) qc[7] = hole;
      VLOAD();
      if (!k3) {
        lds_wait(L + L_SY, 0x0808);
        KREAD3();
        LGKM0();
      }
#pragma unroll
      for (int s = 0; s < 8; ++s) asm volatile("" : "+v"(kf[24 + s]));
#pragma unroll
      for (int s = 0; s < 8; ++s) sacc = mfma32(kf[24 + s], qc[s], sacc);
      lds_wait(L + L_SY + 8, 0x01010101);
      STAMP(5);
      float mx = __builtin_ceilf(sacc[0]);
#pragma unroll
      for (int i = 1; i < 15; i += 2) mx = max3(mx, sacc[i], sacc[i + 1]);
      mx = max3(mx, sacc[15], sacc[15]);
      *(LAS float*)(L + L_ST + l32 * 32 + wave * 8 + hf * 4) = __builtin_ceilf(mx);
    }
  }
  BAR();
  float M = 0.f;
  if (wave < 4) {
    const f32x4 m0 = *(const LAS f32x4*)(L + L_ST + l32 * 32), m1 = *(const LAS f32x4*)(L + L_ST + l32 * 32 + 16);
    M = max3(max3(m0[0], m0[1], m0[2]), max3(m0[3], m1[0], m1[1]), max3(m1[2], m1[3], m1[3]));
    unsigned pp[8];
#pragma unroll
    for (int q = 0; q < 4; ++q) {
      pp[2 * q] = pk2(__builtin_amdgcn_exp2f(sacc[4 * q] - M), __builtin_amdgcn_exp2f(sacc[4 * q + 1] - M));
      pp[2 * q + 1] = pk2(__builtin_amdgcn_exp2f(sacc[4 * q + 2] - M), __builtin_amdgcn_exp2f(sacc[4 * q + 3] - M));
      *(LAS v2u*)(L + L_P + l32 * 256 + (((wave * 4 + q) ^ l16) * 16) + hf * 8) = (v2u){pp[2 * q], pp[2 * q + 1]};
    }
    float ls = 0.f;
#pragma unroll
    for (int q = 0; q < 8; ++q) ls = dot_ones(pp[q], ls);
    *(LAS float*)(L + L_ST + 1024 + l32 * 32 + wave * 8 + hf * 4) = ls;
    STAMP(3);
  }
  BAR();
  if (wave < 4 || wave > 5) STAMP(13);
  {
    v4u pf[8];
    {
      const unsigned pbs = L_P + l16 * 256 + (g4 ^ l16) * 16;
#pragma unroll
      for (int i = 0; i < 8; ++i) pf[i] = *(const LAS v4u*)(L + (pbs ^ ((i & 3) * 64)) + (i >> 2) * 4096);
    }
    tagv = *(const LAS unsigned*)(L + L_SY + 24);
    f32x4 o[2][4];
#pragma unroll
    for (int i = 0; i < 8; ++i) o[i >> 2][i & 3] = (f32x4){0.f, 0.f, 0.f, 0.f};
    const unsigned pbase = WS_PART + ((((bA * 32 + 4 * g4) * 8 + wave) * 32 + (chunk ^ (wave * 4 + g4))) * 16 + l16) * 8;
#define PSTORE(hh, j) __builtin_amdgcn_raw_buffer_store_b64((v2u){pk2(o[hh][0][j], o[hh][1][j]), pk2(o[hh][2][j], o[hh][3][j])}, wsr, pbase + (16 * (hh) + (j)) * (8 * 32 * 128), 0, AUX_SC1)
#pragma unroll
    for (int i = 0; i < 16; ++i) o[0][i & 3] = mfma16(pf[i >> 2], vf[i], o[0][i & 3]);
#pragma unroll
    for (int s = 0; s < 4; ++s) {
      __builtin_amdgcn_sched_barrier(0);
#pragma unroll
      for (int t = 0; t < 4; ++t) o[1][t] = mfma16(pf[4 + s], vf[4 * s + t], o[1][t]);
      __builtin_amdgcn_sched_barrier(0);
      PSTORE(0, s);
    }
    __builtin_amdgcn_sched_barrier(0);
    STAMP(6);
#pragma unroll
    for (int j = 0; j < 4; ++j) PSTORE(1, j);
    if (wave == 3) {
      const f32x4 l0 = *(const LAS f32x4*)(L + L_ST + 1024 + l32 * 32), l1 = *(const LAS f32x4*)(L + L_ST + 1024 + l32 * 32 + 16);
      const float lt = ((l0[0] + l0[1]) + (l0[2] + l0[3])) + ((l1[0] + l1[1]) + (l1[2] + l1[3]));
      if (lane < 32)
        __builtin_amdgcn_raw_buffer_store_b64((v2u){f2u(M), f2u(lt)}, wsr, WS_FL + ((((lane >> 2) * 8 + bA) * 32 + chunk) * 4 + (lane & 3)) * 8, 0, AUX_SC1);
    }
  }
  tag = __builtin_amdgcn_readfirstlane(tagv);
  STAMP(7);
  v4u wv[4];
#define ALOAD(dst, addr) asm volatile("buffer_load_dwordx4 %0, %1, %2, 0 offen sc1" : "=v"(dst) : "v"(addr), "s"(wsr) : "memory")
  {
    const int bC = wave == xg ? bO : wave, hC = wave == xg ? hO : hA;
    v4u gr[2];
    const unsigned gq = wave < ntask && NFFN ? WS_GU + (task0 + wave) * 2048 + lane * 16 : OOB;
    unsigned fa = lane < 8 && lane != xg ? WS_PF + lane * RS + (bA * (32 / FPL) + (unsigned)chunk / FPL) * 256 + ((unsigned)chunk % FPL) * 4 : OOB;
    unsigned fo = lane < 8 ? WS_PF + xg * RS + (bC * (32 / FPL) + lane / (FPL / 4)) * 256 + (lane % (FPL / 4)) * 16 : OOB;
    asm volatile("" : "+v"(fa), "+v"(fo));
    VMWAIT(0);
    if ((lds_ticket(L + L_SY + 28, lane) & 255) == 7) __builtin_amdgcn_raw_buffer_store_b32(tag, wsr, fa, 0, AUX_SC1);
    STAMP(8);
    KEEPV(tpf);
    TAILV();
    if (OV == 0) TAILO(wave);
    ALOAD(gr[0], gq);
    ALOAD(gr[1], gq + 1024);
    {
      v4u f;
      ALOAD(f, fo);
      VMWAIT(1);
      asm volatile("" : "+v"(gr[0]), "+v"(gr[1]));
      if (lane == 0) __hip_atomic_fetch_add((LAS unsigned*)(L + L_SY + 28), 0x100u, __ATOMIC_RELAXED, __HIP_MEMORY_SCOPE_WORKGROUP);
      if (wave < ntask && NFFN) {
#pragma nounroll
        for (unsigned spin = 0; spin < SPIN_CAP; ++spin) {
          if (__all((gr[0].y == tag) & (gr[0].w == tag) & (gr[1].y == tag) & (gr[1].w == tag))) break;
          ALOAD(gr[0], gq);
          ALOAD(gr[1], gq + 1024);
          VMWAIT(0);
          asm volatile("" : "+v"(gr[0]), "+v"(gr[1]));
        }
        const unsigned e = (l16 >> 2) + 4 * (l16 & 1);
        const unsigned v = (l16 & 2) ? 0u : ((e & 1) ? 0x3f800000u : 0x00003f80u);
        f32x4 a = {0.f, 0.f, 0.f, 0.f};
        a = mfma16((v4u){(e >> 1) == 0 ? v : 0u, (e >> 1) == 1 ? v : 0u, (e >> 1) == 2 ? v : 0u, (e >> 1) == 3 ? v : 0u}, (v4u){gr[0].x, gr[0].z, gr[1].x, gr[1].z}, a);
        const float gv = a[0], uv = a[1];
        const float yy = 0.7978845608028654f * (gv + 0.044715f * gv * gv * gv);
        const float th = 1.f - 2.f / (__expf(2.f * yy) + 1.f);
        *(LAS u16*)(L + L_HM + wave * 128 + (l16 & 7) * 16 + ((l16 >> 3) * 4 + g4) * 2) = (u16)pk2(0.5f * gv * (1.f + th) * uv, 0.f);
      }
#pragma nounroll
      for (unsigned spin = 0; spin < SPIN_CAP; ++spin) {
        VMWAIT(0);
        asm volatile("" : "+v"(f));
        if (OK_FLG(f)) fo = OOB;
        if (__all(fo == OOB)) break;
        if (QM && spin == QS && ((QM >> wave) & 1)) {
          const u16* so = p.wo + ((size_t)hB * HD + wave * 64) * DM + sl * 64;
          if (wave < 2) PFR(so, lane * 1024);
          else PFR(p.wd + (size_t)task0 * 8 * DM + sl * 64, min(lane, ntask * 8 - 1) * 1024);
        }
        __builtin_amdgcn_s_sleep(PSLP);
        ALOAD(f, fo);
      }
      asm volatile("" ::: "memory");
    }
    KEEPV(tpf);
    STAMP(14);
    v2u pr[9];
    const unsigned po = WS_PART + ((((bC * 32 + hC) * 8 + sl) * 32) * 16 + lane) * 8;
    pr[8] = __builtin_amdgcn_raw_buffer_load_b64(wsr, WS_FL + ((((hC >> 2) * 8 + bC) * 32 + l32) * 4 + (hA & 3)) * 8, 0, AUX_SC1);
#pragma unroll
    for (int i = 0; i < 8; ++i) pr[i] = __builtin_amdgcn_raw_buffer_load_b64(wsr, po + i * 512, 0, AUX_SC1);
    if (OV == 5 && lane == 0) __hip_atomic_fetch_add((LAS unsigned*)(L + L_SY + 28), 0x1000000u, __ATOMIC_RELAXED, __HIP_MEMORY_SCOPE_WORKGROUP);
    lds_wait(L + L_SY + 28, 0x800);
#pragma unroll
    for (int i = 0; i < 4; ++i) { wv[i] = FRAG((i & 2 ? T_W : T_V) + (wave >> 2) * 8192, wave & 3, i & 1); asm volatile("" : "+v"(wv[i])); }
    const float mj = u2f(pr[8].x);
    float M = mj;
    M = fmaxf(M, dppm<0x121>(M)); M = fmaxf(M, dppm<0x122>(M)); M = fmaxf(M, dppm<0x124>(M)); M = fmaxf(M, dppm<0x128>(M));
    M = xmax16(M);
    const float wj = bfhi(f2u(__builtin_amdgcn_exp2f(mj - M)));
    float Ls = wj * u2f(pr[8].y);
    Ls += dppm<0x121>(Ls); Ls += dppm<0x122>(Ls); Ls += dppm<0x124>(Ls); Ls += dppm<0x128>(Ls);
    Ls = xsum16(Ls);
    const float inv = __builtin_amdgcn_rcpf(Ls);
    const unsigned e4 = l16 & 3;
    const unsigned sh = (e4 & 1) ? 0u : 16u, k0 = e4 < 2 ? ~0u : 0u;
    unsigned wa[16];
#pragma unroll
    for (int u = 0; u < 8; ++u) {
      const unsigned w = (unsigned)__builtin_amdgcn_ds_bpermute(((g4 + 4 * u) ^ (sl * 4 + ((hC >> 2) & 3))) * 4, f2u(wj)) >> sh;
      wa[2 * u] = w & k0; wa[2 * u + 1] = w & ~k0;
    }
    f32x4 acc = {0.f, 0.f, 0.f, 0.f};
#pragma unroll
    for (int u = 0; u < 4; ++u)
      acc = mfma16((v4u){wa[4 * u], wa[4 * u + 1], wa[4 * u + 2], wa[4 * u + 3]}, (v4u){pr[2 * u].x, pr[2 * u].y, pr[2 * u + 1].x, pr[2 * u + 1].y}, acc);
    if (lane < 16) *(LAS v2u*)(L + L_CL + wave * 144 + lane * 8) = (v2u){pk2(acc[0] * inv, acc[1] * inv), pk2(acc[2] * inv, acc[3] * inv)};
    if (OV == 5 && wave < 4) { lds_wait(L + L_SY + 28, 0x8000000); if (OD) __builtin_amdgcn_s_sleep(OD); TAILO(wave); TAILO(wave + 4); }
    BAR();
  }
  v4u ao[4];
  f32x4 accE = {0.f, 0.f, 0.f, 0.f};
  const unsigned one = (l16 & 1) ? 0x3f800000u : 0x00003f80u;
  const unsigned s0 = (l16 & 2) ? 0u : one, s1 = (l16 & 2) ? one : 0u;
  {
    const v4u b0 = *(const LAS v4u*)(L + L_CL + (l16 & 7) * 144 + g4 * 16), b1 = *(const LAS v4u*)(L + L_CL + (l16 & 7) * 144 + 64 + g4 * 16);
    f32x4 acc = {0.f, 0.f, 0.f, 0.f};
    acc = mfma16(wv[0], b0, acc);
    acc = mfma16(wv[1], b1, acc);
    f32x4 acw = {0.f, 0.f, 0.f, 0.f};
    acw = mfma16(wv[2], b0, acw);
    acw = mfma16(wv[3], b1, acw);
    if (l16 == xg) acc = acw;
    if (l16 < 8)
      __builtin_amdgcn_raw_buffer_store_b64((v2u){min(pk2(acc[0], acc[1]), ~1u), min(pk2(acc[2], acc[3]), ~1u)}, wsr,
                                            WS_CX + (((((l16 == xg ? hO : hA) * 8 + wave) * 8 + sl) * 8 + (l16 == xg ? bO : l16)) * 4 + g4) * 8, 0, AUX_SC1);
    STAMP(9);
    if (OV == 4 && wave < 4 && !NOO) { TAILO(wave); TAILO(wave + 4); }
#define OFRAG() do { \
      _Pragma("unroll") for (int ks = 0; ks < 4; ++ks) ao[ks] = FRAG(T_O, wave, ks); \
      const v4u hm0 = *(const LAS v4u*)(L + L_HM + g4 * 128 + (l16 & 7) * 16), hm1 = *(const LAS v4u*)(L + L_HM + 512 + g4 * 128 + (l16 & 7) * 16); \
      accE = mfma16(FRAG(T_D, wave, 0), hm0, accE); \
      accE = mfma16(FRAG(T_D, wave, 1), hon ? hm1 : (v4u){0u, 0u, 0u, 0u}, accE); \
    } while (0)
    if (wave < 4) {
      VMWAIT(0);
      if (lane == 0) __hip_atomic_fetch_add((LAS unsigned*)(L + L_SY + 28), 0x10000u, __ATOMIC_RELAXED, __HIP_MEMORY_SCOPE_WORKGROUP);
      lds_wait(L + L_SY + 28, 0x40000);
      OFRAG();
      STAMP(10);
    } else {
      v4u cr[4];
      const unsigned co = WS_CX + (hB * 8 + 2 * w4) * 2048 + lane * 16;
      if (CDELAY) __builtin_amdgcn_s_sleep(CDELAY);
#pragma nounroll
      for (unsigned spin = 0; spin < SPIN_CAP; ++spin) {
        unsigned m = 0;
#pragma unroll
        for (int i = 0; i < 4; ++i) cr[i] = GLOAD(co + i * 1024);
#pragma unroll
        for (int i = 0; i < 4; ++i) m = max(max(max(m, cr[i].x), cr[i].y), max(cr[i].z, cr[i].w));
        if (!__any(m == ~0u)) break;
        __builtin_amdgcn_s_sleep(1);
      }
      STAMP(10);
      const v4u se = {(l16 >> 1) == 0 ? one : 0u, (l16 >> 1) == 1 ? one : 0u, (l16 >> 1) == 2 ? one : 0u, (l16 >> 1) == 3 ? one : 0u};
#pragma unroll
      for (int u = 0; u < 2; ++u) {
        f32x4 a = {0.f, 0.f, 0.f, 0.f};
        a = mfma16(se, cr[2 * u], a);
        a = mfma16(se, cr[2 * u + 1], a);
        if (lane < 32) *(LAS v2u*)(L + L_CX + (l16 >> 1) * 272 + (2 * w4 + u) * 32 + (l16 & 1) * 16 + g4 * 8) = (v2u){pk2(a[0], a[1]), pk2(a[2], a[3])};
      }
    }
    BAR();
  }
  if (wave < 4) {
#pragma unroll
    for (int ks = 0; ks < 4; ++ks) accE = mfma16(ao[ks], *(const LAS v4u*)(L + L_CX + (l16 & 7) * 272 + ks * 64 + g4 * 16), accE);
    if (l16 < 8)
      __builtin_amdgcn_raw_buffer_store_b128((v4u){pk2(accE[0], accE[1]), tag, pk2(accE[2], accE[3]), tag}, wsr,
                                             WS_OG + ((((sl * 4 + wave) * 32 + hB) * 8 + l16) * 4 + g4) * 16, 0, AUX_SC1);
    STAMP(11);
  } else if (wave == 4) {
    const unsigned oo = WS_OG + ((((wg & 31) * 32 + (lane >> 2)) * 8 + (wg >> 5)) * 4 + (lane & 3)) * 16;
    const v2u xw = *(const GAS v2u*)(p.x + wg * 16 + (lane & 3) * 4);
    v4u r0, r1;
    if (ODELAY) __builtin_amdgcn_s_sleep(ODELAY);
#pragma nounroll
    for (unsigned spin = 0; spin < SPIN_CAP; ++spin) {
      r0 = GLOAD(oo);
      r1 = GLOAD(oo + 16 * 512);
      if (__all((r0.y == tag) & (r0.w == tag) & (r1.y == tag) & (r1.w == tag))) break;
      __builtin_amdgcn_s_sleep(1);
    }
    f32x4 acc = {0.f, 0.f, 0.f, 0.f};
    acc = mfma16((v4u){s0, s1, s0, s1}, (v4u){r0.x, r0.z, r1.x, r1.z}, acc);
#pragma unroll
    for (int j = 0; j < 4; ++j) { acc[j] += dppm<0x124>(acc[j]); acc[j] += dppm<0x128>(acc[j]); }
    if (lane < 4)
      *(GAS v2u*)(p.out + wg * 16 + lane * 4) = (v2u){pk2(bflo(xw.x) + acc[0], bfhi(xw.x) + acc[1]), pk2(bflo(xw.y) + acc[2], bfhi(xw.y) + acc[3])};
    if (wg == 0 && lane == 0) __builtin_amdgcn_raw_buffer_store_b32(tag, wsr, WS_EPOCH, 0, AUX_SC1);
    STAMP(11);
  }
}

extern "C" void kernel_launch(void* const* d_in, const int* in_sizes, int n_in, void* d_out, int out_size, void* d_ws, size_t ws_size,
                              hipStream_t stream) {
  Params p;
  p.x = (const u16*)d_in[0]; p.ckv = (const u16*)d_in[1]; p.wq = (const u16*)d_in[2]; p.wuk = (const u16*)d_in[3];
  p.wuv = (const u16*)d_in[4]; p.wo = (const u16*)d_in[5]; p.wg = (const u16*)d_in[7]; p.wu = (const u16*)d_in[8]; p.wd = (const u16*)d_in[9];
  p.out = (u16*)d_out; p.ws = (unsigned char*)d_ws;
  (void)hipFuncSetAttribute((const void*)fused_kernel, hipFuncAttributeMaxDynamicSharedMemorySize, LDS_BYTES);
  fused_kernel<<<NWG, NTHR, LDS_BYTES, stream>>>(p);
}
```

```cpp
#include <hip/hip_runtime.h>
#include <hip/hip_bf16.h>
#include <stdint.h>

#define STAMPS 0
#define CODE_LINES 167
#define WIN 14
#define CDELAY 8
#define ODELAY 1
#define NFFN 176
#define WIN0 6
#define EMASK 0x010000
#define PSLP 1
#define FPL 32
#define TP 310
#define TQD 110
#define TPD 40
#define TGD 275
#define WIN1 22
#define WIN2 30
#define SA 205
#define SDT 8
#define SL1 20
#define SL2 50
#define SMSK 1
#define PFP 0
#define PFD 300
#define VE 16
#define OPN 2
#define OPS 10
#define PPN 1
#define PPS 7
#define CPN 0
#define CPS 10
#define KF 0
#define KFL "nt"
#define PTAB 0x2a763198541a7631ull

typedef unsigned v4u __attribute__((ext_vector_type(4)));
typedef unsigned v2u __attribute__((ext_vector_type(2)));
typedef float f32x2 __attribute__((ext_vector_type(2)));
typedef float f32x4 __attribute__((ext_vector_type(4)));
typedef float f32x16 __attribute__((ext_vector_type(16)));
typedef short bf16x8 __attribute__((ext_vector_type(8)));
typedef short s16x4 __attribute__((ext_vector_type(4)));
typedef unsigned short u16x2 __attribute__((ext_vector_type(2)));
typedef __bf16 bf16x2 __attribute__((ext_vector_type(2)));
typedef unsigned short u16;
#define LAS __attribute__((address_space(3)))
#define GAS __attribute__((address_space(1)))

#define VQN 0
#define VQF ""
#define PFN 0
#define PFF ""
#define UKX 0
#define FGX 0
#define TVX 0
#define TOX 0
#define VQS 1
#define NPS 0
#define NPL 0
#define NGL 0
#define NCL 0
#define NST 0
#define ALF ""
#define WSO 2
#define MT 0
constexpr int NB = 8, NH = 32, SK = 4096, HD = 128, DM = 512, DC = 512, FF = 1376;
constexpr int NWG = 256, NTHR = 512;
constexpr unsigned WS_EPOCH = 0;
#define RS 262144
constexpr unsigned WS_QF = 4096;
constexpr unsigned WS_PF = WS_QF + 8192;
constexpr unsigned WS_QA = WS_PF + 8192;
constexpr unsigned WS_QP = WS_QA + 32768;
static_assert(WS_QP + 65536 <= RS, "ws map");
constexpr unsigned WS_GU = 8 * RS;
constexpr unsigned WS_FL = WS_GU + 176 * 2048;
constexpr unsigned WS_CX = WS_FL + 8 * 8 * 32 * 4 * 16;
constexpr unsigned WS_OG = WS_CX + 32 * 8 * 8 * 8 * 4 * 16;
constexpr unsigned WS_PART = 6u << 20;
constexpr unsigned WS_END = WS_PART + (8u << 20);
constexpr unsigned WS_STAMP = 32u << 20;
static_assert(WS_OG + 32 * 32 * 8 * 4 * 16 <= WS_PART, "ws map");
constexpr unsigned L_CKV = 0;
constexpr unsigned L_S = 128 * 1024;
constexpr unsigned L_SY = L_S + 24576 + 224;
constexpr unsigned L_WQ = L_S, L_WU = 98304, L_WF = 114688;
constexpr unsigned L_X = L_S + 16384;
constexpr unsigned L_LN = L_S + 25600;
constexpr unsigned L_DUMP = L_S + 26624;
constexpr unsigned T_O = 0, T_V = 16384, T_D = 32768, T_W = 49152;
constexpr unsigned L_P = L_S;
constexpr unsigned L_ST = L_S + 8192;
constexpr unsigned L_CL = L_S + 16384;
constexpr unsigned L_CX = L_S + 16384 + 2048;
constexpr unsigned L_HM = L_S + 16384 + 4608;
constexpr unsigned LDS_BYTES = 160 * 1024;
constexpr int AUX_NT = 2, AUX_SC1 = 16;
constexpr int AUX_PS = AUX_SC1 | (NPS ? AUX_NT : 0), AUX_PL = AUX_SC1 | (NPL ? AUX_NT : 0), AUX_GL = AUX_SC1 | (NGL ? AUX_NT : 0),
              AUX_CL = AUX_SC1 | (NCL ? AUX_NT : 0), AUX_ST = AUX_SC1 | (NST ? AUX_NT : 0);
constexpr unsigned SPIN_CAP = 1u << 18;
constexpr unsigned OOB = 0x80000000u;

struct Params {
  const u16 *x, *ckv, *wq, *wuk, *wuv, *wo, *wg, *wu, *wd;
  u16* out;
  unsigned char* ws;
};

__device__ __forceinline__ float u2f(unsigned u) { return __uint_as_float(u); }
__device__ __forceinline__ unsigned f2u(float f) { return __float_as_uint(f); }
__device__ __forceinline__ float bflo(unsigned w) { return u2f(w << 16); }
__device__ __forceinline__ float bfhi(unsigned w) { return u2f(w & 0xffff0000u); }
__device__ __forceinline__ unsigned pk2(float lo, float hi) {
  f32x2 f = {lo, hi};
  return __builtin_bit_cast(unsigned, __builtin_convertvector(f, bf16x2));
}
__device__ __forceinline__ float dot_ones(unsigned a, float c) {
  return __builtin_amdgcn_fdot2_f32_bf16(__builtin_bit_cast(bf16x2, a), __builtin_bit_cast(bf16x2, 0x3f803f80u), c, false);
}
__device__ __forceinline__ unsigned pksubsat(unsigned a, unsigned b) {
  return __builtin_bit_cast(unsigned, __builtin_elementwise_sub_sat(__builtin_bit_cast(u16x2, a), __builtin_bit_cast(u16x2, b)));
}
__device__ __forceinline__ f32x4 mfma16(v4u a, v4u b, f32x4 c) {
  return __builtin_amdgcn_mfma_f32_16x16x32_bf16(__builtin_bit_cast(bf16x8, a), __builtin_bit_cast(bf16x8, b), c, 0, 0, 0);
}
__device__ __forceinline__ f32x16 mfma32(v4u a, v4u b, f32x16 c) {
  return __builtin_amdgcn_mfma_f32_32x32x16_bf16(__builtin_bit_cast(bf16x8, a), __builtin_bit_cast(bf16x8, b), c, 0, 0, 0);
}
__device__ __forceinline__ float max3(float a, float b, float c) {
  float d;
  asm("v_max3_f32 %0, %1, %2, %3" : "=v"(d) : "v"(a), "v"(b), "v"(c));
  return d;
}
__device__ __forceinline__ unsigned swz(unsigned row) { return ((row & 3) << 2) | ((row >> 2) & 3); }
#define BAR() do { asm volatile("s_waitcnt lgkmcnt(0)" ::: "memory"); __builtin_amdgcn_s_barrier(); asm volatile("" ::: "memory"); } while (0)
#define VMWAIT(n) do { asm volatile("" ::: "memory"); __builtin_amdgcn_s_waitcnt(0x0f70 | ((n) & 15) | (((n) >> 4) << 14)); asm volatile("" ::: "memory"); } while (0)
#define LGKM0() asm volatile("s_waitcnt lgkmcnt(0)" ::: "memory")
#define GLDS_(OP, sbase, voff, dst) asm volatile("s_mov_b32 m0, %2\n\ts_nop 0\n\t" OP :: "v"((unsigned)(voff)), "s"((const void*)(sbase)), "s"((unsigned)(dst)) : "memory", "m0")
#define GLDS(sbase, voff, dst) GLDS_("global_load_lds_dwordx4 %0, %1 nt", sbase, voff, dst)
#define GLDSX(sbase, voff, dst) GLDS_("global_load_lds_dwordx4 %0, %1", sbase, voff, dst)
#define GLDSC(c, sbase, voff, dst) do { if (c) GLDSX(sbase, voff, dst); else GLDS(sbase, voff, dst); } while (0)
#define GLDS4(sbase, voff, dst) GLDS_("global_load_lds_dword %0, %1 nt", sbase, voff, dst)
#define PFR(sbase, voff) asm volatile("global_load_dword %0, %1, %2" PFF : "+v"(tpf) : "v"((unsigned)(voff)), "s"((const void*)(sbase)) : "memory")
#define WLOAD(p) __builtin_nontemporal_load(p)
#define LBYTE(off, v) (*(volatile LAS unsigned char*)(L + L_SY + (off)) = (v))
__device__ __forceinline__ v2u tr_read(LAS unsigned char* a) {
  return __builtin_bit_cast(v2u, __builtin_amdgcn_ds_read_tr16_b64_v4i16((LAS s16x4*)a));
}
__device__ __forceinline__ v4u tr_read2(LAS unsigned char* a, LAS unsigned char* b) {
  const v2u x = tr_read(a), y = tr_read(b);
  return (v4u){x.x, x.y, y.x, y.y};
}
template <int CTRL>
__device__ __forceinline__ unsigned dppu(unsigned v) { return __builtin_amdgcn_update_dpp(v, v, CTRL, 0xf, 0xf, false); }
template <int CTRL>
__device__ __forceinline__ float dppm(float v) { return u2f(dppu<CTRL>(f2u(v))); }
__device__ __forceinline__ float xsum16(float v) {
  auto s = __builtin_amdgcn_permlane16_swap(f2u(v), f2u(v), false, false);
  return u2f(s[0]) + u2f(s[1]);
}
__device__ __forceinline__ float xmax16(float v) {
  auto s = __builtin_amdgcn_permlane16_swap(f2u(v), f2u(v), false, false);
  return fmaxf(u2f(s[0]), u2f(s[1]));
}
__device__ __forceinline__ float xsum32(float v) {
  auto s = __builtin_amdgcn_permlane32_swap(f2u(v), f2u(v), false, false);
  return u2f(s[0]) + u2f(s[1]);
}
__device__ __forceinline__ float xmax32(float v) {
  auto s = __builtin_amdgcn_permlane32_swap(f2u(v), f2u(v), false, false);
  return fmaxf(u2f(s[0]), u2f(s[1]));
}
__device__ __forceinline__ bool rec_ok(v4u r, unsigned tag) { return __all((r.y == tag) & (r.w == tag)); }
__device__ __forceinline__ bool flg_ok(v4u r, unsigned tag) { return __all((r.x == tag) & (r.y == tag) & (r.z == tag) & (r.w == tag)); }
__device__ __forceinline__ unsigned lds_wait(LAS unsigned char* a, unsigned mask) {
  unsigned w = 0;
#pragma nounroll
  for (unsigned spin = 0; spin < (SPIN_CAP << 4); ++spin) {
    w = __builtin_amdgcn_readfirstlane(*(volatile LAS unsigned*)a);
    if ((w & mask) == mask) break;
    __builtin_amdgcn_s_sleep(1);
  }
  asm volatile("" ::: "memory");
  return w;
}

__device__ __forceinline__ unsigned lds_nz(LAS unsigned char* a) {
  unsigned w = 0;
#pragma nounroll
  for (unsigned spin = 0; spin < (SPIN_CAP << 4); ++spin) {
    w = __builtin_amdgcn_readfirstlane(*(volatile LAS unsigned*)a);
    if (w) break;
    __builtin_amdgcn_s_sleep(1);
  }
  asm volatile("" ::: "memory");
  return w;
}
__device__ __forceinline__ unsigned long long lds_peek64(LAS unsigned char* a) {
  const v2u w = *(volatile LAS v2u*)a;
  return (unsigned long long)__builtin_amdgcn_readfirstlane(w.x) | ((unsigned long long)__builtin_amdgcn_readfirstlane(w.y) << 32);
}
__device__ __forceinline__ unsigned long long lds_wait64(LAS unsigned char* a, unsigned long long mask) {
  unsigned long long w = 0;
#pragma nounroll
  for (unsigned spin = 0; spin < (SPIN_CAP << 4); ++spin) {
    w = lds_peek64(a);
    if ((w & mask) == mask) break;
    __builtin_amdgcn_s_sleep(1);
  }
  asm volatile("" ::: "memory");
  return w;
}
__device__ __forceinline__ unsigned lds_ticket(LAS unsigned char* a, int lane) {
  unsigned t = 0;
  if (lane == 0) t = __hip_atomic_fetch_add((LAS unsigned*)a, 1u, __ATOMIC_RELAXED, __HIP_MEMORY_SCOPE_WORKGROUP);
  return __builtin_amdgcn_readfirstlane(t);
}
#define STAMP(k) do { } while (0)

#define GLOAD(ADDR) __builtin_amdgcn_raw_buffer_load_b128(wsr, (ADDR), 0, AUX_GL)
#define GFINISH(N, qr, ADDR, SLP)                                                                            \
  do {                                                                                                       \
    unsigned done = 0;                                                                                       \
    _Pragma("nounroll") for (unsigned spin = 0; spin < SPIN_CAP; ++spin) {                                   \
      _Pragma("unroll") for (int i = 0; i < N; ++i)                                                          \
        if (!((done >> i) & 1)) { if (rec_ok(qr[i], tag)) done |= 1u << i; }                                 \
      if (done == (1u << N) - 1u) break;                                                                     \
      if (SLP) __builtin_amdgcn_s_sleep(SLP);                                                                \
      _Pragma("unroll") for (int i = 0; i < N; ++i)                                                          \
        if (!((done >> i) & 1)) qr[i] = GLOAD(ADDR);                                                         \
    }                                                                                                        \
  } while (0)
#define OK_FLG(r) (((r).x == tag) & ((r).y == tag) & ((r).z == tag) & ((r).w == tag))
#define KEEPV(r) asm volatile("" :: "v"(r))
#define WAIT_UNTIL(t) do { _Pragma("nounroll") for (unsigned spin = 0; spin < SPIN_CAP; ++spin) { if ((unsigned)__builtin_amdgcn_s_memrealtime() - t00 >= (t)) break; __builtin_amdgcn_s_sleep(1); } } while (0)
#define GSTART(N, qr, ADDR) do { _Pragma("unroll") for (int i = 0; i < N; ++i) qr[i] = GLOAD(ADDR); } while (0)

__global__ void __launch_bounds__(NTHR, 2) fused_kernel(Params p) {
  extern __shared__ __attribute__((aligned(16))) unsigned char lds_raw[];
  STAMP(0);
  const unsigned tstart = (unsigned)__builtin_amdgcn_s_memrealtime();
  static_assert(L_DUMP == 0x26800 && L_X == 0x24000 && L_WQ == 0x20000 && L_WU == 0x18000 && L_SY == 0x260e0 && CODE_LINES <= 192 && WS_END == 0xe00000, "volley");
  unsigned epoch;
  asm volatile(
      "s_load_dwordx8 s[40:47], %[ka], 0x0\n\t"
      "v_readfirstlane_b32 s48, %[tid]\n\t"
      "v_and_b32 v13, 63, %[tid]\n\t"
      "v_mov_b32 v10, 0\n\t"
      "v_mov_b32 v11, 0\n\t"
      "v_mov_b32 v12, 0x260e0\n\t"
      "s_getpc_b64 s[50:51]\n\t"
      "s_lshr_b32 s48, s48, 6\n\t"
      "s_cmp_lg_u32 s48, 1\n\t"
      "s_cbranch_scc1 7f\n\t"
      "ds_write2_b64 v12, v[10:11], v[10:11] offset1:1\n\t"
      "ds_write2_b64 v12, v[10:11], v[10:11] offset0:2 offset1:3\n\t"
      "s_waitcnt lgkmcnt(0)\n"
      "7:\n\t"
      "s_mov_b32 s54, 0\n\t"
      "s_and_b32 s49, %[wg], 7\n\t"
      "s_cmp_lg_u32 s48, 4\n\t"
      "s_cbranch_scc1 1f\n\t"
      "s_load_dwordx2 s[62:63], %[ka], 0x50\n\t"
      "s_mov_b32 s61, 0\n\t"
      "s_bfe_u32 s52, %[wg], 0x40003\n\t"
      "s_mul_i32 s52, s52, 12\n\t"
      "v_min_u32 v10, 11, v13\n\t"
      "v_add_u32 v10, s52, v10\n\t"
      "v_min_u32 v10, %[cl], v10\n\t"
      "v_lshlrev_b32 v10, 7, v10\n\t"
      "s_andn2_b32 s50, s50, 0x7f\n\t"
      "s_mov_b32 m0, 0x26800\n\t"
      "s_nop 0\n\t"
      "global_load_lds_dword v10, s[50:51]\n\t"
      "s_branch 4f\n"
      "1:\n\t"
      "s_cmp_lg_u32 s48, 0\n\t"
      "s_cbranch_scc1 2f\n\t"
      "v_lshrrev_b32 v10, 3, v13\n\t"
      "v_and_b32 v11, 7, v13\n\t"
      "v_bfe_u32 v12, v13, 3, 2\n\t"
      "v_lshrrev_b32 v13, 5, v13\n\t"
      "v_lshl_or_b32 v12, v12, 1, v13\n\t"
      "v_xor_b32 v11, v11, v12\n\t"
      "v_lshlrev_b32 v11, 4, v11\n\t"
      "v_lshl_or_b32 v10, v10, 13, v11\n\t"
      "s_lshl_b32 s49, s49, 10\n\t"
      "s_lshr_b32 s52, %[wg], 5\n\t"
      "s_lshl_b32 s52, s52, 7\n\t"
      "s_or_b32 s49, s49, s52\n\t"
      "s_bfe_u32 s52, %[wg], 0x20003\n\t"
      "s_lshl_b32 s52, s52, 20\n\t"
      "s_or_b32 s49, s49, s52\n\t"
      "s_mov_b32 m0, 0x20000\n\t"
      "s_mov_b32 s56, 0x24000\n\t"
      "s_mov_b32 s52, 0x10000\n\t"
      "s_waitcnt lgkmcnt(0)\n\t"
      "s_add_u32 s50, s44, s49\n\t"
      "s_addc_u32 s51, s45, 0\n\t"
      "3:\n\t"
      "v_xor_b32 v11, s54, v10\n\t"
      "global_load_lds_dwordx4 v11, s[50:51] nt\n\t"
      "s_add_u32 s54, s54, s52\n\t"
      "s_add_u32 m0, m0, 0x400\n\t"
      "s_cmp_lg_u32 m0, s56\n\t"
      "s_cbranch_scc1 3b\n\t"
      "s_branch 8f\n"
      "2:\n\t"
      "s_cmp_lg_u32 s48, 5\n\t"
      "s_cbranch_scc1 6f\n\t"
      "v_lshlrev_b32 v10, 4, v13\n\t"
      "s_mov_b32 m0, 0x24000\n\t"
      "s_mov_b32 s56, 0x26000\n\t"
      "s_movk_i32 s52, 0x420\n\t"
      "s_waitcnt lgkmcnt(0)\n\t"
      "s_mov_b64 s[50:51], s[40:41]\n"
      "5:\n\t"
      "v_xor_b32 v11, s54, v10\n\t"
      "global_load_lds_dwordx4 v11, s[50:51]" VQF "\n\t"
      "s_add_u32 s54, s54, s52\n\t"
      "s_add_u32 m0, m0, 0x400\n\t"
      "s_cmp_lg_u32 m0, s56\n\t"
      "s_cbranch_scc1 5b\n\t"
      "s_barrier\n"
      "6:\n\t"
      "s_cmp_lt_u32 s48, 6\n\t"
      "s_cbranch_scc1 9f\n\t"
      "s_cmp_eq_u32 s48, 7\n\t"
      "s_cbranch_scc1 8f\n\t"
      "s_lshr_b32 s60, %[wg], 2\n\t"
      "s_lshl_b32 s61, s48, 5\n\t"
      "s_mov_b32 s62, %[t0]\n\t"
      "s_mov_b32 s63, %[t1]\n\t"
      "s_xor_b32 s60, s60, s61\n\t"
      "s_and_b32 s60, s60, 60\n\t"
      "s_lshr_b64 s[62:63], s[62:63], s60\n\t"
      "s_and_b32 s60, s62, 15\n\t"
      "s_lshl_b32 s61, s60, 3\n\t"
      "s_load_dwordx2 s[62:63], %[ka], s61\n\t"
      "s_mov_b32 s61, 0x3ffffc\n\t"
      "s_cmp_ge_u32 s60, 6\n\t"
      "s_cselect_b32 s61, 0x157ffc, s61\n\t"
      "s_cmp_eq_u32 s60, 9\n\t"
      "s_cselect_b32 s61, 0x1ffc, s61\n\t"
      "s_cmp_eq_u32 s60, 10\n\t"
      "s_cselect_b32 s61, 0xdffffc, s61\n\t"
      "s_bitcmp1_b32 %[wg], 3\n\t"
      "s_cselect_b32 s61, s61, 0\n\t"
      "s_lshl_b32 s49, s49, 22\n\t"
      "s_cmp_eq_u32 s60, 1\n\t"
      "s_cselect_b32 s49, s49, 0\n\t"
      "s_add_u32 s61, s61, s49\n"
      "4:\n\t"
      "s_waitcnt lgkmcnt(0)\n\t"
      "s_add_u32 s62, s62, s61\n\t"
      "s_addc_u32 s63, s63, 0\n\t"
      "s_load_dword %[ep], s[62:63], 0x0\n"
      "8:\n\t"
      "s_barrier\n"
      "9:\n\t"
      "s_waitcnt lgkmcnt(0)"
      : [ep] "=&s"(epoch)
      : [ka] "s"(__builtin_amdgcn_kernarg_segment_ptr()), [wg] "s"(__builtin_amdgcn_workgroup_id_x()), [tid] "v"(__builtin_amdgcn_workitem_id_x()),
        [cl] "n"(CODE_LINES - 1), [t0] "n"((unsigned)(PTAB & 0xffffffffull)), [t1] "n"((unsigned)(PTAB >> 32))
      : "memory", "m0", "scc", "s40", "s41", "s42", "s43", "s44", "s45", "s46", "s47", "s48", "s49", "s50", "s51", "s52", "s54", "s56",
        "s60", "s61", "s62", "s63", "v10", "v11", "v12", "v13");
  asm volatile("" :: "s"(p.x), "s"(p.ckv));
  STAMP(15);
  LAS unsigned char* L = (LAS unsigned char*)lds_raw;
  const int tid = threadIdx.x, lane = tid & 63;
  const int wave = __builtin_amdgcn_readfirstlane(tid >> 6);
  const int wg = blockIdx.x, xg = wg & 7, r = wg >> 3;
  const int w4 = __builtin_amdgcn_readfirstlane((tid >> 6) & 3);
  const int bA = xg, chunk = r;
  const int hA = xg * 4 + (r >> 3), sl = r & 7;
  const int hB = ((xg + 1) & 7) * 4 + (r >> 3);
  const int bO = (xg + 7) & 7, hO = (hA + 28) & 31;
  const int ntask = hB < 12 ? 6 : 5, task0 = hB < 12 ? 6 * hB : 12 + 5 * hB;
  const __amdgpu_buffer_rsrc_t wsr = __builtin_amdgcn_make_buffer_rsrc((void*)p.ws, 0, WS_END, 0x00020000);
  const int l16 = lane & 15, g4 = lane >> 4;
  const int l32 = lane & 31, hf = lane >> 5;
  const unsigned r8 = lane >> 3, q8 = lane & 7;
  const unsigned t00 = tstart;
  unsigned tag, tagv;
#define TAGGET() tag = lds_nz(L + L_SY + 24)
  const unsigned pz = q8 ^ (((r8 & 3) << 1) | (r8 >> 2));
  unsigned tpf = 0, tgs = 0;
  const unsigned fa0 = (8 * g4 + (l16 >> 2)) * 128 + ((((l16 & 3) >> 1) ^ ((l16 >> 2) << 1)) * 16) + (l16 & 1) * 8;
  const unsigned fa1 = (fa0 ^ 16) + 512;
#define FRAG(base, nt, s) tr_read2(L + (base) + (fa0 ^ ((nt) * 32)) + (s) * 4096, L + (base) + (fa1 ^ ((nt) * 32)) + (s) * 4096)
  const bool hon = 4 + g4 < ntask;
  const unsigned zl = swz(l32);
  v4u vf[16];
  unsigned va[2];
#pragma unroll
  for (int tt = 0; tt < 2; ++tt) {
    const unsigned row = 8 * g4 + 4 * tt + (l16 >> 2);
    va[tt] = L_CKV + (wave >> 1) * 32768 + row * 256 + ((((wave & 1) * 8 + ((l16 & 3) >> 1)) ^ swz(row)) * 16) + 8 * (l16 & 1);
  }
#define VLOADR(a, b) do { _Pragma("unroll") for (int i = a; i < b; ++i) vf[i] = tr_read2(L + (va[0] ^ ((i & 3) * 32)) + (i >> 2) * 8192, L + (va[1] ^ ((i & 3) * 32)) + (i >> 2) * 8192); } while (0)
#define VLOAD() VLOADR(0, 16)
  f32x16 sacc;
#define TOUCH() do { \
    PFR(p.ws, min(lane, 5) * (2u << 20) + (2u << 20)); \
    PFR(p.wuv + ((size_t)(sl * 64) * NH + hA) * HD, 0); \
    PFR(p.wo + (size_t)hB * HD * DM, 0); \
    PFR(p.wd, 0); \
    PFR(p.out, 0); \
  } while (0)
#define TAILV() do { \
    const unsigned cr = 2 * wave + (r8 >> 2) + (r8 & 3) * 16; \
    const u16* sv = p.wuv + ((size_t)(sl * 64) * NH + hA) * HD; \
    GLDSC(TVX, sv, cr * 8192 + pz * 16, T_V + wave * 1024); \
    GLDSC(TVX, sv + 64, cr * 8192 + pz * 16, T_V + 8192 + wave * 1024); \
    const u16* sw = p.wuv + ((size_t)(sl * 64) * NH + hO) * HD; \
    GLDSC(TVX, sw, cr * 8192 + pz * 16, T_W + wave * 1024); \
    GLDSC(TVX, sw + 64, cr * 8192 + pz * 16, T_W + 8192 + wave * 1024); \
  } while (0)
#define TAILO(w) do { \
    const u16* so = p.wo + ((size_t)hB * HD + (w) * 8) * DM + sl * 64; \
    GLDSC(TOX, so, r8 * 1024 + pz * 16, T_O + (w) * 1024); \
    GLDSC(TOX, so + 64 * DM, r8 * 1024 + pz * 16, T_O + 8192 + (w) * 1024); \
    GLDSC(TOX, p.wd + (size_t)task0 * 8 * DM + sl * 64, min((w) * 8 + (int)r8, ntask * 8 - 1) * 1024 + pz * 16, T_D + (w) * 1024); \
  } while (0)

  if (wave >= 4) {
    if (wave >= 6) {
      const int v = wave - 6;
      unsigned va0 = ((v + chunk) & 31) * 131072 + chunk * 4096 + g4 * 1024 + (l16 ^ ((g4 << 2) | v)) * 16;
      lds_wait(L + L_SY + 16, 1);
      STAMP(3);
      static_assert(EMASK == 0x10000, "loader");
      int pub;
      unsigned vt, s4 = 0, s5 = 0, s12 = 0;
#define GL(inc) "global_load_lds_dwordx4 %[va], %[src] " KFL "\n\tv_add_u32 %[va], " inc ", %[va]\n\ts_add_u32 m0, m0, 0x800\n\tv_bitop3_b32 %[va], %[va], s47, 32 bitop3:0x6a\n\t"
#define VW(w) "s_waitcnt vmcnt(%[" w "])\n\t"
#define CHK(to) "ds_read_b32 %[vt], %[sy] offset:16\n\ts_waitcnt lgkmcnt(0)\n\tv_readfirstlane_b32 s44, %[vt]\n\ts_bitcmp1_b32 s44, 16\n\ts_cselect_b32 s43, 1, 0\n\ts_cbranch_scc1 " to "\n\t"
#define GRP(w) VW(w) GL("s46") VW(w) GL("s45") VW(w) GL("s46") VW(w) "s_branch 10b\n"
      asm volatile(
          "s_mov_b32 m0, %[dst]\n\t"
          "s_mov_b32 s40, 0\n\t"
          "s_mov_b32 s43, 0\n\t"
          "s_mov_b32 s46, 0x40000\n\t"
          "s_mov_b32 s47, 0x3fffff\n\t"
          "s_mov_b32 %[pub], 15\n\t"
          GL("s46")
          "10:\n\t"
          "s_memrealtime s[48:49]\n\t"
          "s_getreg_b32 s44, hwreg(HW_REG_IB_STS)\n\t"
          "s_bfe_u32 s42, s44, 0x20016\n\t"
          "s_and_b32 s44, s44, 15\n\t"
          "s_lshl4_add_u32 s44, s42, s44\n\t"
          "s_sub_i32 s44, s40, s44\n\t"
          "s_cmp_ge_i32 s44, %[pub]\n\t"
          "s_cbranch_scc1 20f\n"
          "11:\n\t"
          GL("s46")
          "s_cmp_eq_u32 s40, 60\n\t"
          "s_cbranch_scc1 30f\n\t"
          "s_add_u32 s40, s40, 4\n\t"
          "s_add_u32 s41, s41, %[dt4]\n\t"
          "s_and_b32 s44, s40, 15\n\t"
          "s_cselect_b32 s45, s46, 0x40100\n\t"
          "s_cmp_eq_u32 s40, 48\n\t"
          "s_cbranch_scc1 21f\n"
          "12:\n\t"
          "s_waitcnt lgkmcnt(0)\n\t"
          "s_cmp_lt_u32 s43, 2\n\t"
          "s_cbranch_scc1 23f\n\t"
          "s_sub_i32 s44, s48, s41\n\t"
          "s_cmp_ge_i32 s44, 0\n\t"
          "s_cbranch_scc1 25f\n"
          "13:\n\t"
          VW("w")
          "14:\n\t"
          GL("s46") VW("w")
          "15:\n\t"
          GL("s45") VW("w")
          "16:\n\t"
          GL("s46")
          VW("w")
          "s_branch 10b\n"
          "20:\n\t"
          "s_or_b32 %[pub], s44, 15\n\t"
          "s_cmp_eq_u32 %[pub], s44\n\t"
          "s_cselect_b32 s44, 16, 0\n\t"
          "s_add_u32 %[pub], %[pub], s44\n\t"
          "s_sub_i32 s44, s40, %[pub]\n\t"
          "s_getpc_b64 s[50:51]\n\t"
          "s_lshl1_add_u32 s44, s44, 46\n\t"
          "s_add_u32 s50, s50, s44\n\t"
          "s_addc_u32 s51, s51, 0\n\t"
          "s_setpc_b64 s[50:51]\n\t"
          "s_waitcnt vmcnt(1)\n\ts_branch 27f\n\t"
          "s_waitcnt vmcnt(5)\n\ts_branch 27f\n\t"
          "s_waitcnt vmcnt(9)\n\ts_branch 27f\n\t"
          "s_waitcnt vmcnt(13)\n\ts_branch 27f\n\t"
          "s_waitcnt vmcnt(17)\n\ts_branch 27f\n\t"
          "s_waitcnt vmcnt(21)\n\ts_branch 27f\n\t"
          "s_waitcnt vmcnt(25)\n\ts_branch 27f\n\t"
          "s_waitcnt vmcnt(29)\n\ts_branch 27f\n\t"
          "s_waitcnt vmcnt(33)\n\ts_branch 27f\n\t"
          "s_waitcnt vmcnt(37)\n\ts_branch 27f\n\t"
          "s_waitcnt vmcnt(41)\n\ts_branch 27f\n\t"
          "s_waitcnt vmcnt(45)\n\ts_branch 27f\n\t"
          "27:\n\t"
          "s_lshr_b32 s44, %[pub], 4\n\t"
          "s_bfm_b32 s44, s44, 0\n\t"
          "v_mov_b32 %[vt], s44\n\t"
          "ds_write_b8 %[fl], %[vt]\n\t"
          "s_branch 11b\n"
          "21:\n\t"
          "s_mov_b32 s50, 0x400000\n"
          "22:\n\t"
          "ds_read_b32 %[vt], %[sy] offset:12\n\t"
          "s_waitcnt lgkmcnt(0)\n\t"
          "v_readfirstlane_b32 s44, %[vt]\n\t"
          "s_and_b32 s44, s44, 0x10100\n\t"
          "s_cmp_eq_u32 s44, 0x10100\n\t"
          "s_cbranch_scc1 12b\n\t"
          "s_sleep 1\n\t"
          "s_sub_u32 s50, s50, 1\n\t"
          "s_cmp_lg_u32 s50, 0\n\t"
          "s_cbranch_scc1 22b\n\t"
          "s_branch 12b\n"
          "23:\n\t"
          "s_cmp_eq_u32 s43, 0\n\t"
          "s_cbranch_scc1 24f\n\t"
          "ds_read_b32 %[vt], %[sy] offset:16\n\t"
          "s_mul_i32 s41, s40, %[dt]\n\t"
          "s_add_u32 s41, s41, %[t0]\n\t"
          "s_waitcnt lgkmcnt(0)\n\t"
          "v_readfirstlane_b32 s44, %[vt]\n\t"
          "s_lshr_b32 s44, s44, 24\n\t"
          "s_cselect_b32 s43, 2, 1\n\t"
          "s_lshl2_add_u32 s41, s44, s41\n\t"
          "s_branch 13b\n"
          "24:\n\t"
          VW("w0") CHK("14b") GL("s46") VW("w0") CHK("15b") GL("s45") VW("w0") CHK("16b") GL("s46") VW("w0") CHK("10b")
          "s_branch 10b\n"
          "25:\n\t"
          "s_cmp_ge_i32 s44, %[l2]\n\t"
          "s_cbranch_scc1 26f\n\t"
          GRP("w1")
          "26:\n\t"
          GRP("w2")
          "30:\n\t"
          GL("s46")
          "global_load_lds_dwordx4 %[va], %[src] " KFL "\n\t"
          "s_waitcnt lgkmcnt(0)"
          : [va] "+v"(va0), [vt] "=&v"(vt), [pub] "=&s"(pub), [s4] "+s"(s4), [s5] "+s"(s5), [s12] "+s"(s12)
          : [src] "s"(p.ckv + (size_t)bA * SK * DC), [dst] "s"(L_CKV + v * 1024), [sy] "v"(L_SY), [fl] "v"(L_SY + v),
            [t0] "s"(t00 + (SA + SL1 - 35 * SDT)), [dt] "n"(SDT), [dt4] "n"(4 * SDT), [l2] "n"(SL2 - SL1),
            [w0] "n"(WIN0 - 1), [w] "n"(WIN - 1), [w1] "n"(WIN1 - 1), [w2] "n"(WIN2 - 1)
          : "memory", "m0", "scc", "s40", "s41", "s42", "s43", "s44", "s45", "s46", "s47", "s48", "s49", "s50", "s51");
      STAMP(1);
      if (pub < 47) { VMWAIT(32); LBYTE(v, 3); }
      VMWAIT(16); LBYTE(v, 7);
      VMWAIT(0); LBYTE(v, 15);
      STAMP(2);
      lds_wait(L + L_SY, 0x0808);
      VLOAD();
    } else {
      if (wave == 4) {
        tag = epoch + 1u;
        tag += tag == 0;
        *(volatile LAS unsigned*)(L + L_SY + 24) = tag;
        VMWAIT(0);
        lds_wait(L + L_SY + 16, 0x0100);
        {
          const unsigned tq = (unsigned)__builtin_amdgcn_s_memrealtime() - t00 + TQD;
          WAIT_UNTIL(tq);
        }
        STAMP(1);
        int dm = 0;
        {
          unsigned fo = WS_QF + xg * RS + (lane >> 1) * 256 + (lane & 1) * 32;
#pragma nounroll
          for (unsigned spin = 0; spin < SPIN_CAP; ++spin) {
            const v4u f = GLOAD(fo), g = GLOAD(fo + 16);
            if ((f.x == tag) & (f.z == tag) & (g.x == tag) & (g.z == tag)) {
              dm = max(max((int)(f.y - t00), (int)(f.w - t00)), max((int)(g.y - t00), (int)(g.w - t00)));
              fo = OOB;
            }
            if (__all(fo == OOB)) break;
            __builtin_amdgcn_s_sleep(8);
            asm volatile("" ::: "memory");
          }
        }
        STAMP(2);
        if (MT == 0) TOUCH();
        if (MT == 3) PFR(p.ws, min(lane, 5) * (2u << 20) + (2u << 20));
        float df = (float)min(max(dm, 0), 700);
        df = fmaxf(df, dppm<0x121>(df)); df = fmaxf(df, dppm<0x122>(df)); df = fmaxf(df, dppm<0x124>(df)); df = fmaxf(df, dppm<0x128>(df));
        const unsigned tg = TGD + (unsigned)__builtin_amdgcn_readfirstlane((int)xmax32(xmax16(df)));
        LBYTE(19, (tg - TGD + 4) >> 2);
        tgs = tg;
        lds_wait(L + L_SY + 12, 0x00000101);
        WAIT_UNTIL(tg);
        LBYTE(21, 1);
      } else {
        VMWAIT(0);
        LBYTE(2, 1);
        STAMP(1);
        const unsigned xb = L_X + (l16 & 7) * 1024;
        const v4u ones = {0x3f803f80u, 0x3f803f80u, 0x3f803f80u, 0x3f803f80u};
        v4u xs[8];
#pragma unroll
        for (int t = 0; t < 8; ++t) xs[t] = *(const LAS v4u*)(L + xb + (((8 * t + 4 * (l16 >> 3) + g4) ^ (2 * (l16 & 7))) * 16));
        f32x4 G = {0.f, 0.f, 0.f, 0.f}, S = {0.f, 0.f, 0.f, 0.f};
#pragma unroll
        for (int t = 0; t < 8; ++t) { G = mfma16(xs[t], xs[t], G); S = mfma16(ones, xs[t], S); }
        float dg = (l16 & 2) ? ((l16 & 1) ? G[3] : G[2]) : ((l16 & 1) ? G[1] : G[0]);
        dg = g4 == (l16 >> 2) ? dg : 0.f;
        dg = xsum32(xsum16(dg));
        dg += dppm<0x128>(dg);
        const float s0 = S[0];
        const float mean = (s0 + dppm<0x128>(s0)) * (1.f / DM);
        const float rstd = __builtin_amdgcn_rsqf(fmaxf(dg * (1.f / DM) - mean * mean, 0.f) + 1e-5f);
        if (lane < 8) *(LAS v2u*)(L + L_LN + lane * 8) = (v2u){f2u(mean), f2u(rstd)};
        LGKM0();
        LBYTE(2, 3);
        STAMP(2);
        lds_wait(L + L_SY + 20, 0x0100);
      }
      {
        const unsigned qo = WS_QA + bA * RS + w4 * 16384 + lane * 16;
        const unsigned qd = L_S + w4 * 16384 + g4 * 256;
        STAMP(3);
        unsigned todo = 3;
#pragma nounroll
        for (unsigned spin = 0; spin < SPIN_CAP; ++spin) {
          v4u qa[16];
          const unsigned q0 = (todo & 1) ? qo : OOB, q1 = (todo & 2) ? qo + 8192 : OOB;
#pragma unroll
          for (int i = 0; i < 16; ++i) qa[i] = GLOAD((i < 8 ? q0 : q1) + (i & 7) * 1024);
#pragma unroll
          for (int u = 0; u < 2; ++u) {
            if ((todo >> u) & 1) {
              unsigned m = 0;
#pragma unroll
              for (int i = 8 * u; i < 8 * u + 8; ++i) {
                m = max(max(m, qa[i].x), qa[i].y);
                m = max(max(m, qa[i].z), qa[i].w);
                if (!(i == 8 && wave == 5 && lane >= 14 && lane < 16))
                  *(volatile LAS v4u*)(L + qd + u * 8192 + (i & 7) * 1024 + ((l16 ^ ((g4 << 2) | (i & 3))) * 16)) = qa[i];
              }
              if (!__any(m == ~0u)) { LBYTE(4 + 2 * w4 + u, 1); todo &= ~(1u << u); }
            }
          }
          if (!todo) break;
          __builtin_amdgcn_s_sleep(4);
          asm volatile("" ::: "memory");
        }
        STAMP(13);
      }
      lds_wait(L + L_SY, 0x0404);
      VLOAD();
      lds_wait64(L + L_SY, 0x0101010100000808ull);
#define PFTAIL() do { \
        const u16* sv = p.wuv + ((size_t)(sl * 64) * NH + hA) * HD; \
        PFR(sv, l32 * 8192 + hf * 128); \
        PFR(sv + 32 * NH * HD, l32 * 8192 + hf * 128); \
        const u16* sw = p.wuv + ((size_t)(sl * 64) * NH + hO) * HD; \
        PFR(sw, l32 * 8192 + hf * 128); \
        PFR(sw + 32 * NH * HD, l32 * 8192 + hf * 128); \
      } while (0)
      if (PFP == 0 && wave == 4) PFTAIL();
      if (PFP == 3 && wave == 4) { WAIT_UNTIL(tgs + PFD); PFTAIL(); }
    }
  } else {
    const int ft = (xg >> 2) * 128 + r * 4 + (xg & 3);
    if (wave == 0 || (wave == 1 && ft < NFFN)) {
      const unsigned st = wave ? L_WF : L_WQ;
      const int cg = (ft % 88) >> 2, mat = ft >= 88;
      const int kq1 = wave ? (wg & 3) : (sl & 3);
      if (wave) {
        const u16* src = (mat ? p.wu : p.wg) + (size_t)(kq1 * 128) * FF;
        const unsigned vo = (r8 * FF + min(cg * 64 + (int)pz * 8, FF - 8)) * 2;
        BAR();
        lds_wait(L + L_SY + 16, 0x0101);
#pragma unroll
        for (int i = 0; i < 16; ++i) GLDSC(FGX, src + i * 8 * FF, vo, st + i * 1024);
      }
      lds_wait(L + L_SY, 0x010000);
      v4u xr[4];
#pragma unroll
      for (int s = 0; s < 4; ++s) {
        xr[s] = *(const LAS v4u*)(L + L_X + (l16 & 7) * 1024 + (((16 * kq1 + 4 * s + g4) ^ (2 * (l16 & 7))) * 16));
        if (l16 >= 8) xr[s] = (v4u){0x3f803f80u, 0x3f803f80u, 0x3f803f80u, 0x3f803f80u};
      }
      f32x4 acc[4];
      {
#pragma unroll
        for (int nt = 0; nt < 4; ++nt) acc[nt] = (f32x4){0.f, 0.f, 0.f, 0.f};
#pragma unroll
        for (int s = 0; s < 4; ++s) {
          if (s == 0) VMWAIT(12); else if (s == 1) VMWAIT(8); else if (s == 2) VMWAIT(4); else VMWAIT(0);
          if (s == 3) STAMP(1);
          v4u af[4];
#pragma unroll
          for (int nt = 0; nt < 4; ++nt) af[nt] = FRAG(st, nt, s);
#pragma unroll
          for (int nt = 0; nt < 4; ++nt) acc[nt] = mfma16(af[nt], xr[s], acc[nt]);
        }
      }
      TAGGET();
      lds_wait(L + L_SY, 0x020000);
      const v2u ln = *(const LAS v2u*)(L + L_LN + (l16 & 7) * 8);
      const float mn = u2f(ln.x), rs = u2f(ln.y);
      LGKM0();
      LBYTE(12 + wave, 1);
#pragma unroll
      for (int nt = 0; nt < 4; ++nt)
#pragma unroll
        for (int j = 0; j < 4; ++j) acc[nt][j] = (acc[nt][j] - mn * dppm<0x158>(acc[nt][j])) * rs;
      const unsigned ra = wave ? WS_GU + (cg * 8 + 2 * (l16 >> 3) + (g4 >> 1)) * 2048 + mat * 1024 + kq1 * 256 + (g4 & 1) * 128 + (l16 & 7) * 16
                               : WS_QP + xg * RS + (hA & 3) * 16384 + (((sl >> 2) * 4 + (kq1 >> 1)) * 2 + (g4 & 1)) * 1024 +
                                     (2 * (l16 >> 3) + (g4 >> 1)) * 256 + ((kq1 & 1) * 8 + (l16 & 7)) * 16;
#pragma unroll
      for (int i = 0; i < 2; ++i) {
        const unsigned a = pk2(acc[2 * i][0], acc[2 * i][1]), b = pk2(acc[2 * i][2], acc[2 * i][3]);
        const unsigned c = dppu<0x128>(pk2(acc[2 * i + 1][0], acc[2 * i + 1][1])), d = dppu<0x128>(pk2(acc[2 * i + 1][2], acc[2 * i + 1][3]));
        __builtin_amdgcn_raw_buffer_store_b128((v4u){l16 < 8 ? a : c, tag, l16 < 8 ? b : d, tag}, wsr, ra + i * (wave ? 8192 : 4096), 0, AUX_ST);
      }
      asm volatile("" ::: "memory");
      LBYTE(17, 1);
      STAMP(2);
    } else if (wave == 1) {
      BAR();
      LBYTE(13, 1);
    } else if (wave == 2) {
      {
        const u16* src = p.wuk + ((size_t)(sl * 64) * NH + hA) * HD;
        const unsigned vo = g4 * 8192 + (l16 ^ g4) * 16;
        BAR();
#pragma unroll
        for (int n = 0; n < 16; ++n) GLDSC(UKX, src + n * 4 * NH * HD, vo ^ ((n & 3) * 64), L_WU + n * 1024);
        LBYTE(16, 1);
      }
      VMWAIT(0);
      STAMP(1);
      v4u wf[16];
#pragma unroll
      for (int n = 0; n < 16; ++n) wf[n] = *(const LAS v4u*)(L + L_WU + ((n >> 2) * 16 + l16) * 256 + (((4 * (n & 3) + g4) ^ l16) * 16));
      LGKM0();
      LBYTE(14, 1);
      TAGGET();
      lds_wait(L + L_SY + 16, 0x0100);
      {
        const unsigned tp = max((unsigned)__builtin_amdgcn_s_memrealtime() - t00 + TPD, (unsigned)TP);
        WAIT_UNTIL(tp);
      }
      v4u qr[16];
      {
        bool todo = true;
#pragma nounroll
        for (unsigned spin = 0; spin < SPIN_CAP; ++spin) {
          if (todo) {
            GSTART(16, qr, WS_QP + xg * RS + (hA & 3) * 16384 + i * 1024 + lane * 16);
            unsigned bad = 0;
#pragma unroll
            for (int i = 0; i < 16; ++i) { bad |= (qr[i].y ^ tag) | (qr[i].w ^ tag); asm volatile("" : "+v"(bad)); }
            todo = bad != 0;
          }
          if (!__any(todo)) break;
          __builtin_amdgcn_s_sleep(4);
          asm volatile("" ::: "memory");
        }
      }
      STAMP(2);
      const float scale = 0.08838834764831845f * 1.4426950408889634f;
      unsigned yp[2][2];
#pragma unroll
      for (int cp = 0; cp < 2; ++cp) {
        f32x4 a0 = {0.f, 0.f, 0.f, 0.f}, a1 = {0.f, 0.f, 0.f, 0.f};
#pragma unroll
        for (int k = 0; k < 8; ++k) {
          const v4u bq = {qr[2 * k].x, qr[2 * k].z, qr[2 * k + 1].x, qr[2 * k + 1].z};
          a0 = mfma16(wf[cp * 8 + (k >> 1)], bq, a0);
          a1 = mfma16(wf[cp * 8 + 4 + (k >> 1)], bq, a1);
        }
        float y[4];
#pragma unroll
        for (int j = 0; j < 4; ++j) {
          const float keep = l16 < 8 ? a0[j] : a1[j], give = l16 < 8 ? a1[j] : a0[j];
          y[j] = (keep + dppm<0x128>(give)) * scale;
        }
        yp[cp][0] = pk2(y[0], y[1]); yp[cp][1] = pk2(y[2], y[3]);
      }
      lds_wait(L + L_SY + 20, 0x010000);
      {
        auto s0 = __builtin_amdgcn_permlane16_swap(yp[0][0], yp[1][0], false, false);
        auto s1 = __builtin_amdgcn_permlane16_swap(yp[0][1], yp[1][1], false, false);
        __builtin_amdgcn_raw_buffer_store_b128((v4u){min(s0[0], ~1u), min(s1[0], ~1u), min(s0[1], ~1u), min(s1[1], ~1u)}, wsr,
                                               WS_QA + (l16 & 7) * RS + ((sl >> 1) * 32 + hA) * 256 + (sl & 1) * 128 +
                                                   (2 * (g4 & 1) + (l16 >> 3)) * 32 + (g4 >> 1) * 16, 0, AUX_ST);
      }
      asm volatile("" ::: "memory");
      LBYTE(18, 1);
      STAMP(12);
    } else {
      BAR();
      __builtin_amdgcn_raw_buffer_store_b128((v4u){~0u, ~0u, ~0u, ~0u}, wsr, WS_QA + r8 * RS + ((sl >> 1) * 32 + hA) * 256 + (sl & 1) * 128 + q8 * 16, 0, AUX_CL);
#pragma unroll
      for (int i = 0; i < 2; ++i)
        __builtin_amdgcn_raw_buffer_store_b128((v4u){~0u, ~0u, ~0u, ~0u}, wsr, WS_CX + ((hA * 8 + 4 * i + g4) * 8 + sl) * 256 + l16 * 16, 0, AUX_CL);
      VMWAIT(0);
      LBYTE(22, 1);
      TAGGET();
      lds_wait(L + L_SY + 16, 0x0100);
      if (lane < 8) __builtin_amdgcn_raw_buffer_store_b64((v2u){tag, (unsigned)__builtin_amdgcn_s_memrealtime()}, wsr, WS_QF + lane * RS + hA * 256 + sl * 8, 0, AUX_ST);
      STAMP(12);
      if (MT == 1) TOUCH();
    }
    {
#pragma unroll
      for (int i = 0; i < 16; ++i) sacc[i] = 0.f;
      const unsigned tz = (hf ^ zl) * 16;
      const unsigned kb = L_CKV + (wave * 32 + l32) * 256 + tz;
      const unsigned ho = WS_QA + bA * RS + 3 * 8192 + 224 + hf * 16;
      v4u qa[8], qc[8], k0[8], k1[8];
      unsigned ka[8], qx[8];
#pragma unroll
      for (int s = 0; s < 8; ++s) ka[s] = kb ^ (s * 32);
      v2u fl;
#define DSR(dst, addr, off) asm volatile("ds_read_b128 %0, %1 offset:%2" : "=v"(dst) : "v"(addr), "n"(off) : "memory")
#define DSRU(dst, addr, off) asm volatile("ds_read_b128 %0, %1 offset:%2" : "+v"(dst) : "v"(addr), "n"(off) : "memory")
#define KREAD(k, b) do { _Pragma("unroll") for (int s = 0; s < 8; ++s) DSR(k[s], ka[s], (b) * 32768); } while (0)
#define BREAD(q, k, b) do { \
        _Pragma("unroll") for (int s = 0; s < 8; ++s) { \
          if ((b) > 1) DSRU(k[s], ka[s], ((b) & 1) * 32768); \
          if ((b) == 3 && s == 7) asm volatile("s_mov_b64 exec, %2\n\tds_read_b128 %0, %1 offset:24576\n\ts_mov_b64 exec, -1" : "+v"(q[7]) : "v"(qx[7]), "s"(0xfffffffefffffffeull) : "memory"); \
          else DSRU(q[s], qx[s], (b) * 8192); \
        } \
      } while (0)
#define BMASK(b) ((0x100000000ull << (8 * (b))) | ((b) > 1 ? 0x0101ull << (b) : 0ull))
#define QK4(q, k, b) do { \
        asm volatile("ds_read_b64 %0, %1 offset:%2" : "=v"(fl) : "v"(L_SY), "n"((b) == 3 ? 8 : 0) : "memory"); \
        _Pragma("unroll") for (int s = 0; s < 4; ++s) { \
          asm volatile("s_waitcnt lgkmcnt(%3)" : "+v"(q[s]), "+v"(k[s]), "+v"(sacc) : "n"((b) > 1 ? 15 - 2 * s : 8 - s) : "memory"); \
          sacc = mfma32(k[s], q[s], sacc); \
        } \
        asm volatile("s_waitcnt lgkmcnt(0)" : "+v"(fl), "+v"(sacc), "+v"(q[4]), "+v"(q[5]), "+v"(q[6]), "+v"(q[7]), "+v"(k[4]), "+v"(k[5]), "+v"(k[6]), "+v"(k[7]) :: "memory"); \
      } while (0)
#define QK8(q, k) do { _Pragma("unroll") for (int s = 4; s < 8; ++s) sacc = mfma32(k[s], q[s], sacc); asm volatile("" : "+v"(sacc) :: "memory"); } while (0)
#define QKB(q, k, b, qn, kn) do { \
        QK4(q, k, b); \
        if ((b) == 1) LBYTE(8 + wave, 1); \
        const bool nx = __builtin_amdgcn_readfirstlane((fl.x & (unsigned)BMASK((b) + 1)) | (fl.y & (unsigned)(BMASK((b) + 1) >> 32))) == ((unsigned)BMASK((b) + 1) | (unsigned)(BMASK((b) + 1) >> 32)); \
        if (nx) BREAD(qn, kn, (b) + 1); \
        QK8(q, k); \
        if (!nx) { lds_wait64(L + L_SY, BMASK((b) + 1)); BREAD(qn, kn, (b) + 1); } \
      } while (0)
      lds_wait(L + L_SY, 0x0101);
      KREAD(k0, 0);
      lds_wait(L + L_SY, 0x0202);
      KREAD(k1, 1);
      VLOAD();
#pragma unroll
      for (int i = 0; i < 16; ++i) asm volatile("" : "+v"(vf[i]));
#pragma unroll
      for (int s = 0; s < 8; ++s) {
        qx[s] = ka[s] + (L_S - wave * 8192);
        ka[s] += 65536;
        asm volatile("" : "+v"(qx[s]), "+v"(ka[s]), "=v"(qa[s]), "=v"(qc[s]));
      }
      lds_wait(L + L_SY + 20, 0x0100);
      v4u hole = GLOAD(ho);
      lds_wait(L + L_SY + 4, 1);
      STAMP(4);
      BREAD(qa, k0, 0);
      QKB(qa, k0, 0, qc, k1);
      QKB(qc, k1, 1, qa, k0);
#pragma nounroll
      for (unsigned spin = 0; spin < SPIN_CAP; ++spin) {
        if (__all(max(max(hole.x, hole.y), max(hole.z, hole.w)) != ~0u)) break;
        hole = GLOAD(ho);
      }
      qc[7] = hole;
      asm volatile("" : "+v"(qc[7]));
      QKB(qa, k0, 2, qc, k1);
      QK4(qc, k1, 3);
      QK8(qc, k1);
      if (__builtin_amdgcn_readfirstlane(fl.x) != 0x01010101u) lds_wait(L + L_SY + 8, 0x01010101);
      STAMP(5);
      float mx = __builtin_ceilf(sacc[0]);
#pragma unroll
      for (int i = 1; i < 15; i += 2) mx = max3(mx, sacc[i], sacc[i + 1]);
      mx = max3(mx, sacc[15], sacc[15]);
      *(LAS float*)(L + L_ST + l32 * 32 + wave * 8 + hf * 4) = __builtin_ceilf(mx);
    }
  }
  BAR();
  if (PFP == 1 && wave == 4) PFTAIL();
  float M = 0.f;
  if (wave < 4) {
    const f32x4 m0 = *(const LAS f32x4*)(L + L_ST + l32 * 32), m1 = *(const LAS f32x4*)(L + L_ST + l32 * 32 + 16);
    M = max3(max3(m0[0], m0[1], m0[2]), max3(m0[3], m1[0], m1[1]), max3(m1[2], m1[3], m1[3]));
    unsigned pp[8];
#pragma unroll
    for (int q = 0; q < 4; ++q) {
      pp[2 * q] = pk2(__builtin_amdgcn_exp2f(sacc[4 * q] - M), __builtin_amdgcn_exp2f(sacc[4 * q + 1] - M));
      pp[2 * q + 1] = pk2(__builtin_amdgcn_exp2f(sacc[4 * q + 2] - M), __builtin_amdgcn_exp2f(sacc[4 * q + 3] - M));
      *(LAS v2u*)(L + L_P + l32 * 256 + (((wave * 4 + q) ^ l16) * 16) + hf * 8) = (v2u){pp[2 * q], pp[2 * q + 1]};
    }
    float ls = 0.f;
#pragma unroll
    for (int q = 0; q < 8; ++q) ls = dot_ones(pp[q], ls);
    *(LAS float*)(L + L_ST + 1024 + l32 * 32 + wave * 8 + hf * 4) = ls;
    STAMP(3);
  }
  BAR();
  if (wave < 4 || wave > 5) STAMP(13);
  {
    v4u pf[8];
    {
      const unsigned pbs = L_P + l16 * 256 + (g4 ^ l16) * 16;
#pragma unroll
      for (int i = 0; i < 8; ++i) pf[i] = *(const LAS v4u*)(L + (pbs ^ ((i & 3) * 64)) + (i >> 2) * 4096);
    }
    tagv = *(const LAS unsigned*)(L + L_SY + 24);
    f32x4 o[2][4];
#pragma unroll
    for (int i = 0; i < 8; ++i) o[i >> 2][i & 3] = (f32x4){0.f, 0.f, 0.f, 0.f};
    const unsigned pbase = WS_PART + ((((bA * 32 + 4 * g4) * 8 + wave) * 32 + (chunk ^ (wave * 4 + g4))) * 16 + l16) * 8;
#define PSTORE(hh, j) __builtin_amdgcn_raw_buffer_store_b64((v2u){pk2(o[hh][0][j], o[hh][1][j]), pk2(o[hh][2][j], o[hh][3][j])}, wsr, pbase + (16 * (hh) + (j)) * (8 * 32 * 128), 0, AUX_PS)
#pragma unroll
    for (int i = 0; i < 16; ++i) o[0][i & 3] = mfma16(pf[i >> 2], vf[i], o[0][i & 3]);
#pragma unroll
    for (int s = 0; s < 4; ++s) {
      __builtin_amdgcn_sched_barrier(0);
#pragma unroll
      for (int t = 0; t < 4; ++t) o[1][t] = mfma16(pf[4 + s], vf[4 * s + t], o[1][t]);
      __builtin_amdgcn_sched_barrier(0);
      PSTORE(0, s);
    }
    __builtin_amdgcn_sched_barrier(0);
    STAMP(6);
#pragma unroll
    for (int j = 0; j < 4; ++j) PSTORE(1, j);
    if (wave == 3) {
      const f32x4 l0 = *(const LAS f32x4*)(L + L_ST + 1024 + l32 * 32), l1 = *(const LAS f32x4*)(L + L_ST + 1024 + l32 * 32 + 16);
      const float lt = ((l0[0] + l0[1]) + (l0[2] + l0[3])) + ((l1[0] + l1[1]) + (l1[2] + l1[3]));
      if (lane < 32)
        __builtin_amdgcn_raw_buffer_store_b64((v2u){f2u(M), f2u(lt)}, wsr, WS_FL + ((((lane >> 2) * 8 + bA) * 32 + chunk) * 4 + (lane & 3)) * 8, 0, AUX_ST);
    }
  }
  tag = __builtin_amdgcn_readfirstlane(tagv);
  STAMP(7);
  v4u wv[4];
  v2u xw;
#define ALOAD(dst, addr) asm volatile("buffer_load_dwordx4 %0, %1, %2, 0 offen sc1" ALF : "=v"(dst) : "v"(addr), "s"(wsr) : "memory")
#define ALOADU(dst, addr) asm volatile("buffer_load_dwordx4 %0, %1, %2, 0 offen sc1" ALF : "+v"(dst) : "v"(addr), "s"(wsr) : "memory")
  {
    const int bC = wave == xg ? bO : wave, hC = wave == xg ? hO : hA;
    v4u gr[2];
    v4u fp[PPN];
    const unsigned gq = wave < ntask && NFFN ? WS_GU + (task0 + wave) * 2048 + lane * 16 : OOB;
    unsigned fa = lane < 8 && lane != xg ? WS_PF + lane * RS + (bA * (32 / FPL) + (unsigned)chunk / FPL) * 256 + ((unsigned)chunk % FPL) * 4 : OOB;
    unsigned fo = lane < 8 ? WS_PF + xg * RS + (bC * (32 / FPL) + lane / (FPL / 4)) * 256 + (lane % (FPL / 4)) * 16 : OOB;
    unsigned po = WS_PART + ((((bC * 32 + hC) * 8 + sl) * 32) * 16 + lane) * 8;
    unsigned lo = WS_FL + ((((hC >> 2) * 8 + bC) * 32 + l32) * 4 + (hA & 3)) * 8;
    asm volatile("" : "+v"(fa), "+v"(fo), "+v"(po), "+v"(lo));
    VMWAIT(0);
    if ((lds_ticket(L + L_SY + 28, lane) & 255) == 7) __builtin_amdgcn_raw_buffer_store_b32(tag, wsr, fa, 0, AUX_ST);
    STAMP(8);
    KEEPV(tpf);
    TAILV();
    ALOAD(gr[0], gq);
    ALOAD(gr[1], gq + 1024);
    {
      v4u f;
      ALOAD(f, fo);
      VMWAIT(1);
      asm volatile("" : "+v"(gr[0]), "+v"(gr[1]));
      if (lane == 0) __hip_atomic_fetch_add((LAS unsigned*)(L + L_SY + 28), 0x100u, __ATOMIC_RELAXED, __HIP_MEMORY_SCOPE_WORKGROUP);
      if (wave < ntask && NFFN) {
#pragma nounroll
        for (unsigned spin = 0; spin < SPIN_CAP; ++spin) {
          if (__all((gr[0].y == tag) & (gr[0].w == tag) & (gr[1].y == tag) & (gr[1].w == tag))) break;
          ALOAD(gr[0], gq);
          ALOAD(gr[1], gq + 1024);
          VMWAIT(0);
          asm volatile("" : "+v"(gr[0]), "+v"(gr[1]));
        }
        const unsigned e = (l16 >> 2) + 4 * (l16 & 1);
        const unsigned v = (l16 & 2) ? 0u : ((e & 1) ? 0x3f800000u : 0x00003f80u);
        f32x4 a = {0.f, 0.f, 0.f, 0.f};
        a = mfma16((v4u){(e >> 1) == 0 ? v : 0u, (e >> 1) == 1 ? v : 0u, (e >> 1) == 2 ? v : 0u, (e >> 1) == 3 ? v : 0u}, (v4u){gr[0].x, gr[0].z, gr[1].x, gr[1].z}, a);
        const float gv = a[0], uv = a[1];
        const float yy = 0.7978845608028654f * (gv + 0.044715f * gv * gv * gv);
        const float th = 1.f - 2.f / (__expf(2.f * yy) + 1.f);
        *(LAS u16*)(L + L_HM + wave * 128 + (l16 & 7) * 16 + ((l16 >> 3) * 4 + g4) * 2) = (u16)pk2(0.5f * gv * (1.f + th) * uv, 0.f);
      }
#pragma nounroll
      for (unsigned spin = 0; spin < SPIN_CAP; ++spin) {
        VMWAIT(0);
        asm volatile("" : "+v"(f));
        if (OK_FLG(f)) fo = OOB;
        if (__all(fo == OOB)) break;
        __builtin_amdgcn_s_sleep(PSLP);
        ALOAD(f, fo);
      }
      asm volatile("" ::: "memory");
    }
    KEEPV(tpf);
    STAMP(14);
    v2u pr[9];
    pr[8] = __builtin_amdgcn_raw_buffer_load_b64(wsr, lo, 0, AUX_PL);
#pragma unroll
    for (int i = 0; i < 8; ++i) pr[i] = __builtin_amdgcn_raw_buffer_load_b64(wsr, po + i * 512, 0, AUX_PL);
    xw = *(const GAS v2u*)(p.x + wg * 16 + (lane & 3) * 4);
    if (lane == 0) __hip_atomic_fetch_add((LAS unsigned*)(L + L_SY + 28), 0x1000000u, __ATOMIC_RELAXED, __HIP_MEMORY_SCOPE_WORKGROUP);
    lds_wait(L + L_SY + 28, 0x800);
#pragma unroll
    for (int i = 0; i < 4; ++i) { wv[i] = FRAG((i & 2 ? T_W : T_V) + (wave >> 2) * 8192, wave & 3, i & 1); asm volatile("" : "+v"(wv[i])); }
    const float mj = u2f(pr[8].x);
    float M = mj;
    M = fmaxf(M, dppm<0x121>(M)); M = fmaxf(M, dppm<0x122>(M)); M = fmaxf(M, dppm<0x124>(M)); M = fmaxf(M, dppm<0x128>(M));
    M = xmax16(M);
    const float wj = bfhi(f2u(__builtin_amdgcn_exp2f(mj - M)));
    float Ls = wj * u2f(pr[8].y);
    Ls += dppm<0x121>(Ls); Ls += dppm<0x122>(Ls); Ls += dppm<0x124>(Ls); Ls += dppm<0x128>(Ls);
    Ls = xsum16(Ls);
    const float inv = __builtin_amdgcn_rcpf(Ls);
    const unsigned e4 = l16 & 3;
    const unsigned sh = (e4 & 1) ? 0u : 16u, k0 = e4 < 2 ? ~0u : 0u;
    unsigned wa[16];
#pragma unroll
    for (int u = 0; u < 8; ++u) {
      const unsigned w = (unsigned)__builtin_amdgcn_ds_bpermute(((g4 + 4 * u) ^ (sl * 4 + ((hC >> 2) & 3))) * 4, f2u(wj)) >> sh;
      wa[2 * u] = w & k0; wa[2 * u + 1] = w & ~k0;
    }
    f32x4 acc = {0.f, 0.f, 0.f, 0.f};
#pragma unroll
    for (int u = 0; u < 4; ++u)
      acc = mfma16((v4u){wa[4 * u], wa[4 * u + 1], wa[4 * u + 2], wa[4 * u + 3]}, (v4u){pr[2 * u].x, pr[2 * u].y, pr[2 * u + 1].x, pr[2 * u + 1].y}, acc);
    if (lane < 16) *(LAS v2u*)(L + L_CL + wave * 144 + lane * 8) = (v2u){pk2(acc[0] * inv, acc[1] * inv), pk2(acc[2] * inv, acc[3] * inv)};
    if (wave < 4) { lds_wait(L + L_SY + 28, 0x8000000); TAILO(wave); TAILO(wave + 4); }
    BAR();
  }
  v4u ao[4];
  v4u cp[CPN + 1][4];
  f32x4 accE = {0.f, 0.f, 0.f, 0.f};
  const unsigned one = (l16 & 1) ? 0x3f800000u : 0x00003f80u;
  const unsigned s0 = (l16 & 2) ? 0u : one, s1 = (l16 & 2) ? one : 0u;
  {
    const v4u b0 = *(const LAS v4u*)(L + L_CL + (l16 & 7) * 144 + g4 * 16), b1 = *(const LAS v4u*)(L + L_CL + (l16 & 7) * 144 + 64 + g4 * 16);
    f32x4 acc = {0.f, 0.f, 0.f, 0.f};
    acc = mfma16(wv[0], b0, acc);
    acc = mfma16(wv[1], b1, acc);
    f32x4 acw = {0.f, 0.f, 0.f, 0.f};
    acw = mfma16(wv[2], b0, acw);
    acw = mfma16(wv[3], b1, acw);
    if (l16 == xg) acc = acw;
    if (l16 < 8)
      __builtin_amdgcn_raw_buffer_store_b64((v2u){min(pk2(acc[0], acc[1]), ~1u), min(pk2(acc[2], acc[3]), ~1u)}, wsr,
                                            WS_CX + (((((l16 == xg ? hO : hA) * 8 + wave) * 8 + sl) * 8 + (l16 == xg ? bO : l16)) * 4 + g4) * 8, 0, AUX_ST);
    STAMP(9);
#define OFRAG() do { \
      _Pragma("unroll") for (int ks = 0; ks < 4; ++ks) ao[ks] = FRAG(T_O, wave, ks); \
      const v4u hm0 = *(const LAS v4u*)(L + L_HM + g4 * 128 + (l16 & 7) * 16), hm1 = *(const LAS v4u*)(L + L_HM + 512 + g4 * 128 + (l16 & 7) * 16); \
      accE = mfma16(FRAG(T_D, wave, 0), hm0, accE); \
      accE = mfma16(FRAG(T_D, wave, 1), hon ? hm1 : (v4u){0u, 0u, 0u, 0u}, accE); \
    } while (0)
    if (wave < 4) {
      VMWAIT(0);
      if (lane == 0) __hip_atomic_fetch_add((LAS unsigned*)(L + L_SY + 28), 0x10000u, __ATOMIC_RELAXED, __HIP_MEMORY_SCOPE_WORKGROUP);
      lds_wait(L + L_SY + 28, 0x40000);
      OFRAG();
      STAMP(10);
    } else {
      v4u cr[4];
      const unsigned co = WS_CX + (hB * 8 + 2 * w4) * 2048 + lane * 16;
      if (CDELAY) __builtin_amdgcn_s_sleep(CDELAY);
#pragma nounroll
      for (unsigned spin = 0; spin < SPIN_CAP; ++spin) {
        unsigned m = 0;
#pragma unroll
        for (int i = 0; i < 4; ++i) cr[i] = GLOAD(co + i * 1024);
#pragma unroll
        for (int i = 0; i < 4; ++i) m = max(max(max(m, cr[i].x), cr[i].y), max(cr[i].z, cr[i].w));
        if (!__any(m == ~0u)) break;
        __builtin_amdgcn_s_sleep(1);
      }
      asm volatile("" : "+v"(xw));
      STAMP(10);
      const v4u se = {(l16 >> 1) == 0 ? one : 0u, (l16 >> 1) == 1 ? one : 0u, (l16 >> 1) == 2 ? one : 0u, (l16 >> 1) == 3 ? one : 0u};
#pragma unroll
      for (int u = 0; u < 2; ++u) {
        f32x4 a = {0.f, 0.f, 0.f, 0.f};
        a = mfma16(se, cr[2 * u], a);
        a = mfma16(se, cr[2 * u + 1], a);
        if (lane < 32) *(LAS v2u*)(L + L_CX + (l16 >> 1) * 272 + (2 * w4 + u) * 32 + (l16 & 1) * 16 + g4 * 8) = (v2u){pk2(a[0], a[1]), pk2(a[2], a[3])};
      }
    }
    BAR();
  }
  if (wave < 4) {
#pragma unroll
    for (int ks = 0; ks < 4; ++ks) accE = mfma16(ao[ks], *(const LAS v4u*)(L + L_CX + (l16 & 7) * 272 + ks * 64 + g4 * 16), accE);
    if (l16 < 8)
      __builtin_amdgcn_raw_buffer_store_b128((v4u){pk2(accE[0], accE[1]), tag, pk2(accE[2], accE[3]), tag}, wsr,
                                             WS_OG + ((((sl * 4 + wave) * 32 + hB) * 8 + l16) * 4 + g4) * 16, 0, AUX_ST);
    STAMP(11);
  } else if (wave == 4) {
    const unsigned oo = WS_OG + ((((wg & 31) * 32 + (lane >> 2)) * 8 + (wg >> 5)) * 4 + (lane & 3)) * 16;
    v4u r0, r1;
    if (ODELAY) __builtin_amdgcn_s_sleep(ODELAY);
    v4u pa[OPN], pb[OPN];
    unsigned oa = oo, ob = oo + 16 * 512;
    r0 = GLOAD(oa);
    r1 = GLOAD(ob);
    if ((r0.y == tag) & (r0.w == tag)) oa = OOB;
    if ((r1.y == tag) & (r1.w == tag)) ob = OOB;
#pragma unroll
    for (int i = 0; i < OPN; ++i) asm volatile("" : "=v"(pa[i]), "=v"(pb[i]));
    if (!__all((oa & ob) == OOB)) {
#pragma unroll
      for (int i = 0; i < OPN; ++i) { ALOADU(pa[i], oa); ALOADU(pb[i], ob); if (i + 1 < OPN) __builtin_amdgcn_s_sleep(OPS); }
#pragma nounroll
      for (unsigned spin = 0; spin < SPIN_CAP; ++spin) {
        bool ok = false;
#pragma unroll
        for (int i = 0; i < OPN; ++i) {
          VMWAIT(2 * OPN - 2);
          asm volatile("" : "+v"(pa[i]), "+v"(pb[i]));
          if ((pa[i].y == tag) & (pa[i].w == tag)) { oa = OOB; r0.x = pa[i].x; r0.z = pa[i].z; }
          if ((pb[i].y == tag) & (pb[i].w == tag)) { ob = OOB; r1.x = pb[i].x; r1.z = pb[i].z; }
          asm volatile("" : "+v"(r0.x), "+v"(r0.z), "+v"(r1.x), "+v"(r1.z), "+v"(oa), "+v"(ob));
          ok = __all((oa & ob) == OOB);
          if (ok) break;
          ALOADU(pa[i], oa); ALOADU(pb[i], ob);
        }
        if (ok) break;
      }
    }
    f32x4 acc = {0.f, 0.f, 0.f, 0.f};
    acc = mfma16((v4u){s0, s1, s0, s1}, (v4u){r0.x, r0.z, r1.x, r1.z}, acc);
#pragma unroll
    for (int j = 0; j < 4; ++j) { acc[j] += dppm<0x124>(acc[j]); acc[j] += dppm<0x128>(acc[j]); }
    if (lane < 4)
      *(GAS v2u*)(p.out + wg * 16 + lane * 4) = (v2u){pk2(bflo(xw.x) + acc[0], bfhi(xw.x) + acc[1]), pk2(bflo(xw.y) + acc[2], bfhi(xw.y) + acc[3])};
    if (wg == 0 && lane == 0) __builtin_amdgcn_raw_buffer_store_b32(tag, wsr, WS_EPOCH, 0, AUX_SC1);
    STAMP(11);
    VMWAIT(0);
#pragma unroll
    for (int i = 0; i < OPN; ++i) asm volatile("" :: "v"(pa[i]), "v"(pb[i]));
  }
}

extern "C" void kernel_launch(void* const* d_in, const int* in_sizes, int n_in, void* d_out, int out_size, void* d_ws, size_t ws_size,
                              hipStream_t stream) {
  Params p;
  p.x = (const u16*)d_in[0]; p.ckv = (const u16*)d_in[1]; p.wq = (const u16*)d_in[2]; p.wuk = (const u16*)d_in[3];
  p.wuv = (const u16*)d_in[4]; p.wo = (const u16*)d_in[5]; p.wg = (const u16*)d_in[7]; p.wu = (const u16*)d_in[8]; p.wd = (const u16*)d_in[9];
  p.out = (u16*)d_out; p.ws = (unsigned char*)d_ws;
  if (WSO == 1 && ws_size >= WS_END) p.ws += (ws_size - WS_END) & ~(size_t)((2u << 20) - 1);
  if (WSO == 2 && ws_size >= 2 * (size_t)WS_END) p.ws += (ws_size / 2) & ~(size_t)((2u << 20) - 1);
  (void)hipFuncSetAttribute((const void*)fused_kernel, hipFuncAttributeMaxDynamicSharedMemorySize, LDS_BYTES);
  fused_kernel<<<NWG, NTHR, LDS_BYTES, stream>>>(p);
}
```
